```python
import torch

def _softmax(x, axis=-1):
    return torch.softmax(x, dim=axis)
def _gelu(x):
    return torch.nn.functional.gelu(x, approximate="tanh")
def _silu(x):
    return torch.nn.functional.silu(x)
def _sigmoid(x):
    return torch.sigmoid(x)
def _layernorm(x, axis=-1, eps=1e-5):
    m = x.mean(dim=axis, keepdim=True)
    v = x.var(dim=axis, keepdim=True, unbiased=False)
    return (x - m) / torch.sqrt(v + eps)
def _scatter_add(tgt, idx, src, axis=0):
    return tgt.clone().index_add_(axis, idx, src)
def _segment_sum(src, seg, n):
    out = torch.zeros((n,) + src.shape[1:], dtype=src.dtype)
    return out.index_add_(0, seg, src)
def _pad(x, pw):
    flat = tuple(v for pair in reversed(pw) for v in pair)
    return torch.nn.functional.pad(x, flat)

kernel_name = 'sequential_preLN__mha_block__mixer_token_mlp__prompt'

def setup_inputs():

    _w = (lambda a: (lambda t: t.to(torch.bfloat16) if t.is_floating_point() else t)(torch.as_tensor(a)))
    def _A_setup():
        B, S, D, H = 1, 2048, 448, 16
        return {
            "x": _w((lambda *s: torch.randn(*s, dtype=torch.bfloat16) * 0.05)(B, S, D)),
            "wqkv": _w((lambda *s: torch.randn(*s, dtype=torch.bfloat16) * 0.05)(D, 3 * D)),
            "wo": _w((lambda *s: torch.randn(*s, dtype=torch.bfloat16) * 0.05)(D, D)),
        }
    _A_in = _A_setup()

    def _B_setup():
        B, S, D, F = 1, 2048, 448, 1792
        return {
            "x": _w((lambda *s: torch.randn(*s, dtype=torch.bfloat16) * 0.05)(B, S, D)),
            "w1": _w((lambda *s: torch.randn(*s, dtype=torch.bfloat16) * 0.8)(S, F) * (S ** -0.5)),
            "b1": _w((lambda *s: torch.randn(*s, dtype=torch.bfloat16) * 12.8)(F) * 0.02),
            "w2": _w((lambda *s: torch.randn(*s, dtype=torch.bfloat16) * 0.2)(F, S) * (F ** -0.5)),
            "b2": _w((lambda *s: torch.randn(*s, dtype=torch.bfloat16) * 1.6)(S) * 0.02),
        }
    _B_in = _B_setup()

    return {
        "A_x": _A_in["x"],
        "A_wqkv": _A_in["wqkv"],
        "A_wo": _A_in["wo"],
        "B_x": _B_in["x"],
        "B_w1": _B_in["w1"],
        "B_b1": _B_in["b1"],
        "B_w2": _B_in["w2"],
        "B_b2": _B_in["b2"],
    }

def reference(A_x, A_wqkv, A_wo, B_x, B_w1, B_b1, B_w2, B_b2):
    def _A_ref(x, wqkv, wo):
        B, S, D = x.shape
        H = 16
        Dh = D // H
        qkv = (lambda x, *d: x.permute(*d))((x @ wqkv).reshape(B, S, 3, H, Dh), 2, 0, 3, 1, 4)
        q, k, v = qkv[0], qkv[1], qkv[2]
        scores = torch.einsum("bhqd,bhkd->bhqk", q, k) * (Dh ** -0.5)
        p = _softmax(scores, axis=-1)
        o = torch.einsum("bhqk,bhkd->bhqd", p, v)
        return (lambda x, *d: x.permute(*d))(o, 0, 2, 1, 3).reshape(B, S, D) @ wo

    def _B_ref(x, w1, b1, w2, b2):
        B, S, D = x.shape
        h = _layernorm(x, axis=-1)
        u = torch.einsum("bsd,sf->bdf", h, w1) + b1
        u = _gelu(u)
        y = torch.einsum("bdf,fs->bds", u, w2) + b2
        return (lambda x, *d: x.permute(*d))(y, 0, 2, 1)

    x = A_x
    h = x + _A_ref(_layernorm(x, axis=-1), A_wqkv, A_wo)
    h2 = h + _B_ref(_layernorm(h, axis=-1), B_w1, B_b1, B_w2, B_b2)
    return h2
```

```cpp
#include <hip/hip_runtime.h>
#include <hip/hip_bf16.h>

constexpr int LDS_A = 163840, LDS_B = 163840;

extern "C" __global__ __launch_bounds__(320, 2) void k_a(const char* x, const char* wqkv, const char* wo, const char* w1, const char* w2, char* ws) {
  asm volatile(
  "s_load_dwordx8 s[4:11], %[ka], 0\n"
  "s_load_dwordx4 s[12:15], %[ka], 32\n"
  "v_lshrrev_b32 v2, 6, %[tid]\n"
  "v_and_b32 v1, 63, %[tid]\n"
  "s_and_b32 s49, %[bid], 7\n"
  "s_lshr_b32 s50, %[bid], 3\n"
  "v_readfirstlane_b32 s48, v2\n"
  "s_and_b32 s51, s50, 3\n"
  "s_lshr_b32 s56, s50, 2\n"
  "s_bitcmp0_b32 s49, 0\n"
  "s_cbranch_scc1 .La_even\n"
  "s_cmp_ge_u32 s50, 24\n"
  "s_cbranch_scc1 .La_idle\n"
  "s_mul_i32 s56, s50, 43\n"
  "s_lshr_b32 s56, s56, 7\n"
  "s_mul_i32 s53, s56, 3\n"
  "s_sub_u32 s51, s50, s53\n"
  "s_add_u32 s51, s51, 4\n"
  ".La_even:\n"
  "s_lshr_b32 s53, s49, 1\n"
  "s_lshl_b32 s53, s53, 3\n"
  "s_add_u32 s52, s53, s56\n"
  "s_cmp_lg_u32 s48, 4\n"
  "s_cbranch_scc1 .La_comp\n"
  "v_lshrrev_b32 v13, 3, v1\n"
  "v_mul_u32_u24 v13, 2688, v13\n"
  "v_lshrrev_b32 v14, 2, v1\n"
  "v_and_b32 v14, 4, v14\n"
  "v_and_b32 v15, 7, v1\n"
  "v_xor_b32 v14, v14, v15\n"
  "v_lshl_add_u32 v3, v14, 4, v13\n"
  "v_add_u32 v3, 4096, v3\n"
  "v_add_u32 v4, 20480, v3\n"
  "v_add_u32 v5, -1152, v3\n"
  "v_add_u32 v6, -1152, v4\n"
  "v_add_u32 v7, 1792, v3\n"
  "v_add_u32 v8, 1792, v4\n"
  "v_lshrrev_b32 v13, 3, v1\n"
  "v_mul_u32_u24 v13, 896, v13\n"
  "v_lshrrev_b32 v14, 4, v1\n"
  "v_xor_b32 v14, v14, v15\n"
  "v_lshl_add_u32 v13, v14, 4, v13\n"
  "v_add_u32 v9, 2048, v13\n"
  "v_xor_b32 v13, 64, v13\n"
  "v_add_u32 v10, 8192, v13\n"
  "s_mul_i32 s62, s52, 57344\n"
  "s_lshl_b32 s63, s51, 7\n"
  "s_add_u32 s53, s56, s51\n"
  "s_mul_i32 s54, s53, 128\n"
  "s_lshr_b32 s54, s54, 8\n"
  "s_mul_i32 s54, s54, 2\n"
  "s_sub_u32 s53, s53, s54\n"
  "s_mov_b32 s66, 0x6543210\n"
  "s_cmp_eq_u32 s53, 1\n"
  "s_cselect_b32 s66, 0x5634120, s66\n"
  "s_waitcnt lgkmcnt(0)\n"
  "s_mov_b64 s[40:41], s[4:5]\n"
  "s_sub_u32 s40, s40, 4096\n"
  "s_subb_u32 s41, s41, 0\n"
  "s_mov_b32 s42, 1839104\n"
  "s_mov_b32 s43, 0x20000\n"
  "s_mov_b64 s[44:45], s[6:7]\n"
  "s_sub_u32 s44, s44, 4096\n"
  "s_subb_u32 s45, s45, 0\n"
  "s_mov_b32 s46, 1208320\n"
  "s_mov_b32 s47, 0x20000\n"
  "s_mov_b32 s60, 0\n"
  "s_mov_b32 s61, 0\n"
  ".La_chunk:\n"
  "s_and_b32 s57, s66, 7\n"
  "s_lshr_b32 s66, s66, 4\n"
  "s_lshl_b32 s58, s57, 7\n"
  "s_add_u32 s58, s58, s62\n"
  "s_mul_i32 s59, s57, 172032\n"
  "s_add_u32 s59, s59, s63\n"
  ".La_rows:\n"
  "s_add_u32 m0, s60, 4096\n"
  "s_add_u32 s55, s60, 8192\n"
  "buffer_load_dwordx4 v9, s[40:43], s58 offen offset:2048 lds\n"
  "buffer_load_dwordx4 v10, s[40:43], s58 offen offset:3072 lds\n"
  "buffer_load_dwordx4 v7, s[44:47], s59 offen offset:0 lds\n"
  "buffer_load_dwordx4 v8, s[44:47], s59 offen offset:1024 lds\n"
  "s_mov_b32 m0, s60\n"
  "s_mov_b32 s60, s55\n"
  "buffer_load_dwordx4 v3, s[44:47], s59 offen offset:0 lds\n"
  "buffer_load_dwordx4 v4, s[44:47], s59 offen offset:1024 lds\n"
  "buffer_load_dwordx4 v5, s[44:47], s59 offen offset:2048 lds\n"
  "buffer_load_dwordx4 v6, s[44:47], s59 offen offset:3072 lds\n"
  "s_add_u32 s58, s58, 14336\n"
  "s_add_u32 s59, s59, 43008\n"
  "s_and_b32 s55, s60, 0x6000\n"
  "s_cbranch_scc1 .La_rows\n"
  "s_cmp_eq_u32 s60, 163840\n"
  "s_cselect_b32 s60, 0, s60\n"
  "s_add_u32 s61, s61, 1\n"
  "s_cmp_eq_u32 s61, 1\n"
  "s_cbranch_scc1 .La_chunk\n"
  "s_waitcnt vmcnt(32)\n"
  "s_barrier\n"
  "s_cmp_lg_u32 s61, 7\n"
  "s_cbranch_scc1 .La_chunk\n"
  "s_waitcnt vmcnt(0)\n"
  "s_barrier\n"
  "s_getpc_b64 s[76:77]\n"
  "s_add_u32 s76, s76, k_b@rel32@lo+4\n"
  "s_addc_u32 s77, s77, k_b@rel32@hi+12\n"
  "s_mov_b32 s78, .Lb_codeend-k_b\n"
  "s_mov_b32 s79, 0x20000\n"
  "v_lshlrev_b32 v12, 7, v1\n"
  "s_lshl_b32 s53, s50, 11\n"
  "s_mov_b64 exec, 0xffff\n"
  "buffer_load_dword v11, v12, s[76:79], s53 offen\n"
  "s_endpgm\n"
  ".La_comp:\n"
  "s_cmp_lg_u32 s48, 0\n"
  "s_cbranch_scc1 .La_go\n"
  "s_getpc_b64 s[76:77]\n"
  "s_add_u32 s76, s76, k_a@rel32@lo+4\n"
  "s_addc_u32 s77, s77, k_a@rel32@hi+12\n"
  "s_mov_b32 s78, .La_end-k_a\n"
  "s_mov_b32 s79, 0x20000\n"
  "v_lshlrev_b32 v12, 7, v1\n"
  "s_mul_i32 s53, s50, 384\n"
  "s_mov_b64 exec, 7\n"
  "buffer_load_dword v244, v12, s[76:79], s53 offen\n"
  "s_mov_b64 exec, -1\n"
  ".La_go:\n"
  "v_lshlrev_b32 v2, 4, v1\n"
  "s_and_b32 s64, s48, 1\n"
  "s_lshr_b32 s65, s48, 1\n"
  "v_and_b32 v16, 31, v1\n"
  "v_lshrrev_b32 v17, 5, v1\n"
  "v_bfe_u32 v18, v16, 1, 3\n"
  "v_xor_b32 v18, v18, v17\n"
  "v_lshlrev_b32 v18, 4, v18\n"
  "v_and_b32 v19, 15, v1\n"
  "v_lshl_add_u32 v18, v19, 7, v18\n"
  "v_bfe_u32 v19, v1, 4, 1\n"
  "v_lshl_add_u32 v18, v19, 13, v18\n"
  "s_lshl_b32 s55, s64, 14\n"
  "s_add_u32 s55, s55, 6144\n"
  "v_lshrrev_b32 v13, 5, v1\n"
  "v_bfe_u32 v14, v1, 2, 2\n"
  "v_lshl_add_u32 v13, v13, 3, v14\n"
  "v_lshlrev_b32 v13, 7, v13\n"
  "v_bfe_u32 v14, v1, 3, 1\n"
  "v_xor_b32 v14, s65, v14\n"
  "v_lshl_add_u32 v13, v14, 6, v13\n"
  "v_bfe_u32 v14, v1, 4, 1\n"
  "v_lshl_add_u32 v13, v14, 5, v13\n"
  "v_and_b32 v14, 1, v1\n"
  "v_lshl_add_u32 v13, v14, 4, v13\n"
  "v_bfe_u32 v14, v1, 1, 1\n"
  "v_lshl_add_u32 v13, v14, 3, v13\n"
  "s_xor_b32 s53, s48, 0\n"
  "s_lshl_b32 s54, s53, 5\n"
  "v_xor_b32 v19, s54, v18\n"
  "v_add_u32 v144, s55, v19\n"
  "s_lshl_b32 s54, s53, 13\n"
  "v_add_u32 v148, s54, v13\n"
  "v_add_u32 v152, 65536, v144\n"
  "v_add_u32 v156, 65536, v148\n"
  "v_add_u32 v160, 131072, v144\n"
  "v_add_u32 v164, 131072, v148\n"
  "s_xor_b32 s53, s48, 1\n"
  "s_lshl_b32 s54, s53, 5\n"
  "v_xor_b32 v19, s54, v18\n"
  "v_add_u32 v145, s55, v19\n"
  "s_lshl_b32 s54, s53, 13\n"
  "v_add_u32 v149, s54, v13\n"
  "v_add_u32 v153, 65536, v145\n"
  "v_add_u32 v157, 65536, v149\n"
  "v_add_u32 v161, 131072, v145\n"
  "v_add_u32 v165, 131072, v149\n"
  "s_xor_b32 s53, s48, 2\n"
  "s_lshl_b32 s54, s53, 5\n"
  "v_xor_b32 v19, s54, v18\n"
  "v_add_u32 v146, s55, v19\n"
  "s_lshl_b32 s54, s53, 13\n"
  "v_add_u32 v150, s54, v13\n"
  "v_add_u32 v154, 65536, v146\n"
  "v_add_u32 v158, 65536, v150\n"
  "v_add_u32 v162, 131072, v146\n"
  "v_add_u32 v166, 131072, v150\n"
  "s_xor_b32 s53, s48, 3\n"
  "s_lshl_b32 s54, s53, 5\n"
  "v_xor_b32 v19, s54, v18\n"
  "v_add_u32 v147, s55, v19\n"
  "s_lshl_b32 s54, s53, 13\n"
  "v_add_u32 v151, s54, v13\n"
  "v_add_u32 v155, 65536, v147\n"
  "v_add_u32 v159, 65536, v151\n"
  "v_add_u32 v163, 131072, v147\n"
  "v_add_u32 v167, 131072, v151\n"
  "v_lshrrev_b32 v19, 2, v1\n"
  "v_xor_b32 v19, v19, v1\n"
  "v_and_b32 v19, 4, v19\n"
  "v_cmp_eq_u32 vcc, 0, v19\n"
  "v_mov_b32 v20, 0x3f803f80\n"
  "v_and_b32 v19, 1, v1\n"
  "v_cmp_ne_u32 s[70:71], 0, v19\n"
  "v_and_b32 v19, 2, v1\n"
  "v_cmp_ne_u32 s[72:73], 0, v19\n"
  "v_cndmask_b32 v188, 0, v20, vcc\n"
  "v_cndmask_b32 v189, 0, v20, vcc\n"
  "v_cndmask_b32 v190, 0, v20, vcc\n"
  "v_cndmask_b32 v191, 0, v20, vcc\n"
  "v_mov_b32 v194, 0\n"
  "v_mov_b32 v195, 0\n"
  "v_mov_b32 v198, 0\n"
  "v_mov_b32 v199, 0\n"
  "v_mov_b32 v202, 0\n"
  "v_mov_b32 v203, 0\n"
  "v_mov_b32 v206, 0\n"
  "v_mov_b32 v207, 0\n"
  "v_lshlrev_b32 v19, 3, v1\n"
  "s_lshl_b32 s53, s48, 9\n"
  "s_add_u32 s53, s53, 65536\n"
  "v_add_u32 v208, s53, v19\n"
  "v_xor_b32 v209, 1024, v208\n"
  "v_lshlrev_b32 v19, 2, v1\n"
  "s_lshl_b32 s53, s48, 10\n"
  "s_add_u32 s53, s53, 69632\n"
  "v_add_u32 v210, s53, v19\n"
  "v_xor_b32 v211, 1024, v210\n"
  "s_lshl_b32 s53, s48, 7\n"
  "s_add_u32 s53, s53, 67584\n"
  "v_lshlrev_b32 v19, 2, v16\n"
  "v_add_u32 v212, s53, v19\n"
  "v_lshlrev_b32 v19, 4, v17\n"
  "v_add_u32 v213, s53, v19\n"
  "v_cvt_f32_u32 v19, v17\n"
  "v_sub_f32 v214, 1.0, v19\n"
  "v_mov_b32 v224, 0\n"
  "v_mov_b32 v225, 0\n"
  "v_mov_b32 v220, 0\n"
  "s_lshl_b32 s53, s51, 3\n"
  "s_lshl_b32 s54, s65, 2\n"
  "s_add_u32 s53, s53, s54\n"
  "v_lshlrev_b32 v21, 4, v16\n"
  "v_add_u32 v19, s53, v17\n"
  "v_mul_u32_u24 v20, 37, v19\n"
  "v_lshrrev_b32 v20, 8, v20\n"
  "v_mul_u32_u24 v22, 7, v20\n"
  "v_sub_u32 v19, v19, v22\n"
  "v_cmp_lt_u32 vcc, 3, v19\n"
  "v_cmp_eq_u32 s[66:67], 3, v19\n"
  "v_lshlrev_b32 v20, 1, v20\n"
  "v_cndmask_b32 v22, 0, 1, vcc\n"
  "v_add_u32 v23, 1, v20\n"
  "v_add_u32 v20, v20, v22\n"
  "v_mul_u32_u24 v22, 3, v22\n"
  "v_sub_u32 v19, v19, v22\n"
  "v_lshl_add_u32 v19, v19, 9, v21\n"
  "v_lshl_add_u32 v216, v20, 17, v19\n"
  "v_lshl_add_u32 v218, v20, 18, v19\n"
  "v_cndmask_b32 v220, v220, v23, s[66:67]\n"
  "v_add_u32 v19, s53, v17\n"
  "v_add_u32 v19, 2, v19\n"
  "v_mul_u32_u24 v20, 37, v19\n"
  "v_lshrrev_b32 v20, 8, v20\n"
  "v_mul_u32_u24 v22, 7, v20\n"
  "v_sub_u32 v19, v19, v22\n"
  "v_cmp_lt_u32 vcc, 3, v19\n"
  "v_cmp_eq_u32 s[68:69], 3, v19\n"
  "v_lshlrev_b32 v20, 1, v20\n"
  "v_cndmask_b32 v22, 0, 1, vcc\n"
  "v_add_u32 v23, 1, v20\n"
  "v_add_u32 v20, v20, v22\n"
  "v_mul_u32_u24 v22, 3, v22\n"
  "v_sub_u32 v19, v19, v22\n"
  "v_lshl_add_u32 v19, v19, 9, v21\n"
  "v_lshl_add_u32 v217, v20, 17, v19\n"
  "v_lshl_add_u32 v219, v20, 18, v19\n"
  "v_cndmask_b32 v220, v220, v23, s[68:69]\n"
  "v_lshl_add_u32 v221, v220, 18, v21\n"
  "v_lshl_add_u32 v220, v220, 17, v21\n"
  "v_and_b32 v19, 0x13, v16\n"
  "v_bfe_u32 v20, v16, 2, 1\n"
  "v_lshl_add_u32 v19, v20, 3, v19\n"
  "v_bfe_u32 v20, v16, 3, 1\n"
  "v_lshl_add_u32 v19, v20, 2, v19\n"
  "s_lshl_b32 s53, s53, 3\n"
  "v_add_u32 v19, s53, v19\n"
  "v_mul_u32_u24 v20, 2341, v19\n"
  "v_lshrrev_b32 v20, 16, v20\n"
  "v_mul_u32_u24 v22, 28, v20\n"
  "v_sub_u32 v19, v19, v22\n"
  "v_and_b32 v22, 0x13, v19\n"
  "v_bfe_u32 v23, v19, 2, 1\n"
  "v_lshl_add_u32 v22, v23, 3, v22\n"
  "v_bfe_u32 v23, v19, 3, 1\n"
  "v_lshl_add_u32 v22, v23, 2, v22\n"
  "v_lshlrev_b32 v22, 4, v22\n"
  "v_lshl_add_u32 v22, v17, 9, v22\n"
  "v_lshl_add_u32 v222, v20, 18, v22\n"
  "s_lshl_b32 s54, s52, 1\n"
  "s_add_u32 s54, s54, s64\n"
  "s_lshl_b32 s74, s54, 11\n"
  "s_lshl_b32 s75, s54, 12\n"
  "s_add_u32 s75, s75, 2097152\n"
  "s_mul_i32 s54, s53, 2341\n"
  "s_lshr_b32 s54, s54, 16\n"
  "v_bfe_u32 v19, v1, 4, 2\n"
  "v_add_u32 v20, s54, v19\n"
  "v_mul_u32_u24 v22, 28, v20\n"
  "s_add_u32 s55, s53, 5\n"
  "v_cmp_gt_u32 vcc, s55, v22\n"
  "v_and_b32 v19, 3, v1\n"
  "v_lshlrev_b32 v22, 4, v19\n"
  "v_bfe_u32 v23, v1, 2, 2\n"
  "v_lshl_add_u32 v22, v23, 9, v22\n"
  "v_lshl_add_u32 v22, v20, 18, v22\n"
  "v_add_u32 v22, 2496, v22\n"
  "v_mov_b32 v23, 2130706432\n"
  "v_cndmask_b32 v22, v23, v22, vcc\n"
  "v_cmp_eq_u32 vcc, 0, v19\n"
  "v_mov_b32 v23, 0x3f803f80\n"
  "s_nop 0\n"
  "v_cndmask_b32 v24, 0, v23, vcc\n"
  "v_cndmask_b32 v25, 0, v23, vcc\n"
  "v_cndmask_b32 v26, 0, v23, vcc\n"
  "v_cndmask_b32 v27, 0, v23, vcc\n"
  "v_mov_b32 v28, v22\n"
  "s_waitcnt lgkmcnt(0)\n"
  "s_mov_b64 s[36:37], s[14:15]\n"
  "s_mov_b32 s38, 16777216\n"
  "s_mov_b32 s39, 0x20000\n"
  "s_cmp_lg_u32 s48, 1\n"
  "s_cbranch_scc1 .La_t1\n"
  "s_cmp_ge_u32 s50, 24\n"
  "s_cbranch_scc1 .La_t1\n"
  "s_mul_i32 s53, s50, 3\n"
  "v_add_u32 v12, s53, v1\n"
  "s_mov_b64 exec, 7\n"
  "s_mov_b64 s[76:77], s[10:11]\n"
  "s_mov_b32 s78, 7340032\n"
  "s_mov_b32 s79, 0x20000\n"
  "v_mul_u32_u24 v11, 101888, v12\n"
  "s_mov_b32 s53, 0\n"
  "buffer_load_dword v244, v11, s[76:79], s53 offen\n"
  "s_mov_b64 s[76:77], s[12:13]\n"
  "s_mov_b32 s78, 7340032\n"
  "s_mov_b32 s79, 0x20000\n"
  "v_mul_u32_u24 v11, 101888, v12\n"
  "s_mov_b32 s53, 0\n"
  "buffer_load_dword v244, v11, s[76:79], s53 offen\n"
  "s_mov_b64 s[76:77], s[14:15]\n"
  "s_mov_b32 s78, 16777216\n"
  "s_mov_b32 s79, 0x20000\n"
  "v_mul_u32_u24 v11, 116480, v12\n"
  "s_mov_b32 s53, 6291456\n"
  "buffer_load_dword v244, v11, s[76:79], s53 offen\n"
  "s_mov_b64 s[76:77], s[8:9]\n"
  "s_mov_b32 s78, 401408\n"
  "s_mov_b32 s79, 0x20000\n"
  "v_mul_u32_u24 v11, 5504, v12\n"
  "s_mov_b32 s53, 0\n"
  "buffer_load_dword v244, v11, s[76:79], s53 offen\n"
  "s_mov_b64 exec, -1\n"
  ".La_t1:\n"
  "s_barrier\n"
  "ds_read_b128 v[112:115], v144 offset:0\n"
  "ds_read_b64_tr_b16 v[116:117], v148 offset:0\n"
  "ds_read_b64_tr_b16 v[118:119], v148 offset:512\n"
  "ds_read_b64_tr_b16 v[120:121], v148 offset:2048\n"
  "ds_read_b64_tr_b16 v[122:123], v148 offset:2560\n"
  "ds_read_b64_tr_b16 v[124:125], v148 offset:4096\n"
  "ds_read_b64_tr_b16 v[126:127], v148 offset:4608\n"
  "ds_read_b128 v[128:131], v145 offset:0\n"
  "ds_read_b64_tr_b16 v[132:133], v149 offset:0\n"
  "ds_read_b64_tr_b16 v[134:135], v149 offset:512\n"
  "ds_read_b64_tr_b16 v[136:137], v149 offset:2048\n"
  "ds_read_b64_tr_b16 v[138:139], v149 offset:2560\n"
  "ds_read_b64_tr_b16 v[140:141], v149 offset:4096\n"
  "ds_read_b64_tr_b16 v[142:143], v149 offset:4608\n"
  "s_waitcnt lgkmcnt(7)\n"
  "v_mfma_f32_32x32x16_bf16 v[64:79], v[116:119], v[112:115], 0\n"
  "ds_read_b128 v[228:231], v146 offset:0\n"
  "ds_read_b64_tr_b16 v[232:233], v150 offset:0\n"
  "ds_read_b64_tr_b16 v[234:235], v150 offset:512\n"
  "v_mfma_f32_4x4x4_16b_bf16 v[172:175], v[112:113], v[112:113], 0\n"
  "v_mfma_f32_32x32x16_bf16 v[80:95], v[120:123], v[112:115], 0\n"
  "ds_read_b64_tr_b16 v[236:237], v150 offset:2048\n"
  "ds_read_b64_tr_b16 v[238:239], v150 offset:2560\n"
  "v_mfma_f32_16x16x32_bf16 v[168:171], v[188:191], v[112:115], 0\n"
  "v_mfma_f32_4x4x4_16b_bf16 v[172:175], v[114:115], v[114:115], v[172:175]\n"
  "v_mfma_f32_16x16x32_bf16 v[176:179], v[188:191], v[116:119], 0\n"
  "v_mfma_f32_16x16x32_bf16 v[180:183], v[188:191], v[120:123], 0\n"
  "v_mfma_f32_16x16x32_bf16 v[184:187], v[188:191], v[124:127], 0\n"
  "v_mfma_f32_32x32x16_bf16 v[96:111], v[112:115], v[124:127], 0\n"
  "ds_read_b64_tr_b16 v[240:241], v150 offset:4096\n"
  "ds_read_b64_tr_b16 v[242:243], v150 offset:4608\n"
  "s_waitcnt lgkmcnt(7)\n"
  "v_mfma_f32_32x32x16_bf16 v[64:79], v[132:135], v[128:131], v[64:79]\n"
  "ds_read_b128 v[112:115], v147 offset:0\n"
  "ds_read_b64_tr_b16 v[116:117], v151 offset:0\n"
  "ds_read_b64_tr_b16 v[118:119], v151 offset:512\n"
  "v_mfma_f32_4x4x4_16b_bf16 v[172:175], v[128:129], v[128:129], v[172:175]\n"
  "v_mfma_f32_32x32x16_bf16 v[80:95], v[136:139], v[128:131], v[80:95]\n"
  "ds_read_b64_tr_b16 v[120:121], v151 offset:2048\n"
  "ds_read_b64_tr_b16 v[122:123], v151 offset:2560\n"
  "v_mfma_f32_16x16x32_bf16 v[168:171], v[188:191], v[128:131], v[168:171]\n"
  "v_mfma_f32_4x4x4_16b_bf16 v[172:175], v[130:131], v[130:131], v[172:175]\n"
  "v_mfma_f32_32x32x16_bf16 v[96:111], v[128:131], v[140:143], v[96:111]\n"
  "ds_read_b64_tr_b16 v[124:125], v151 offset:4096\n"
  "ds_read_b64_tr_b16 v[126:127], v151 offset:4608\n"
  "s_waitcnt lgkmcnt(7)\n"
  "v_mfma_f32_32x32x16_bf16 v[64:79], v[232:235], v[228:231], v[64:79]\n"
  "s_barrier\n"
  "ds_read_b128 v[128:131], v144 offset:32768\n"
  "ds_read_b64_tr_b16 v[132:133], v148 offset:32768\n"
  "ds_read_b64_tr_b16 v[134:135], v148 offset:33280\n"
  "v_mfma_f32_32x32x16_bf16 v[80:95], v[236:239], v[228:231], v[80:95]\n"
  "ds_read_b64_tr_b16 v[136:137], v148 offset:34816\n"
  "ds_read_b64_tr_b16 v[138:139], v148 offset:35328\n"
  "v_mfma_f32_16x16x32_bf16 v[176:179], v[188:191], v[232:235], v[176:179]\n"
  "v_mfma_f32_16x16x32_bf16 v[180:183], v[188:191], v[236:239], v[180:183]\n"
  "v_mfma_f32_16x16x32_bf16 v[184:187], v[188:191], v[240:243], v[184:187]\n"
  "v_mfma_f32_32x32x16_bf16 v[96:111], v[228:231], v[240:243], v[96:111]\n"
  "ds_read_b64_tr_b16 v[140:141], v148 offset:36864\n"
  "ds_read_b64_tr_b16 v[142:143], v148 offset:37376\n"
  "s_waitcnt lgkmcnt(7)\n"
  "v_mfma_f32_32x32x16_bf16 v[64:79], v[116:119], v[112:115], v[64:79]\n"
  "ds_read_b128 v[228:231], v145 offset:32768\n"
  "ds_read_b64_tr_b16 v[232:233], v149 offset:32768\n"
  "ds_read_b64_tr_b16 v[234:235], v149 offset:33280\n"
  "v_mfma_f32_32x32x16_bf16 v[80:95], v[120:123], v[112:115], v[80:95]\n"
  "ds_read_b64_tr_b16 v[236:237], v149 offset:34816\n"
  "ds_read_b64_tr_b16 v[238:239], v149 offset:35328\n"
  "v_mfma_f32_32x32x16_bf16 v[96:111], v[112:115], v[124:127], v[96:111]\n"
  "ds_read_b64_tr_b16 v[240:241], v149 offset:36864\n"
  "ds_read_b64_tr_b16 v[242:243], v149 offset:37376\n"
  "s_waitcnt lgkmcnt(7)\n"
  "v_mfma_f32_32x32x16_bf16 v[64:79], v[132:135], v[128:131], v[64:79]\n"
  "ds_read_b128 v[112:115], v146 offset:32768\n"
  "ds_read_b64_tr_b16 v[116:117], v150 offset:32768\n"
  "ds_read_b64_tr_b16 v[118:119], v150 offset:33280\n"
  "v_mfma_f32_4x4x4_16b_bf16 v[172:175], v[128:129], v[128:129], v[172:175]\n"
  "v_mfma_f32_32x32x16_bf16 v[80:95], v[136:139], v[128:131], v[80:95]\n"
  "ds_read_b64_tr_b16 v[120:121], v150 offset:34816\n"
  "ds_read_b64_tr_b16 v[122:123], v150 offset:35328\n"
  "v_mfma_f32_16x16x32_bf16 v[168:171], v[188:191], v[128:131], v[168:171]\n"
  "v_mfma_f32_4x4x4_16b_bf16 v[172:175], v[130:131], v[130:131], v[172:175]\n"
  "v_mfma_f32_16x16x32_bf16 v[176:179], v[188:191], v[132:135], v[176:179]\n"
  "v_mfma_f32_16x16x32_bf16 v[180:183], v[188:191], v[136:139], v[180:183]\n"
  "v_mfma_f32_16x16x32_bf16 v[184:187], v[188:191], v[140:143], v[184:187]\n"
  "v_mfma_f32_32x32x16_bf16 v[96:111], v[128:131], v[140:143], v[96:111]\n"
  "ds_read_b64_tr_b16 v[124:125], v150 offset:36864\n"
  "ds_read_b64_tr_b16 v[126:127], v150 offset:37376\n"
  "s_waitcnt lgkmcnt(7)\n"
  "v_mfma_f32_32x32x16_bf16 v[64:79], v[232:235], v[228:231], v[64:79]\n"
  "ds_read_b128 v[128:131], v147 offset:32768\n"
  "ds_read_b64_tr_b16 v[132:133], v151 offset:32768\n"
  "ds_read_b64_tr_b16 v[134:135], v151 offset:33280\n"
  "v_mfma_f32_4x4x4_16b_bf16 v[172:175], v[228:229], v[228:229], v[172:175]\n"
  "v_mfma_f32_32x32x16_bf16 v[80:95], v[236:239], v[228:231], v[80:95]\n"
  "ds_read_b64_tr_b16 v[136:137], v151 offset:34816\n"
  "ds_read_b64_tr_b16 v[138:139], v151 offset:35328\n"
  "v_mfma_f32_16x16x32_bf16 v[168:171], v[188:191], v[228:231], v[168:171]\n"
  "v_mfma_f32_4x4x4_16b_bf16 v[172:175], v[230:231], v[230:231], v[172:175]\n"
  "v_mfma_f32_32x32x16_bf16 v[96:111], v[228:231], v[240:243], v[96:111]\n"
  "ds_read_b64_tr_b16 v[140:141], v151 offset:36864\n"
  "ds_read_b64_tr_b16 v[142:143], v151 offset:37376\n"
  "s_waitcnt lgkmcnt(7)\n"
  "v_mfma_f32_32x32x16_bf16 v[64:79], v[116:119], v[112:115], v[64:79]\n"
  "s_barrier\n"
  "ds_read_b128 v[228:231], v152 offset:0\n"
  "ds_read_b64_tr_b16 v[232:233], v156 offset:0\n"
  "ds_read_b64_tr_b16 v[234:235], v156 offset:512\n"
  "v_mfma_f32_32x32x16_bf16 v[80:95], v[120:123], v[112:115], v[80:95]\n"
  "ds_read_b64_tr_b16 v[236:237], v156 offset:2048\n"
  "ds_read_b64_tr_b16 v[238:239], v156 offset:2560\n"
  "v_mfma_f32_16x16x32_bf16 v[176:179], v[188:191], v[116:119], v[176:179]\n"
  "v_mfma_f32_16x16x32_bf16 v[180:183], v[188:191], v[120:123], v[180:183]\n"
  "v_mfma_f32_16x16x32_bf16 v[184:187], v[188:191], v[124:127], v[184:187]\n"
  "v_mfma_f32_32x32x16_bf16 v[96:111], v[112:115], v[124:127], v[96:111]\n"
  "ds_read_b64_tr_b16 v[240:241], v156 offset:4096\n"
  "ds_read_b64_tr_b16 v[242:243], v156 offset:4608\n"
  "s_waitcnt lgkmcnt(7)\n"
  "v_mfma_f32_32x32x16_bf16 v[64:79], v[132:135], v[128:131], v[64:79]\n"
  "ds_read_b128 v[112:115], v153 offset:0\n"
  "ds_read_b64_tr_b16 v[116:117], v157 offset:0\n"
  "ds_read_b64_tr_b16 v[118:119], v157 offset:512\n"
  "v_mfma_f32_32x32x16_bf16 v[80:95], v[136:139], v[128:131], v[80:95]\n"
  "ds_read_b64_tr_b16 v[120:121], v157 offset:2048\n"
  "ds_read_b64_tr_b16 v[122:123], v157 offset:2560\n"
  "v_mfma_f32_32x32x16_bf16 v[96:111], v[128:131], v[140:143], v[96:111]\n"
  "ds_read_b64_tr_b16 v[124:125], v157 offset:4096\n"
  "ds_read_b64_tr_b16 v[126:127], v157 offset:4608\n"
  "s_waitcnt lgkmcnt(7)\n"
  "v_mfma_f32_32x32x16_bf16 v[64:79], v[232:235], v[228:231], v[64:79]\n"
  "ds_read_b128 v[128:131], v154 offset:0\n"
  "ds_read_b64_tr_b16 v[132:133], v158 offset:0\n"
  "ds_read_b64_tr_b16 v[134:135], v158 offset:512\n"
  "v_mfma_f32_4x4x4_16b_bf16 v[172:175], v[228:229], v[228:229], v[172:175]\n"
  "v_mfma_f32_32x32x16_bf16 v[80:95], v[236:239], v[228:231], v[80:95]\n"
  "ds_read_b64_tr_b16 v[136:137], v158 offset:2048\n"
  "ds_read_b64_tr_b16 v[138:139], v158 offset:2560\n"
  "v_mfma_f32_16x16x32_bf16 v[168:171], v[188:191], v[228:231], v[168:171]\n"
  "v_mfma_f32_4x4x4_16b_bf16 v[172:175], v[230:231], v[230:231], v[172:175]\n"
  "v_mfma_f32_16x16x32_bf16 v[176:179], v[188:191], v[232:235], v[176:179]\n"
  "v_mfma_f32_16x16x32_bf16 v[180:183], v[188:191], v[236:239], v[180:183]\n"
  "v_mfma_f32_16x16x32_bf16 v[184:187], v[188:191], v[240:243], v[184:187]\n"
  "v_mfma_f32_32x32x16_bf16 v[96:111], v[228:231], v[240:243], v[96:111]\n"
  "ds_read_b64_tr_b16 v[140:141], v158 offset:4096\n"
  "ds_read_b64_tr_b16 v[142:143], v158 offset:4608\n"
  "s_waitcnt lgkmcnt(7)\n"
  "v_mfma_f32_32x32x16_bf16 v[64:79], v[116:119], v[112:115], v[64:79]\n"
  "ds_read_b128 v[228:231], v155 offset:0\n"
  "ds_read_b64_tr_b16 v[232:233], v159 offset:0\n"
  "ds_read_b64_tr_b16 v[234:235], v159 offset:512\n"
  "v_mfma_f32_4x4x4_16b_bf16 v[172:175], v[112:113], v[112:113], v[172:175]\n"
  "v_mfma_f32_32x32x16_bf16 v[80:95], v[120:123], v[112:115], v[80:95]\n"
  "ds_read_b64_tr_b16 v[236:237], v159 offset:2048\n"
  "ds_read_b64_tr_b16 v[238:239], v159 offset:2560\n"
  "v_mfma_f32_16x16x32_bf16 v[168:171], v[188:191], v[112:115], v[168:171]\n"
  "v_mfma_f32_4x4x4_16b_bf16 v[172:175], v[114:115], v[114:115], v[172:175]\n"
  "v_mfma_f32_32x32x16_bf16 v[96:111], v[112:115], v[124:127], v[96:111]\n"
  "ds_read_b64_tr_b16 v[240:241], v159 offset:4096\n"
  "ds_read_b64_tr_b16 v[242:243], v159 offset:4608\n"
  "s_waitcnt lgkmcnt(7)\n"
  "v_mfma_f32_32x32x16_bf16 v[64:79], v[132:135], v[128:131], v[64:79]\n"
  "s_barrier\n"
  "ds_read_b128 v[112:115], v152 offset:32768\n"
  "ds_read_b64_tr_b16 v[116:117], v156 offset:32768\n"
  "ds_read_b64_tr_b16 v[118:119], v156 offset:33280\n"
  "v_mfma_f32_32x32x16_bf16 v[80:95], v[136:139], v[128:131], v[80:95]\n"
  "ds_read_b64_tr_b16 v[120:121], v156 offset:34816\n"
  "ds_read_b64_tr_b16 v[122:123], v156 offset:35328\n"
  "v_mfma_f32_16x16x32_bf16 v[176:179], v[188:191], v[132:135], v[176:179]\n"
  "v_mfma_f32_16x16x32_bf16 v[180:183], v[188:191], v[136:139], v[180:183]\n"
  "v_mfma_f32_16x16x32_bf16 v[184:187], v[188:191], v[140:143], v[184:187]\n"
  "v_mfma_f32_32x32x16_bf16 v[96:111], v[128:131], v[140:143], v[96:111]\n"
  "ds_read_b64_tr_b16 v[124:125], v156 offset:36864\n"
  "ds_read_b64_tr_b16 v[126:127], v156 offset:37376\n"
  "s_waitcnt lgkmcnt(7)\n"
  "v_mfma_f32_32x32x16_bf16 v[64:79], v[232:235], v[228:231], v[64:79]\n"
  "ds_read_b128 v[128:131], v153 offset:32768\n"
  "ds_read_b64_tr_b16 v[132:133], v157 offset:32768\n"
  "ds_read_b64_tr_b16 v[134:135], v157 offset:33280\n"
  "v_mfma_f32_32x32x16_bf16 v[80:95], v[236:239], v[228:231], v[80:95]\n"
  "ds_read_b64_tr_b16 v[136:137], v157 offset:34816\n"
  "ds_read_b64_tr_b16 v[138:139], v157 offset:35328\n"
  "v_mfma_f32_32x32x16_bf16 v[96:111], v[228:231], v[240:243], v[96:111]\n"
  "ds_read_b64_tr_b16 v[140:141], v157 offset:36864\n"
  "ds_read_b64_tr_b16 v[142:143], v157 offset:37376\n"
  "s_waitcnt lgkmcnt(7)\n"
  "v_mfma_f32_32x32x16_bf16 v[64:79], v[116:119], v[112:115], v[64:79]\n"
  "ds_read_b128 v[228:231], v154 offset:32768\n"
  "ds_read_b64_tr_b16 v[232:233], v158 offset:32768\n"
  "ds_read_b64_tr_b16 v[234:235], v158 offset:33280\n"
  "v_mfma_f32_4x4x4_16b_bf16 v[172:175], v[112:113], v[112:113], v[172:175]\n"
  "v_mfma_f32_32x32x16_bf16 v[80:95], v[120:123], v[112:115], v[80:95]\n"
  "ds_read_b64_tr_b16 v[236:237], v158 offset:34816\n"
  "ds_read_b64_tr_b16 v[238:239], v158 offset:35328\n"
  "v_mfma_f32_16x16x32_bf16 v[168:171], v[188:191], v[112:115], v[168:171]\n"
  "v_mfma_f32_4x4x4_16b_bf16 v[172:175], v[114:115], v[114:115], v[172:175]\n"
  "v_mfma_f32_16x16x32_bf16 v[176:179], v[188:191], v[116:119], v[176:179]\n"
  "v_mfma_f32_16x16x32_bf16 v[180:183], v[188:191], v[120:123], v[180:183]\n"
  "v_mfma_f32_16x16x32_bf16 v[184:187], v[188:191], v[124:127], v[184:187]\n"
  "v_mfma_f32_32x32x16_bf16 v[96:111], v[112:115], v[124:127], v[96:111]\n"
  "ds_read_b64_tr_b16 v[240:241], v158 offset:36864\n"
  "ds_read_b64_tr_b16 v[242:243], v158 offset:37376\n"
  "s_waitcnt lgkmcnt(7)\n"
  "v_mfma_f32_32x32x16_bf16 v[64:79], v[132:135], v[128:131], v[64:79]\n"
  "ds_read_b128 v[112:115], v155 offset:32768\n"
  "ds_read_b64_tr_b16 v[116:117], v159 offset:32768\n"
  "ds_read_b64_tr_b16 v[118:119], v159 offset:33280\n"
  "v_mfma_f32_4x4x4_16b_bf16 v[172:175], v[128:129], v[128:129], v[172:175]\n"
  "v_mfma_f32_32x32x16_bf16 v[80:95], v[136:139], v[128:131], v[80:95]\n"
  "ds_read_b64_tr_b16 v[120:121], v159 offset:34816\n"
  "ds_read_b64_tr_b16 v[122:123], v159 offset:35328\n"
  "v_mfma_f32_16x16x32_bf16 v[168:171], v[188:191], v[128:131], v[168:171]\n"
  "v_mfma_f32_4x4x4_16b_bf16 v[172:175], v[130:131], v[130:131], v[172:175]\n"
  "v_mfma_f32_32x32x16_bf16 v[96:111], v[128:131], v[140:143], v[96:111]\n"
  "ds_read_b64_tr_b16 v[124:125], v159 offset:36864\n"
  "ds_read_b64_tr_b16 v[126:127], v159 offset:37376\n"
  "s_waitcnt lgkmcnt(7)\n"
  "v_mfma_f32_32x32x16_bf16 v[64:79], v[232:235], v[228:231], v[64:79]\n"
  "s_barrier\n"
  "ds_read_b128 v[128:131], v160 offset:0\n"
  "ds_read_b64_tr_b16 v[132:133], v164 offset:0\n"
  "ds_read_b64_tr_b16 v[134:135], v164 offset:512\n"
  "v_mfma_f32_32x32x16_bf16 v[80:95], v[236:239], v[228:231], v[80:95]\n"
  "ds_read_b64_tr_b16 v[136:137], v164 offset:2048\n"
  "ds_read_b64_tr_b16 v[138:139], v164 offset:2560\n"
  "v_mfma_f32_16x16x32_bf16 v[176:179], v[188:191], v[232:235], v[176:179]\n"
  "v_mfma_f32_16x16x32_bf16 v[180:183], v[188:191], v[236:239], v[180:183]\n"
  "v_mfma_f32_16x16x32_bf16 v[184:187], v[188:191], v[240:243], v[184:187]\n"
  "v_mfma_f32_32x32x16_bf16 v[96:111], v[228:231], v[240:243], v[96:111]\n"
  "ds_read_b64_tr_b16 v[140:141], v164 offset:4096\n"
  "ds_read_b64_tr_b16 v[142:143], v164 offset:4608\n"
  "s_waitcnt lgkmcnt(7)\n"
  "v_mfma_f32_32x32x16_bf16 v[64:79], v[116:119], v[112:115], v[64:79]\n"
  "ds_read_b128 v[228:231], v161 offset:0\n"
  "ds_read_b64_tr_b16 v[232:233], v165 offset:0\n"
  "ds_read_b64_tr_b16 v[234:235], v165 offset:512\n"
  "v_mfma_f32_32x32x16_bf16 v[80:95], v[120:123], v[112:115], v[80:95]\n"
  "ds_read_b64_tr_b16 v[236:237], v165 offset:2048\n"
  "ds_read_b64_tr_b16 v[238:239], v165 offset:2560\n"
  "v_mfma_f32_32x32x16_bf16 v[96:111], v[112:115], v[124:127], v[96:111]\n"
  "ds_read_b64_tr_b16 v[240:241], v165 offset:4096\n"
  "ds_read_b64_tr_b16 v[242:243], v165 offset:4608\n"
  "s_waitcnt lgkmcnt(7)\n"
  "v_mfma_f32_32x32x16_bf16 v[64:79], v[132:135], v[128:131], v[64:79]\n"
  "ds_read_b128 v[112:115], v162 offset:0\n"
  "ds_read_b64_tr_b16 v[116:117], v166 offset:0\n"
  "ds_read_b64_tr_b16 v[118:119], v166 offset:512\n"
  "v_mfma_f32_4x4x4_16b_bf16 v[172:175], v[128:129], v[128:129], v[172:175]\n"
  "v_mfma_f32_32x32x16_bf16 v[80:95], v[136:139], v[128:131], v[80:95]\n"
  "ds_read_b64_tr_b16 v[120:121], v166 offset:2048\n"
  "ds_read_b64_tr_b16 v[122:123], v166 offset:2560\n"
  "v_mfma_f32_16x16x32_bf16 v[168:171], v[188:191], v[128:131], v[168:171]\n"
  "v_mfma_f32_4x4x4_16b_bf16 v[172:175], v[130:131], v[130:131], v[172:175]\n"
  "v_mfma_f32_16x16x32_bf16 v[176:179], v[188:191], v[132:135], v[176:179]\n"
  "v_mfma_f32_16x16x32_bf16 v[180:183], v[188:191], v[136:139], v[180:183]\n"
  "v_mfma_f32_16x16x32_bf16 v[184:187], v[188:191], v[140:143], v[184:187]\n"
  "v_mfma_f32_32x32x16_bf16 v[96:111], v[128:131], v[140:143], v[96:111]\n"
  "ds_read_b64_tr_b16 v[124:125], v166 offset:4096\n"
  "ds_read_b64_tr_b16 v[126:127], v166 offset:4608\n"
  "s_waitcnt lgkmcnt(7)\n"
  "v_mfma_f32_32x32x16_bf16 v[64:79], v[232:235], v[228:231], v[64:79]\n"
  "ds_read_b128 v[128:131], v163 offset:0\n"
  "ds_read_b64_tr_b16 v[132:133], v167 offset:0\n"
  "ds_read_b64_tr_b16 v[134:135], v167 offset:512\n"
  "v_mfma_f32_4x4x4_16b_bf16 v[172:175], v[228:229], v[228:229], v[172:175]\n"
  "v_mfma_f32_32x32x16_bf16 v[80:95], v[236:239], v[228:231], v[80:95]\n"
  "ds_read_b64_tr_b16 v[136:137], v167 offset:2048\n"
  "ds_read_b64_tr_b16 v[138:139], v167 offset:2560\n"
  "v_mfma_f32_16x16x32_bf16 v[168:171], v[188:191], v[228:231], v[168:171]\n"
  "v_mfma_f32_4x4x4_16b_bf16 v[172:175], v[230:231], v[230:231], v[172:175]\n"
  "v_mfma_f32_32x32x16_bf16 v[96:111], v[228:231], v[240:243], v[96:111]\n"
  "ds_read_b64_tr_b16 v[140:141], v167 offset:4096\n"
  "ds_read_b64_tr_b16 v[142:143], v167 offset:4608\n"
  "s_waitcnt lgkmcnt(7)\n"
  "v_mfma_f32_32x32x16_bf16 v[64:79], v[116:119], v[112:115], v[64:79]\n"
  "s_barrier\n"
  "ds_read_b128 v[228:231], v144 offset:0\n"
  "ds_read_b64_tr_b16 v[232:233], v148 offset:0\n"
  "ds_read_b64_tr_b16 v[234:235], v148 offset:512\n"
  "v_mfma_f32_32x32x16_bf16 v[80:95], v[120:123], v[112:115], v[80:95]\n"
  "ds_read_b64_tr_b16 v[236:237], v148 offset:2048\n"
  "ds_read_b64_tr_b16 v[238:239], v148 offset:2560\n"
  "v_mfma_f32_16x16x32_bf16 v[176:179], v[188:191], v[116:119], v[176:179]\n"
  "v_mfma_f32_16x16x32_bf16 v[180:183], v[188:191], v[120:123], v[180:183]\n"
  "v_mfma_f32_16x16x32_bf16 v[184:187], v[188:191], v[124:127], v[184:187]\n"
  "v_mfma_f32_32x32x16_bf16 v[96:111], v[112:115], v[124:127], v[96:111]\n"
  "ds_read_b64_tr_b16 v[240:241], v148 offset:4096\n"
  "ds_read_b64_tr_b16 v[242:243], v148 offset:4608\n"
  "s_waitcnt lgkmcnt(7)\n"
  "v_mfma_f32_32x32x16_bf16 v[64:79], v[132:135], v[128:131], v[64:79]\n"
  "ds_read_b128 v[112:115], v145 offset:0\n"
  "ds_read_b64_tr_b16 v[116:117], v149 offset:0\n"
  "ds_read_b64_tr_b16 v[118:119], v149 offset:512\n"
  "v_mfma_f32_32x32x16_bf16 v[80:95], v[136:139], v[128:131], v[80:95]\n"
  "ds_read_b64_tr_b16 v[120:121], v149 offset:2048\n"
  "ds_read_b64_tr_b16 v[122:123], v149 offset:2560\n"
  "v_mfma_f32_32x32x16_bf16 v[96:111], v[128:131], v[140:143], v[96:111]\n"
  "ds_read_b64_tr_b16 v[124:125], v149 offset:4096\n"
  "ds_read_b64_tr_b16 v[126:127], v149 offset:4608\n"
  "s_waitcnt lgkmcnt(7)\n"
  "v_mfma_f32_32x32x16_bf16 v[64:79], v[232:235], v[228:231], v[64:79]\n"
  "ds_read_b128 v[128:131], v146 offset:0\n"
  "ds_read_b64_tr_b16 v[132:133], v150 offset:0\n"
  "ds_read_b64_tr_b16 v[134:135], v150 offset:512\n"
  "v_mfma_f32_4x4x4_16b_bf16 v[172:175], v[228:229], v[228:229], v[172:175]\n"
  "v_mfma_f32_32x32x16_bf16 v[80:95], v[236:239], v[228:231], v[80:95]\n"
  "ds_read_b64_tr_b16 v[136:137], v150 offset:2048\n"
  "ds_read_b64_tr_b16 v[138:139], v150 offset:2560\n"
  "v_mfma_f32_16x16x32_bf16 v[168:171], v[188:191], v[228:231], v[168:171]\n"
  "v_mfma_f32_4x4x4_16b_bf16 v[172:175], v[230:231], v[230:231], v[172:175]\n"
  "v_mfma_f32_16x16x32_bf16 v[176:179], v[188:191], v[232:235], v[176:179]\n"
  "v_mfma_f32_16x16x32_bf16 v[180:183], v[188:191], v[236:239], v[180:183]\n"
  "v_mfma_f32_16x16x32_bf16 v[184:187], v[188:191], v[240:243], v[184:187]\n"
  "v_mfma_f32_32x32x16_bf16 v[96:111], v[228:231], v[240:243], v[96:111]\n"
  "ds_read_b64_tr_b16 v[140:141], v150 offset:4096\n"
  "ds_read_b64_tr_b16 v[142:143], v150 offset:4608\n"
  "s_waitcnt lgkmcnt(7)\n"
  "v_mfma_f32_32x32x16_bf16 v[64:79], v[116:119], v[112:115], v[64:79]\n"
  "ds_read_b128 v[228:231], v147 offset:0\n"
  "ds_read_b64_tr_b16 v[232:233], v151 offset:0\n"
  "ds_read_b64_tr_b16 v[234:235], v151 offset:512\n"
  "v_mfma_f32_4x4x4_16b_bf16 v[172:175], v[112:113], v[112:113], v[172:175]\n"
  "v_mfma_f32_32x32x16_bf16 v[80:95], v[120:123], v[112:115], v[80:95]\n"
  "ds_read_b64_tr_b16 v[236:237], v151 offset:2048\n"
  "ds_read_b64_tr_b16 v[238:239], v151 offset:2560\n"
  "v_mfma_f32_16x16x32_bf16 v[168:171], v[188:191], v[112:115], v[168:171]\n"
  "v_mfma_f32_4x4x4_16b_bf16 v[172:175], v[114:115], v[114:115], v[172:175]\n"
  "v_mfma_f32_32x32x16_bf16 v[96:111], v[112:115], v[124:127], v[96:111]\n"
  "ds_read_b64_tr_b16 v[240:241], v151 offset:4096\n"
  "ds_read_b64_tr_b16 v[242:243], v151 offset:4608\n"
  "s_waitcnt lgkmcnt(7)\n"
  "v_mfma_f32_32x32x16_bf16 v[64:79], v[132:135], v[128:131], v[64:79]\n"
  "s_barrier\n"
  "ds_read_b128 v[112:115], v144 offset:32768\n"
  "ds_read_b64_tr_b16 v[116:117], v148 offset:32768\n"
  "ds_read_b64_tr_b16 v[118:119], v148 offset:33280\n"
  "v_mfma_f32_32x32x16_bf16 v[80:95], v[136:139], v[128:131], v[80:95]\n"
  "ds_read_b64_tr_b16 v[120:121], v148 offset:34816\n"
  "ds_read_b64_tr_b16 v[122:123], v148 offset:35328\n"
  "v_mfma_f32_16x16x32_bf16 v[176:179], v[188:191], v[132:135], v[176:179]\n"
  "v_mfma_f32_16x16x32_bf16 v[180:183], v[188:191], v[136:139], v[180:183]\n"
  "v_mfma_f32_16x16x32_bf16 v[184:187], v[188:191], v[140:143], v[184:187]\n"
  "v_mfma_f32_32x32x16_bf16 v[96:111], v[128:131], v[140:143], v[96:111]\n"
  "ds_read_b64_tr_b16 v[124:125], v148 offset:36864\n"
  "ds_read_b64_tr_b16 v[126:127], v148 offset:37376\n"
  "s_waitcnt lgkmcnt(7)\n"
  "v_mfma_f32_32x32x16_bf16 v[64:79], v[232:235], v[228:231], v[64:79]\n"
  "ds_read_b128 v[128:131], v145 offset:32768\n"
  "ds_read_b64_tr_b16 v[132:133], v149 offset:32768\n"
  "ds_read_b64_tr_b16 v[134:135], v149 offset:33280\n"
  "v_mfma_f32_32x32x16_bf16 v[80:95], v[236:239], v[228:231], v[80:95]\n"
  "ds_read_b64_tr_b16 v[136:137], v149 offset:34816\n"
  "ds_read_b64_tr_b16 v[138:139], v149 offset:35328\n"
  "v_mfma_f32_32x32x16_bf16 v[96:111], v[228:231], v[240:243], v[96:111]\n"
  "ds_read_b64_tr_b16 v[140:141], v149 offset:36864\n"
  "ds_read_b64_tr_b16 v[142:143], v149 offset:37376\n"
  "s_waitcnt lgkmcnt(7)\n"
  "v_mfma_f32_32x32x16_bf16 v[64:79], v[116:119], v[112:115], v[64:79]\n"
  "ds_read_b128 v[228:231], v146 offset:32768\n"
  "ds_read_b64_tr_b16 v[232:233], v150 offset:32768\n"
  "ds_read_b64_tr_b16 v[234:235], v150 offset:33280\n"
  "v_mfma_f32_4x4x4_16b_bf16 v[172:175], v[112:113], v[112:113], v[172:175]\n"
  "v_mfma_f32_32x32x16_bf16 v[80:95], v[120:123], v[112:115], v[80:95]\n"
  "ds_read_b64_tr_b16 v[236:237], v150 offset:34816\n"
  "ds_read_b64_tr_b16 v[238:239], v150 offset:35328\n"
  "v_mfma_f32_16x16x32_bf16 v[168:171], v[188:191], v[112:115], v[168:171]\n"
  "v_mfma_f32_4x4x4_16b_bf16 v[172:175], v[114:115], v[114:115], v[172:175]\n"
  "v_mfma_f32_16x16x32_bf16 v[176:179], v[188:191], v[116:119], v[176:179]\n"
  "v_mfma_f32_16x16x32_bf16 v[180:183], v[188:191], v[120:123], v[180:183]\n"
  "v_mfma_f32_16x16x32_bf16 v[184:187], v[188:191], v[124:127], v[184:187]\n"
  "v_mfma_f32_32x32x16_bf16 v[96:111], v[112:115], v[124:127], v[96:111]\n"
  "ds_read_b64_tr_b16 v[240:241], v150 offset:36864\n"
  "ds_read_b64_tr_b16 v[242:243], v150 offset:37376\n"
  "s_waitcnt lgkmcnt(7)\n"
  "v_mfma_f32_32x32x16_bf16 v[64:79], v[132:135], v[128:131], v[64:79]\n"
  "ds_read_b128 v[112:115], v147 offset:32768\n"
  "ds_read_b64_tr_b16 v[116:117], v151 offset:32768\n"
  "ds_read_b64_tr_b16 v[118:119], v151 offset:33280\n"
  "v_mfma_f32_4x4x4_16b_bf16 v[172:175], v[128:129], v[128:129], v[172:175]\n"
  "v_mfma_f32_32x32x16_bf16 v[80:95], v[136:139], v[128:131], v[80:95]\n"
  "ds_read_b64_tr_b16 v[120:121], v151 offset:34816\n"
  "ds_read_b64_tr_b16 v[122:123], v151 offset:35328\n"
  "v_mfma_f32_16x16x32_bf16 v[168:171], v[188:191], v[128:131], v[168:171]\n"
  "v_mfma_f32_4x4x4_16b_bf16 v[172:175], v[130:131], v[130:131], v[172:175]\n"
  "v_mfma_f32_32x32x16_bf16 v[96:111], v[128:131], v[140:143], v[96:111]\n"
  "ds_read_b64_tr_b16 v[124:125], v151 offset:36864\n"
  "ds_read_b64_tr_b16 v[126:127], v151 offset:37376\n"
  "s_waitcnt lgkmcnt(7)\n"
  "v_mfma_f32_32x32x16_bf16 v[64:79], v[232:235], v[228:231], v[64:79]\n"
  "v_mfma_f32_32x32x16_bf16 v[80:95], v[236:239], v[228:231], v[80:95]\n"
  "v_mfma_f32_16x16x32_bf16 v[176:179], v[188:191], v[232:235], v[176:179]\n"
  "v_mfma_f32_16x16x32_bf16 v[180:183], v[188:191], v[236:239], v[180:183]\n"
  "v_mfma_f32_16x16x32_bf16 v[184:187], v[188:191], v[240:243], v[184:187]\n"
  "v_mfma_f32_32x32x16_bf16 v[96:111], v[228:231], v[240:243], v[96:111]\n"
  "v_cndmask_b32 v18, v172, v173, s[70:71]\n"
  "v_cndmask_b32 v19, v174, v175, s[70:71]\n"
  "v_cndmask_b32 v169, v18, v19, s[72:73]\n"
  "v_mov_b32 v12, 0x3b124925\n"
  "ds_write_b64 v208, v[168:169]\n"
  "s_nop 0\n"
  "ds_write2st64_b32 v210, v176, v180 offset1:1\n"
  "ds_write_b32 v210, v184 offset:512\n"
  "s_waitcnt lgkmcnt(3)\n"
  "v_mfma_f32_32x32x16_bf16 v[64:79], v[116:119], v[112:115], v[64:79]\n"
  "v_mfma_f32_32x32x16_bf16 v[80:95], v[120:123], v[112:115], v[80:95]\n"
  "s_waitcnt lgkmcnt(0)\n"
  "s_barrier\n"
  "ds_read_b64 v[18:19], v209\n"
  "ds_read2st64_b32 v[20:21], v211 offset1:1\n"
  "ds_read_b32 v22, v211 offset:512\n"
  "v_mfma_f32_32x32x16_bf16 v[96:111], v[112:115], v[124:127], v[96:111]\n"
  "s_waitcnt lgkmcnt(2)\n"
  "v_pk_add_f32 v[168:169], v[168:169], v[18:19]\n"
  "v_mov_b32 v19, v169\n"
  "v_mul_f32 v168, v12, v168\n"
  "s_nop 0\n"
  "v_permlane32_swap_b32 v169, v19\n"
  "v_mul_f32 v11, v168, v168\n"
  "v_add_f32 v19, v19, v169\n"
  "v_fma_f32 v11, v19, v12, -v11\n"
  "v_max_f32 v11, 0, v11\n"
  "v_add_f32 v11, 0x3727c5ac, v11\n"
  "v_rsq_f32 v12, v11\n"
  "v_mul_f32 v11, v168, v214\n"
  "v_sub_f32 v11, 0, v11\n"
  "ds_write_b32 v212, v12\n"
  "ds_read_b128 v[128:131], v213 offset:0\n"
  "ds_read_b128 v[132:135], v213 offset:32\n"
  "ds_read_b128 v[136:139], v213 offset:64\n"
  "ds_read_b128 v[140:143], v213 offset:96\n"
  "v_and_b32 v14, 0xffff0000, v11\n"
  "v_sub_f32 v15, v11, v14\n"
  "v_cvt_pk_bf16_f32 v204, v14, v15\n"
  "v_mov_b32 v205, v204\n"
  "s_waitcnt lgkmcnt(5)\n"
  "v_add_f32 v176, v176, v20\n"
  "v_and_b32 v177, 0xffff0000, v176\n"
  "v_sub_f32 v178, v176, v177\n"
  "v_cvt_pk_bf16_f32 v192, v177, v177\n"
  "v_cvt_pk_bf16_f32 v193, v178, v178\n"
  "v_add_f32 v180, v180, v21\n"
  "v_and_b32 v181, 0xffff0000, v180\n"
  "v_sub_f32 v182, v180, v181\n"
  "v_cvt_pk_bf16_f32 v196, v181, v181\n"
  "v_cvt_pk_bf16_f32 v197, v182, v182\n"
  "v_mfma_f32_32x32x16_bf16 v[64:79], v[192:195], v[204:207], v[64:79]\n"
  "v_add_f32 v184, v184, v22\n"
  "v_and_b32 v185, 0xffff0000, v184\n"
  "v_sub_f32 v186, v184, v185\n"
  "v_cvt_pk_bf16_f32 v200, v185, v185\n"
  "v_cvt_pk_bf16_f32 v201, v186, v186\n"
  "v_mfma_f32_32x32x16_bf16 v[80:95], v[196:199], v[204:207], v[80:95]\n"
  "v_mul_f32 v14, 0x3e8b97f6, v12\n"
  "v_mfma_f32_32x32x16_bf16 v[96:111], v[204:207], v[200:203], v[96:111]\n"
  "buffer_store_dwordx4 v[24:27], v28, s[36:39], s75 offen sc1\n"
  "s_nop 2\n"
  "v_pk_mul_f32 v[64:65], v[64:65], v[14:15] op_sel_hi:[1,0]\n"
  "v_pk_mul_f32 v[66:67], v[66:67], v[14:15] op_sel_hi:[1,0]\n"
  "v_pk_mul_f32 v[68:69], v[68:69], v[14:15] op_sel_hi:[1,0]\n"
  "v_pk_mul_f32 v[70:71], v[70:71], v[14:15] op_sel_hi:[1,0]\n"
  "v_pk_mul_f32 v[72:73], v[72:73], v[14:15] op_sel_hi:[1,0]\n"
  "v_pk_mul_f32 v[74:75], v[74:75], v[14:15] op_sel_hi:[1,0]\n"
  "v_pk_mul_f32 v[76:77], v[76:77], v[14:15] op_sel_hi:[1,0]\n"
  "v_pk_mul_f32 v[78:79], v[78:79], v[14:15] op_sel_hi:[1,0]\n"
  "v_cvt_pk_bf16_f32 v144, v64, v65\n"
  "v_cvt_pk_bf16_f32 v145, v66, v67\n"
  "v_cvt_pk_bf16_f32 v146, v68, v69\n"
  "v_cvt_pk_bf16_f32 v147, v70, v71\n"
  "v_cvt_pk_bf16_f32 v148, v72, v73\n"
  "v_cvt_pk_bf16_f32 v149, v74, v75\n"
  "v_cvt_pk_bf16_f32 v150, v76, v77\n"
  "v_cvt_pk_bf16_f32 v151, v78, v79\n"
  "s_cmp_lg_u64 s[66:67], 0\n"
  "s_cbranch_scc1 .La_q0\n"
  ".La_q0b:\n"
  "buffer_store_dwordx4 v[144:147], v216, s[36:39], s74 offen sc1\n"
  "s_cmp_lg_u64 s[68:69], 0\n"
  "s_cbranch_scc1 .La_q1\n"
  ".La_q1b:\n"
  "buffer_store_dwordx4 v[148:151], v217, s[36:39], s74 offen sc1\n"
  "v_pk_mul_f32 v[80:81], v[80:81], v[12:13] op_sel_hi:[1,0]\n"
  "v_pk_mul_f32 v[82:83], v[82:83], v[12:13] op_sel_hi:[1,0]\n"
  "v_pk_mul_f32 v[84:85], v[84:85], v[12:13] op_sel_hi:[1,0]\n"
  "v_pk_mul_f32 v[86:87], v[86:87], v[12:13] op_sel_hi:[1,0]\n"
  "v_pk_mul_f32 v[88:89], v[88:89], v[12:13] op_sel_hi:[1,0]\n"
  "v_pk_mul_f32 v[90:91], v[90:91], v[12:13] op_sel_hi:[1,0]\n"
  "v_pk_mul_f32 v[92:93], v[92:93], v[12:13] op_sel_hi:[1,0]\n"
  "v_pk_mul_f32 v[94:95], v[94:95], v[12:13] op_sel_hi:[1,0]\n"
  "v_cvt_pk_bf16_f32 v152, v80, v81\n"
  "v_cvt_pk_bf16_f32 v153, v82, v83\n"
  "v_cvt_pk_bf16_f32 v154, v84, v85\n"
  "v_cvt_pk_bf16_f32 v155, v86, v87\n"
  "v_cvt_pk_bf16_f32 v156, v88, v89\n"
  "v_cvt_pk_bf16_f32 v157, v90, v91\n"
  "v_cvt_pk_bf16_f32 v158, v92, v93\n"
  "v_cvt_pk_bf16_f32 v159, v94, v95\n"
  "s_cmp_lg_u64 s[66:67], 0\n"
  "s_cbranch_scc1 .La_k0\n"
  ".La_k0b:\n"
  "buffer_store_dwordx4 v[152:155], v218, s[36:39], s75 offen sc1\n"
  "s_cmp_lg_u64 s[68:69], 0\n"
  "s_cbranch_scc1 .La_k1\n"
  ".La_k1b:\n"
  "buffer_store_dwordx4 v[156:159], v219, s[36:39], s75 offen sc1\n"
  "s_waitcnt lgkmcnt(0)\n"
  "v_pk_mul_f32 v[96:97], v[96:97], v[128:129]\n"
  "v_pk_mul_f32 v[98:99], v[98:99], v[130:131]\n"
  "v_pk_mul_f32 v[100:101], v[100:101], v[132:133]\n"
  "v_pk_mul_f32 v[102:103], v[102:103], v[134:135]\n"
  "v_pk_mul_f32 v[104:105], v[104:105], v[136:137]\n"
  "v_pk_mul_f32 v[106:107], v[106:107], v[138:139]\n"
  "v_pk_mul_f32 v[108:109], v[108:109], v[140:141]\n"
  "v_pk_mul_f32 v[110:111], v[110:111], v[142:143]\n"
  "v_cvt_pk_bf16_f32 v160, v96, v97\n"
  "v_cvt_pk_bf16_f32 v161, v98, v99\n"
  "v_cvt_pk_bf16_f32 v162, v100, v101\n"
  "v_cvt_pk_bf16_f32 v163, v102, v103\n"
  "v_cvt_pk_bf16_f32 v164, v104, v105\n"
  "v_cvt_pk_bf16_f32 v165, v106, v107\n"
  "v_cvt_pk_bf16_f32 v166, v108, v109\n"
  "v_cvt_pk_bf16_f32 v167, v110, v111\n"
  "buffer_store_dwordx4 v[160:163], v222, s[36:39], s75 offen offset:2048 sc1\n"
  "buffer_store_dwordx4 v[164:167], v222, s[36:39], s75 offen offset:3072 sc1\n"
  "s_endpgm\n"
  ".La_q0:\n"
  "v_mov_b32 v226, v146\n"
  "v_mov_b32 v227, v147\n"
  "v_cndmask_b32 v146, v146, 0, s[66:67]\n"
  "v_cndmask_b32 v147, v147, 0, s[66:67]\n"
  "s_mov_b64 exec, s[66:67]\n"
  "buffer_store_dwordx4 v[224:227], v220, s[36:39], s74 offen sc1\n"
  "s_mov_b64 exec, -1\n"
  "s_branch .La_q0b\n"
  ".La_q1:\n"
  "v_mov_b32 v226, v150\n"
  "v_mov_b32 v227, v151\n"
  "v_cndmask_b32 v150, v150, 0, s[68:69]\n"
  "v_cndmask_b32 v151, v151, 0, s[68:69]\n"
  "s_mov_b64 exec, s[68:69]\n"
  "buffer_store_dwordx4 v[224:227], v220, s[36:39], s74 offen sc1\n"
  "s_mov_b64 exec, -1\n"
  "s_branch .La_q1b\n"
  ".La_k0:\n"
  "s_mov_b64 exec, s[66:67]\n"
  "buffer_store_dwordx4 v[152:155], v221, s[36:39], s75 offen sc1\n"
  "s_mov_b64 exec, -1\n"
  "s_branch .La_k0b\n"
  ".La_k1:\n"
  "s_mov_b64 exec, s[68:69]\n"
  "buffer_store_dwordx4 v[156:159], v221, s[36:39], s75 offen sc1\n"
  "s_mov_b64 exec, -1\n"
  "s_branch .La_k1b\n"
  ".La_idle:\n"
  "s_waitcnt lgkmcnt(0)\n"
  "s_mov_b64 s[36:37], s[14:15]\n"
  "s_mov_b32 s38, 16777216\n"
  "s_mov_b32 s39, 0x20000\n"
  "s_cmp_eq_u32 s48, 4\n"
  "s_cbranch_scc1 .La_stop\n"
  "s_sub_u32 s53, s50, 24\n"
  "s_lshl_b32 s53, s53, 2\n"
  "s_lshr_b32 s54, s49, 1\n"
  "s_add_u32 s53, s53, s54\n"
  "s_lshl_b32 s53, s53, 2\n"
  "s_add_u32 s53, s53, s48\n"
  "v_lshlrev_b32 v12, 7, v1\n"
  "s_cmp_ge_u32 s53, 8\n"
  "s_cbranch_scc1 .La_i2\n"
  "s_getpc_b64 s[76:77]\n"
  "s_add_u32 s76, s76, k_b@rel32@lo+4\n"
  "s_addc_u32 s77, s77, k_b@rel32@hi+12\n"
  "s_mov_b32 s78, .Lb_codeend-k_b\n"
  "s_mov_b32 s79, 0x20000\n"
  "s_lshl_b32 s54, s53, 13\n"
  "buffer_load_dword v11, v12, s[76:79], s54 offen\n"
  ".La_i2:\n"
  "s_mov_b64 s[40:41], s[4:5]\n"
  "s_mov_b32 s42, 1835008\n"
  "s_mov_b32 s43, 0x20000\n"
  "s_mov_b64 s[44:45], s[6:7]\n"
  "s_mov_b32 s46, 1204224\n"
  "s_mov_b32 s47, 0x20000\n"
  "s_mul_i32 s54, s53, 64\n"
  "v_add_u32 v13, s54, v1\n"
  "v_and_b32 v14, 2047, v13\n"
  "v_mul_u32_u24 v14, 896, v14\n"
  "v_lshrrev_b32 v13, 11, v13\n"
  "v_lshl_add_u32 v13, v13, 7, v14\n"
  "s_mul_i32 s54, s53, 5376\n"
  "s_add_u32 s54, s54, 516096\n"
  "s_bfm_b64 exec, 42, 0\n"
  "buffer_load_dword v11, v12, s[44:47], s54 offen\n"
  "s_mov_b64 exec, -1\n"
  "buffer_load_dword v11, v13, s[40:43], 0 offen offset:384\n"
  "s_mov_b64 exec, -1\n"
  "v_mov_b32 v4, -1\n"
  "v_mov_b32 v5, -1\n"
  "v_mov_b32 v6, -1\n"
  "v_mov_b32 v7, -1\n"
  "v_lshlrev_b32 v3, 4, v1\n"
  "s_mul_i32 s54, s53, 45056\n"
  "s_add_u32 s54, s54, 6291456\n"
  "s_mov_b32 s61, 11\n"
  ".La_rs:\n"
  "buffer_store_dwordx4 v[4:7], v3, s[36:39], s54 offen offset:0 sc1\n"
  "buffer_store_dwordx4 v[4:7], v3, s[36:39], s54 offen offset:1024 sc1\n"
  "buffer_store_dwordx4 v[4:7], v3, s[36:39], s54 offen offset:2048 sc1\n"
  "buffer_store_dwordx4 v[4:7], v3, s[36:39], s54 offen offset:3072 sc1\n"
  "s_add_u32 s54, s54, 4096\n"
  "s_sub_u32 s61, s61, 1\n"
  "s_cmp_lg_u32 s61, 0\n"
  "s_cbranch_scc1 .La_rs\n"
  ".La_stop:\n"
  "s_endpgm\n"
  ".La_end:\n"
    :: [ka] "s"(__builtin_amdgcn_kernarg_segment_ptr()), [bid] "s"(blockIdx.x), [tid] "v"(threadIdx.x)
    : "v1","v2","v3","v4","v5","v6","v7","v8","v9","v10","v11","v12","v13","v14","v15","v16","v17","v18","v19","v20","v21","v22","v23","v24","v25","v26","v27","v28","v29","v30","v31","v32","v33","v34","v35","v36","v37","v38","v39","v40","v41","v42","v43","v44","v45","v46","v47","v48","v49","v50","v51","v52","v53","v54","v55","v56","v57","v58","v59","v60","v61","v62","v63","v64","v65","v66","v67","v68","v69","v70","v71","v72","v73","v74","v75","v76","v77","v78","v79","v80","v81","v82","v83","v84","v85","v86","v87","v88","v89","v90","v91","v92","v93","v94","v95","v96","v97","v98","v99","v100","v101","v102","v103","v104","v105","v106","v107","v108","v109","v110","v111","v112","v113","v114","v115","v116","v117","v118","v119","v120","v121","v122","v123","v124","v125","v126","v127","v128","v129","v130","v131","v132","v133","v134","v135","v136","v137","v138","v139","v140","v141","v142","v143","v144","v145","v146","v147","v148","v149","v150","v151","v152","v153","v154","v155","v156","v157","v158","v159","v160","v161","v162","v163","v164","v165","v166","v167","v168","v169","v170","v171","v172","v173","v174","v175","v176","v177","v178","v179","v180","v181","v182","v183","v184","v185","v186","v187","v188","v189","v190","v191","v192","v193","v194","v195","v196","v197","v198","v199","v200","v201","v202","v203","v204","v205","v206","v207","v208","v209","v210","v211","v212","v213","v214","v215","v216","v217","v218","v219","v220","v221","v222","v223","v224","v225","v226","v227","v228","v229","v230","v231","v232","v233","v234","v235","v236","v237","v238","v239","v240","v241","v242","v243","v244", "s4","s5","s6","s7","s8","s9","s10","s11","s12","s13","s14","s15","s16","s17","s18","s19","s20","s21","s22","s23","s24","s25","s26","s27","s28","s29","s30","s31","s34","s35","s36","s37","s38","s39","s40","s41","s42","s43","s44","s45","s46","s47","s48","s49","s50","s51","s52","s53","s54","s55","s56","s57","s58","s59","s60","s61","s62","s63","s64","s65","s66","s67","s68","s69","s70","s71","s72","s73","s74","s75","s76","s77","s78","s79","s80","s81","s82","s83","s84","s85","s86","s87","s88","s89","s90","s91","s92","s93","s94","s95","s96","s97","s98","s99", "vcc", "scc", "memory");
}

extern "C" __global__ __launch_bounds__(320, 2) void k_b(const char* x, const char* wo, const char* w1, const char* b1, const char* w2, const char* b2, char* ws, char* out) {
  asm volatile(
  "s_load_dwordx16 s[4:19], %[ka], 0\n"
  "v_lshrrev_b32 v2, 6, %[tid]\n"
  "v_and_b32 v1, 63, %[tid]\n"
  "s_and_b32 s49, %[bid], 7\n"
  "s_lshr_b32 s50, %[bid], 3\n"
  "v_readfirstlane_b32 s48, v2\n"
  "s_lshl_b32 s52, s49, 1\n"
  "s_and_b32 s53, s50, 1\n"
  "s_add_u32 s60, s52, s53\n"
  "s_lshr_b32 s61, s50, 1\n"
  "v_lshlrev_b32 v2, 4, v1\n"
  "s_lshl_b32 s52, s60, 18\n"
  "s_lshl_b32 s53, s48, 16\n"
  "s_add_u32 s52, s52, s53\n"
  "s_add_u32 s30, s52, 2097152\n"
  "s_mov_b32 s42, 65536\n"
  "s_mov_b32 s43, 0x20000\n"
  "s_movk_i32 s65, 4096\n"
  "s_lshl_b32 s52, s60, 17\n"
  "s_lshl_b32 s53, s61, 13\n"
  "s_add_u32 s54, s52, s53\n"
  "s_add_u32 s52, s48, 1\n"
  "s_and_b32 s52, s52, 3\n"
  "s_lshl_b32 s52, s52, 11\n"
  "s_add_u32 s34, s52, s54\n"
  "s_add_u32 s52, s48, 2\n"
  "s_and_b32 s52, s52, 3\n"
  "s_lshl_b32 s52, s52, 11\n"
  "s_add_u32 s35, s52, s54\n"
  "s_add_u32 s52, s48, 3\n"
  "s_and_b32 s52, s52, 3\n"
  "s_lshl_b32 s52, s52, 11\n"
  "s_add_u32 s62, s52, s54\n"
  "s_add_u32 s52, s48, 4\n"
  "s_and_b32 s52, s52, 3\n"
  "s_lshl_b32 s52, s52, 11\n"
  "s_add_u32 s25, s52, s54\n"
  "s_mov_b32 s38, 16777216\n"
  "s_mov_b32 s39, 0x20000\n"
  ".Lb_coord:\n"
  "s_mul_i32 s58, s50, 37\n"
  "s_lshr_b32 s58, s58, 8\n"
  "s_mul_i32 s52, s58, 7\n"
  "s_sub_u32 s59, s50, s52\n"
  "s_lshl_b32 s52, s49, 2\n"
  "s_add_u32 s84, s52, s58\n"
  "s_and_b32 s52, s49, 1\n"
  "s_lshl_b32 s53, s52, 2\n"
  "s_add_u32 s85, s53, s58\n"
  "s_lshr_b32 s54, s49, 1\n"
  "s_mul_i32 s55, s54, 7\n"
  "s_add_u32 s86, s55, s59\n"
  "s_lshr_b32 s55, s50, 3\n"
  "s_add_u32 s87, s53, s55\n"
  "s_lshl_b32 s54, s54, 3\n"
  "s_and_b32 s55, s50, 7\n"
  "s_add_u32 s88, s54, s55\n"
  "s_mul_i32 s53, s52, 7\n"
  "s_sub_u32 s53, 28, s53\n"
  "s_cmp_lt_u32 s50, s53\n"
  "s_cselect_b32 s89, 1, 0\n"
  "s_lshl_b32 s53, s52, 3\n"
  "s_sub_u32 s53, 32, s53\n"
  "s_cmp_lt_u32 s50, s53\n"
  "s_cselect_b32 s90, 1, 0\n"
  "s_mov_b32 s63, 0\n"
  "s_cmp_eq_u32 s48, 4\n"
  "s_cbranch_scc1 .Lb_help\n"
  "s_mov_b32 s66, 8192\n"
  "s_mov_b32 s67, 12288\n"
  "s_mov_b32 s68, 16384\n"
  "s_mov_b32 s69, 20480\n"
  "s_mov_b32 s70, 24576\n"
  "s_mov_b32 s71, 28672\n"
  "s_mov_b32 s72, 32768\n"
  "s_mov_b32 s73, 36864\n"
  "s_mov_b32 s74, 40960\n"
  "s_mov_b32 s75, 45056\n"
  "s_mov_b32 s76, 49152\n"
  "s_mov_b32 s77, 53248\n"
  "s_mov_b32 s78, 57344\n"
  "s_mov_b32 s79, 61440\n"
  "s_mul_i32 s51, s48, 40960\n"
  "v_add_u32 v6, s51, v2\n"
  "s_lshr_b32 s91, s48, 1\n"
  "s_and_b32 s92, s48, 1\n"
  "s_sub_u32 s52, s48, 1\n"
  "s_and_b32 s52, s52, 3\n"
  "s_mul_i32 s52, s52, 40960\n"
  "v_add_u32 v3, s52, v2\n"
  "s_sub_u32 s52, s48, 2\n"
  "s_and_b32 s52, s52, 3\n"
  "s_mul_i32 s52, s52, 40960\n"
  "v_add_u32 v4, s52, v2\n"
  "s_sub_u32 s52, s48, 3\n"
  "s_and_b32 s52, s52, 3\n"
  "s_mul_i32 s52, s52, 40960\n"
  "v_add_u32 v5, s52, v2\n"
  "v_lshrrev_b32 v13, 5, v1\n"
  "v_bfe_u32 v14, v1, 2, 2\n"
  "v_lshl_add_u32 v13, v13, 3, v14\n"
  "v_lshlrev_b32 v13, 7, v13\n"
  "v_bfe_u32 v14, v1, 3, 1\n"
  "v_xor_b32 v14, s91, v14\n"
  "v_lshl_add_u32 v13, v14, 6, v13\n"
  "v_bfe_u32 v14, v1, 4, 1\n"
  "v_lshl_add_u32 v13, v14, 5, v13\n"
  "v_and_b32 v14, 1, v1\n"
  "v_lshl_add_u32 v13, v14, 4, v13\n"
  "v_bfe_u32 v14, v1, 1, 1\n"
  "v_lshl_add_u32 v13, v14, 3, v13\n"
  "v_add_u32 v7, s51, v13\n"
  "v_xor_b32 v8, 64, v7\n"
  "v_lshrrev_b32 v13, 5, v1\n"
  "v_bfe_u32 v14, v1, 2, 2\n"
  "v_lshl_add_u32 v13, v13, 3, v14\n"
  "v_lshlrev_b32 v13, 7, v13\n"
  "v_bfe_u32 v14, v1, 3, 1\n"
  "v_xor_b32 v14, s91, v14\n"
  "v_lshl_add_u32 v13, v14, 6, v13\n"
  "v_bfe_u32 v14, v1, 4, 1\n"
  "v_lshl_add_u32 v13, v14, 5, v13\n"
  "v_and_b32 v14, 3, v1\n"
  "v_lshl_add_u32 v13, v14, 3, v13\n"
  "v_add_u32 v38, s51, v13\n"
  "v_xor_b32 v39, 64, v38\n"
  "s_waitcnt lgkmcnt(0)\n"
  "s_add_u32 s40, s16, s30\n"
  "s_addc_u32 s41, s17, 0\n"
  "s_mov_b64 s[36:37], s[16:17]\n"
  "buffer_load_dwordx4 v[192:195], v2, s[40:43], 0 offen offset:0\n"
  "buffer_load_dwordx4 v[196:199], v2, s[40:43], 0 offen offset:1024\n"
  "buffer_load_dwordx4 v[160:163], v2, s[36:39], s34 offen\n"
  "buffer_load_dwordx4 v[164:167], v2, s[36:39], s34 offen offset:1024\n"
  "v_lshrrev_b32 v13, 3, v1\n"
  "v_mul_u32_u24 v13, 3584, v13\n"
  "v_lshrrev_b32 v14, 2, v1\n"
  "v_and_b32 v14, 4, v14\n"
  "v_and_b32 v15, 7, v1\n"
  "v_xor_b32 v14, v14, v15\n"
  "v_lshl_add_u32 v9, v14, 4, v13\n"
  "v_add_u32 v10, 27648, v9\n"
  "v_lshrrev_b32 v13, 3, v1\n"
  "v_mul_u32_u24 v13, 4096, v13\n"
  "v_lshrrev_b32 v14, 2, v1\n"
  "v_and_b32 v14, 4, v14\n"
  "v_and_b32 v15, 7, v1\n"
  "v_xor_b32 v14, v14, v15\n"
  "v_lshl_add_u32 v36, v14, 4, v13\n"
  "v_add_u32 v37, 31744, v36\n"
  "s_mul_i32 s95, s48, 1835008\n"
  "s_lshl_b32 s53, s86, 7\n"
  "s_add_u32 s95, s95, s53\n"
  "s_mul_i32 s20, s48, 1835008\n"
  "s_lshl_b32 s53, s88, 7\n"
  "s_add_u32 s20, s20, s53\n"
  "s_lshl_b32 s53, s61, 2\n"
  "s_add_u32 s53, s53, s48\n"
  "s_lshl_b32 s53, s53, 4\n"
  "s_add_u32 s53, s53, s60\n"
  "s_lshl_b32 s53, s53, 11\n"
  "s_add_u32 s56, s53, 6291456\n"
  "buffer_load_dwordx4 v[168:171], v2, s[36:39], s35 offen\n"
  "buffer_load_dwordx4 v[172:175], v2, s[36:39], s35 offen offset:1024\n"
  "buffer_load_dwordx4 v[200:203], v2, s[40:43], 0 offen offset:2048\n"
  "v_cmp_gt_u32 s[26:27], 32, v1\n"
  "s_lshl_b32 s93, s59, 1\n"
  "s_add_u32 s93, s93, s91\n"
  "s_lshl_b32 s94, s84, 1\n"
  "s_add_u32 s94, s94, s92\n"
  "v_and_b32 v13, 31, v1\n"
  "v_lshrrev_b32 v14, 5, v1\n"
  "v_mul_u32_u24 v15, 896, v13\n"
  "v_lshl_add_u32 v32, v14, 4, v15\n"
  "v_lshlrev_b32 v40, 4, v14\n"
  "v_lshlrev_b32 v41, 1, v13\n"
  "v_lshlrev_b32 v13, 3, v13\n"
  "v_lshl_add_u32 v34, v14, 2, v13\n"
  "v_mul_u32_u24 v14, 1792, v14\n"
  "v_add_u32 v35, v13, v14\n"
  "s_mul_i32 s53, s94, 14\n"
  "s_lshl_b32 s53, s53, 8\n"
  "s_add_u32 s22, s53, 10223616\n"
  "s_lshl_b32 s53, s93, 6\n"
  "s_add_u32 s53, s53, s94\n"
  "s_lshl_b32 s57, s53, 11\n"
  "buffer_load_dwordx4 v[176:179], v2, s[36:39], s62 offen\n"
  "buffer_load_dwordx4 v[180:183], v2, s[36:39], s62 offen offset:1024\n"
  "buffer_load_dwordx4 v[204:207], v2, s[40:43], 0 offen offset:3072\n"
  "v_and_b32 v13, 31, v1\n"
  "v_lshrrev_b32 v14, 5, v1\n"
  "v_bfe_u32 v15, v13, 1, 2\n"
  "v_xor_b32 v14, v14, v15\n"
  "v_lshlrev_b32 v14, 4, v14\n"
  "v_lshl_add_u32 v13, v13, 6, v14\n"
  "s_add_u32 s53, s51, 38912\n"
  "v_add_u32 v42, s53, v13\n"
  "v_xor_b32 v43, 32, v42\n"
  "v_lshrrev_b32 v13, 5, v1\n"
  "v_bfe_u32 v14, v1, 2, 2\n"
  "v_lshl_add_u32 v13, v13, 3, v14\n"
  "v_bfe_u32 v14, v1, 4, 1\n"
  "v_and_b32 v15, 1, v1\n"
  "v_lshl_add_u32 v14, v14, 1, v15\n"
  "v_bfe_u32 v15, v13, 1, 1\n"
  "v_xor_b32 v14, v14, v15\n"
  "v_lshlrev_b32 v13, 6, v13\n"
  "v_lshl_add_u32 v13, v14, 4, v13\n"
  "v_bfe_u32 v14, v1, 1, 1\n"
  "v_lshl_add_u32 v13, v14, 3, v13\n"
  "v_add_u32 v44, s53, v13\n"
  "v_xor_b32 v45, 32, v44\n"
  "s_mul_i32 s53, s94, 28672\n"
  "s_lshl_b32 s54, s93, 6\n"
  "s_add_u32 s21, s53, s54\n"
  "s_lshl_b32 s53, s86, 7\n"
  "s_lshl_b32 s54, s91, 6\n"
  "s_add_u32 s23, s53, s54\n"
  "s_lshl_b32 s53, s88, 1\n"
  "s_add_u32 s53, s53, s91\n"
  "s_lshl_b32 s24, s53, 6\n"
  "s_mov_b64 s[44:45], s[8:9]\n"
  "s_mov_b32 s46, 7340032\n"
  "s_mov_b32 s47, 0x20000\n"
  "s_cmp_eq_u32 s89, 0\n"
  "s_cselect_b32 s46, 0, s46\n"
  "s_mov_b64 s[96:97], s[4:5]\n"
  "s_mov_b32 s98, 1835008\n"
  "s_mov_b32 s99, 0x20000\n"
  "buffer_load_dwordx4 v[184:187], v2, s[36:39], s25 offen\n"
  "buffer_load_dwordx4 v[188:191], v2, s[36:39], s25 offen offset:1024\n"
  "buffer_load_dwordx4 v[16:19], v2, s[40:43], s65 offen offset:0\n"
  "buffer_load_dwordx4 v[20:23], v2, s[40:43], s65 offen offset:1024\n"
  "s_waitcnt vmcnt(10)\n"
  "v_mfma_f32_32x32x16_bf16 v[128:143], v[192:195], v[160:163], 0\n"
  "v_mfma_f32_32x32x16_bf16 v[128:143], v[196:199], v[164:167], v[128:143]\n"
  "s_waitcnt vmcnt(8)\n"
  "v_mfma_f32_32x32x16_bf16 v[144:159], v[192:195], v[168:171], 0\n"
  "s_nop 9\n"
  "v_exp_f32 v48, v128\n"
  "v_exp_f32 v49, v129\n"
  "v_exp_f32 v50, v130\n"
  "v_exp_f32 v51, v131\n"
  "buffer_load_dwordx4 v[24:27], v2, s[40:43], s65 offen offset:2048\n"
  "v_cvt_pk_bf16_f32 v56, v48, v49\n"
  "v_exp_f32 v52, v132\n"
  "v_exp_f32 v53, v133\n"
  "v_cvt_pk_bf16_f32 v57, v50, v51\n"
  "v_exp_f32 v54, v134\n"
  "v_exp_f32 v55, v135\n"
  "v_mfma_f32_32x32x16_bf16 v[144:159], v[196:199], v[172:175], v[144:159]\n"
  "v_cvt_pk_bf16_f32 v58, v52, v53\n"
  "v_exp_f32 v48, v136\n"
  "v_exp_f32 v49, v137\n"
  "v_cvt_pk_bf16_f32 v59, v54, v55\n"
  "v_exp_f32 v50, v138\n"
  "v_exp_f32 v51, v139\n"
  "s_waitcnt vmcnt(8)\n"
  "v_mfma_f32_32x32x16_bf16 v[64:79], v[200:203], v[56:59], 0\n"
  "buffer_load_dwordx4 v[28:31], v2, s[40:43], s65 offen offset:3072\n"
  "v_cvt_pk_bf16_f32 v60, v48, v49\n"
  "v_exp_f32 v52, v140\n"
  "v_exp_f32 v53, v141\n"
  "v_cvt_pk_bf16_f32 v61, v50, v51\n"
  "v_exp_f32 v54, v142\n"
  "v_exp_f32 v55, v143\n"
  "s_waitcnt vmcnt(7)\n"
  "v_mfma_f32_32x32x16_bf16 v[128:143], v[192:195], v[176:179], 0\n"
  "v_cvt_pk_bf16_f32 v62, v52, v53\n"
  "v_exp_f32 v48, v144\n"
  "v_exp_f32 v49, v145\n"
  "v_cvt_pk_bf16_f32 v63, v54, v55\n"
  "v_exp_f32 v50, v146\n"
  "v_exp_f32 v51, v147\n"
  "s_waitcnt vmcnt(6)\n"
  "v_mfma_f32_32x32x16_bf16 v[64:79], v[204:207], v[60:63], v[64:79]\n"
  "buffer_load_dwordx4 v[208:211], v2, s[40:43], s66 offen offset:0\n"
  "v_cvt_pk_bf16_f32 v56, v48, v49\n"
  "v_exp_f32 v52, v148\n"
  "v_exp_f32 v53, v149\n"
  "v_cvt_pk_bf16_f32 v57, v50, v51\n"
  "v_exp_f32 v54, v150\n"
  "v_exp_f32 v55, v151\n"
  "v_mfma_f32_32x32x16_bf16 v[128:143], v[196:199], v[180:183], v[128:143]\n"
  "v_cvt_pk_bf16_f32 v58, v52, v53\n"
  "v_exp_f32 v48, v152\n"
  "v_exp_f32 v49, v153\n"
  "v_cvt_pk_bf16_f32 v59, v54, v55\n"
  "v_exp_f32 v50, v154\n"
  "v_exp_f32 v51, v155\n"
  "v_mfma_f32_32x32x16_bf16 v[80:95], v[200:203], v[56:59], 0\n"
  "buffer_load_dwordx4 v[212:215], v2, s[40:43], s66 offen offset:1024\n"
  "v_cvt_pk_bf16_f32 v60, v48, v49\n"
  "v_exp_f32 v52, v156\n"
  "v_exp_f32 v53, v157\n"
  "v_cvt_pk_bf16_f32 v61, v50, v51\n"
  "v_exp_f32 v54, v158\n"
  "v_exp_f32 v55, v159\n"
  "s_waitcnt vmcnt(6)\n"
  "v_mfma_f32_32x32x16_bf16 v[144:159], v[192:195], v[184:187], 0\n"
  "v_cvt_pk_bf16_f32 v62, v52, v53\n"
  "v_exp_f32 v48, v128\n"
  "v_exp_f32 v49, v129\n"
  "v_cvt_pk_bf16_f32 v63, v54, v55\n"
  "v_exp_f32 v50, v130\n"
  "v_exp_f32 v51, v131\n"
  "v_mfma_f32_32x32x16_bf16 v[80:95], v[204:207], v[60:63], v[80:95]\n"
  "buffer_load_dwordx4 v[216:219], v2, s[40:43], s66 offen offset:2048\n"
  "v_cvt_pk_bf16_f32 v56, v48, v49\n"
  "v_exp_f32 v52, v132\n"
  "v_exp_f32 v53, v133\n"
  "v_cvt_pk_bf16_f32 v57, v50, v51\n"
  "v_exp_f32 v54, v134\n"
  "v_exp_f32 v55, v135\n"
  "v_mfma_f32_32x32x16_bf16 v[144:159], v[196:199], v[188:191], v[144:159]\n"
  "v_cvt_pk_bf16_f32 v58, v52, v53\n"
  "v_exp_f32 v48, v136\n"
  "v_exp_f32 v49, v137\n"
  "v_cvt_pk_bf16_f32 v59, v54, v55\n"
  "v_exp_f32 v50, v138\n"
  "v_exp_f32 v51, v139\n"
  "v_mfma_f32_32x32x16_bf16 v[96:111], v[200:203], v[56:59], 0\n"
  "buffer_load_dwordx4 v[220:223], v2, s[40:43], s66 offen offset:3072\n"
  "v_cvt_pk_bf16_f32 v60, v48, v49\n"
  "v_exp_f32 v52, v140\n"
  "v_exp_f32 v53, v141\n"
  "v_cvt_pk_bf16_f32 v61, v50, v51\n"
  "v_exp_f32 v54, v142\n"
  "v_exp_f32 v55, v143\n"
  "s_waitcnt vmcnt(6)\n"
  "v_mfma_f32_32x32x16_bf16 v[128:143], v[16:19], v[160:163], 0\n"
  "v_cvt_pk_bf16_f32 v62, v52, v53\n"
  "v_exp_f32 v48, v144\n"
  "v_exp_f32 v49, v145\n"
  "v_cvt_pk_bf16_f32 v63, v54, v55\n"
  "v_exp_f32 v50, v146\n"
  "v_exp_f32 v51, v147\n"
  "v_mfma_f32_32x32x16_bf16 v[96:111], v[204:207], v[60:63], v[96:111]\n"
  "buffer_load_dwordx4 v[192:195], v2, s[40:43], s69 offen offset:0\n"
  "v_cvt_pk_bf16_f32 v56, v48, v49\n"
  "v_exp_f32 v52, v148\n"
  "v_exp_f32 v53, v149\n"
  "v_cvt_pk_bf16_f32 v57, v50, v51\n"
  "v_exp_f32 v54, v150\n"
  "v_exp_f32 v55, v151\n"
  "v_mfma_f32_32x32x16_bf16 v[128:143], v[20:23], v[164:167], v[128:143]\n"
  "buffer_load_dwordx4 v[224:227], v2, s[40:43], s67 offen offset:0\n"
  "v_cvt_pk_bf16_f32 v58, v52, v53\n"
  "v_exp_f32 v48, v152\n"
  "v_exp_f32 v49, v153\n"
  "v_cvt_pk_bf16_f32 v59, v54, v55\n"
  "v_exp_f32 v50, v154\n"
  "v_exp_f32 v51, v155\n"
  "v_mfma_f32_32x32x16_bf16 v[112:127], v[200:203], v[56:59], 0\n"
  "buffer_load_dwordx4 v[228:231], v2, s[40:43], s67 offen offset:1024\n"
  "v_cvt_pk_bf16_f32 v60, v48, v49\n"
  "v_exp_f32 v52, v156\n"
  "v_exp_f32 v53, v157\n"
  "v_cvt_pk_bf16_f32 v61, v50, v51\n"
  "v_exp_f32 v54, v158\n"
  "v_exp_f32 v55, v159\n"
  "v_mfma_f32_32x32x16_bf16 v[144:159], v[16:19], v[168:171], 0\n"
  "v_cvt_pk_bf16_f32 v62, v52, v53\n"
  "v_exp_f32 v48, v128\n"
  "v_exp_f32 v49, v129\n"
  "v_cvt_pk_bf16_f32 v63, v54, v55\n"
  "v_exp_f32 v50, v130\n"
  "v_exp_f32 v51, v131\n"
  "v_mfma_f32_32x32x16_bf16 v[112:127], v[204:207], v[60:63], v[112:127]\n"
  "buffer_load_dwordx4 v[196:199], v2, s[40:43], s69 offen offset:1024\n"
  "v_cvt_pk_bf16_f32 v56, v48, v49\n"
  "v_exp_f32 v52, v132\n"
  "v_exp_f32 v53, v133\n"
  "v_cvt_pk_bf16_f32 v57, v50, v51\n"
  "v_exp_f32 v54, v134\n"
  "v_exp_f32 v55, v135\n"
  "v_mfma_f32_32x32x16_bf16 v[144:159], v[20:23], v[172:175], v[144:159]\n"
  "buffer_load_dwordx4 v[232:235], v2, s[40:43], s67 offen offset:2048\n"
  "v_cvt_pk_bf16_f32 v58, v52, v53\n"
  "v_exp_f32 v48, v136\n"
  "v_exp_f32 v49, v137\n"
  "v_cvt_pk_bf16_f32 v59, v54, v55\n"
  "v_exp_f32 v50, v138\n"
  "v_exp_f32 v51, v139\n"
  "s_waitcnt vmcnt(9)\n"
  "v_mfma_f32_32x32x16_bf16 v[64:79], v[24:27], v[56:59], v[64:79]\n"
  "buffer_load_dwordx4 v[236:239], v2, s[40:43], s67 offen offset:3072\n"
  "v_cvt_pk_bf16_f32 v60, v48, v49\n"
  "v_exp_f32 v52, v140\n"
  "v_exp_f32 v53, v141\n"
  "v_cvt_pk_bf16_f32 v61, v50, v51\n"
  "v_exp_f32 v54, v142\n"
  "v_exp_f32 v55, v143\n"
  "v_mfma_f32_32x32x16_bf16 v[128:143], v[16:19], v[176:179], 0\n"
  "v_cvt_pk_bf16_f32 v62, v52, v53\n"
  "v_exp_f32 v48, v144\n"
  "v_exp_f32 v49, v145\n"
  "v_cvt_pk_bf16_f32 v63, v54, v55\n"
  "v_exp_f32 v50, v146\n"
  "v_exp_f32 v51, v147\n"
  "v_mfma_f32_32x32x16_bf16 v[64:79], v[28:31], v[60:63], v[64:79]\n"
  "buffer_load_dwordx4 v[200:203], v2, s[40:43], s69 offen offset:2048\n"
  "v_cvt_pk_bf16_f32 v56, v48, v49\n"
  "v_exp_f32 v52, v148\n"
  "v_exp_f32 v53, v149\n"
  "v_cvt_pk_bf16_f32 v57, v50, v51\n"
  "v_exp_f32 v54, v150\n"
  "v_exp_f32 v55, v151\n"
  "v_mfma_f32_32x32x16_bf16 v[128:143], v[20:23], v[180:183], v[128:143]\n"
  "buffer_load_dwordx4 v[240:243], v2, s[40:43], s68 offen offset:0\n"
  "v_cvt_pk_bf16_f32 v58, v52, v53\n"
  "v_exp_f32 v48, v152\n"
  "v_exp_f32 v49, v153\n"
  "v_cvt_pk_bf16_f32 v59, v54, v55\n"
  "v_exp_f32 v50, v154\n"
  "v_exp_f32 v51, v155\n"
  "v_mfma_f32_32x32x16_bf16 v[80:95], v[24:27], v[56:59], v[80:95]\n"
  "buffer_load_dwordx4 v[244:247], v2, s[40:43], s68 offen offset:1024\n"
  "v_cvt_pk_bf16_f32 v60, v48, v49\n"
  "v_exp_f32 v52, v156\n"
  "v_exp_f32 v53, v157\n"
  "v_cvt_pk_bf16_f32 v61, v50, v51\n"
  "v_exp_f32 v54, v158\n"
  "v_exp_f32 v55, v159\n"
  "v_mfma_f32_32x32x16_bf16 v[144:159], v[16:19], v[184:187], 0\n"
  "v_cvt_pk_bf16_f32 v62, v52, v53\n"
  "v_exp_f32 v48, v128\n"
  "v_exp_f32 v49, v129\n"
  "v_cvt_pk_bf16_f32 v63, v54, v55\n"
  "v_exp_f32 v50, v130\n"
  "v_exp_f32 v51, v131\n"
  "v_mfma_f32_32x32x16_bf16 v[80:95], v[28:31], v[60:63], v[80:95]\n"
  "buffer_load_dwordx4 v[204:207], v2, s[40:43], s69 offen offset:3072\n"
  "v_cvt_pk_bf16_f32 v56, v48, v49\n"
  "v_exp_f32 v52, v132\n"
  "v_exp_f32 v53, v133\n"
  "v_cvt_pk_bf16_f32 v57, v50, v51\n"
  "v_exp_f32 v54, v134\n"
  "v_exp_f32 v55, v135\n"
  "v_mfma_f32_32x32x16_bf16 v[144:159], v[20:23], v[188:191], v[144:159]\n"
  "buffer_load_dwordx4 v[248:251], v2, s[40:43], s68 offen offset:2048\n"
  "v_cvt_pk_bf16_f32 v58, v52, v53\n"
  "v_exp_f32 v48, v136\n"
  "v_exp_f32 v49, v137\n"
  "v_cvt_pk_bf16_f32 v59, v54, v55\n"
  "v_exp_f32 v50, v138\n"
  "v_exp_f32 v51, v139\n"
  "v_mfma_f32_32x32x16_bf16 v[96:111], v[24:27], v[56:59], v[96:111]\n"
  "v_cvt_pk_bf16_f32 v60, v48, v49\n"
  "v_exp_f32 v52, v140\n"
  "v_exp_f32 v53, v141\n"
  "v_cvt_pk_bf16_f32 v61, v50, v51\n"
  "v_exp_f32 v54, v142\n"
  "v_exp_f32 v55, v143\n"
  "s_waitcnt vmcnt(13)\n"
  "v_mfma_f32_32x32x16_bf16 v[128:143], v[208:211], v[160:163], 0\n"
  "v_cvt_pk_bf16_f32 v62, v52, v53\n"
  "v_exp_f32 v48, v144\n"
  "v_exp_f32 v49, v145\n"
  "v_cvt_pk_bf16_f32 v63, v54, v55\n"
  "v_exp_f32 v50, v146\n"
  "v_exp_f32 v51, v147\n"
  "v_mfma_f32_32x32x16_bf16 v[96:111], v[28:31], v[60:63], v[96:111]\n"
  "buffer_load_dwordx4 v[16:19], v2, s[40:43], s70 offen offset:0\n"
  "v_cvt_pk_bf16_f32 v56, v48, v49\n"
  "v_exp_f32 v52, v148\n"
  "v_exp_f32 v53, v149\n"
  "v_cvt_pk_bf16_f32 v57, v50, v51\n"
  "v_exp_f32 v54, v150\n"
  "v_exp_f32 v55, v151\n"
  "v_mfma_f32_32x32x16_bf16 v[128:143], v[212:215], v[164:167], v[128:143]\n"
  "buffer_load_dwordx4 v[252:255], v2, s[40:43], s68 offen offset:3072\n"
  "v_cvt_pk_bf16_f32 v58, v52, v53\n"
  "v_exp_f32 v48, v152\n"
  "v_exp_f32 v49, v153\n"
  "v_cvt_pk_bf16_f32 v59, v54, v55\n"
  "v_exp_f32 v50, v154\n"
  "v_exp_f32 v51, v155\n"
  "v_mfma_f32_32x32x16_bf16 v[112:127], v[24:27], v[56:59], v[112:127]\n"
  "v_cvt_pk_bf16_f32 v60, v48, v49\n"
  "v_exp_f32 v52, v156\n"
  "v_exp_f32 v53, v157\n"
  "v_cvt_pk_bf16_f32 v61, v50, v51\n"
  "v_exp_f32 v54, v158\n"
  "v_exp_f32 v55, v159\n"
  "v_mfma_f32_32x32x16_bf16 v[144:159], v[208:211], v[168:171], 0\n"
  "v_cvt_pk_bf16_f32 v62, v52, v53\n"
  "v_exp_f32 v48, v128\n"
  "v_exp_f32 v49, v129\n"
  "v_cvt_pk_bf16_f32 v63, v54, v55\n"
  "v_exp_f32 v50, v130\n"
  "v_exp_f32 v51, v131\n"
  "v_mfma_f32_32x32x16_bf16 v[112:127], v[28:31], v[60:63], v[112:127]\n"
  "buffer_load_dwordx4 v[20:23], v2, s[40:43], s70 offen offset:1024\n"
  "v_cvt_pk_bf16_f32 v56, v48, v49\n"
  "v_exp_f32 v52, v132\n"
  "v_exp_f32 v53, v133\n"
  "v_cvt_pk_bf16_f32 v57, v50, v51\n"
  "v_exp_f32 v54, v134\n"
  "v_exp_f32 v55, v135\n"
  "v_mfma_f32_32x32x16_bf16 v[144:159], v[212:215], v[172:175], v[144:159]\n"
  "v_cvt_pk_bf16_f32 v58, v52, v53\n"
  "v_exp_f32 v48, v136\n"
  "v_exp_f32 v49, v137\n"
  "v_cvt_pk_bf16_f32 v59, v54, v55\n"
  "v_exp_f32 v50, v138\n"
  "v_exp_f32 v51, v139\n"
  "s_waitcnt vmcnt(14)\n"
  "v_mfma_f32_32x32x16_bf16 v[64:79], v[216:219], v[56:59], v[64:79]\n"
  "v_cvt_pk_bf16_f32 v60, v48, v49\n"
  "v_exp_f32 v52, v140\n"
  "v_exp_f32 v53, v141\n"
  "v_cvt_pk_bf16_f32 v61, v50, v51\n"
  "v_exp_f32 v54, v142\n"
  "v_exp_f32 v55, v143\n"
  "v_mfma_f32_32x32x16_bf16 v[128:143], v[208:211], v[176:179], 0\n"
  "v_cvt_pk_bf16_f32 v62, v52, v53\n"
  "v_exp_f32 v48, v144\n"
  "v_exp_f32 v49, v145\n"
  "v_cvt_pk_bf16_f32 v63, v54, v55\n"
  "v_exp_f32 v50, v146\n"
  "v_exp_f32 v51, v147\n"
  "v_mfma_f32_32x32x16_bf16 v[64:79], v[220:223], v[60:63], v[64:79]\n"
  "buffer_load_dwordx4 v[24:27], v2, s[40:43], s70 offen offset:2048\n"
  "v_cvt_pk_bf16_f32 v56, v48, v49\n"
  "v_exp_f32 v52, v148\n"
  "v_exp_f32 v53, v149\n"
  "v_cvt_pk_bf16_f32 v57, v50, v51\n"
  "v_exp_f32 v54, v150\n"
  "v_exp_f32 v55, v151\n"
  "v_mfma_f32_32x32x16_bf16 v[128:143], v[212:215], v[180:183], v[128:143]\n"
  "v_cvt_pk_bf16_f32 v58, v52, v53\n"
  "v_exp_f32 v48, v152\n"
  "v_exp_f32 v49, v153\n"
  "v_cvt_pk_bf16_f32 v59, v54, v55\n"
  "v_exp_f32 v50, v154\n"
  "v_exp_f32 v51, v155\n"
  "v_mfma_f32_32x32x16_bf16 v[80:95], v[216:219], v[56:59], v[80:95]\n"
  "v_cvt_pk_bf16_f32 v60, v48, v49\n"
  "v_exp_f32 v52, v156\n"
  "v_exp_f32 v53, v157\n"
  "v_cvt_pk_bf16_f32 v61, v50, v51\n"
  "v_exp_f32 v54, v158\n"
  "v_exp_f32 v55, v159\n"
  "v_mfma_f32_32x32x16_bf16 v[144:159], v[208:211], v[184:187], 0\n"
  "v_cvt_pk_bf16_f32 v62, v52, v53\n"
  "v_exp_f32 v48, v128\n"
  "v_exp_f32 v49, v129\n"
  "v_cvt_pk_bf16_f32 v63, v54, v55\n"
  "v_exp_f32 v50, v130\n"
  "v_exp_f32 v51, v131\n"
  "v_mfma_f32_32x32x16_bf16 v[80:95], v[220:223], v[60:63], v[80:95]\n"
  "buffer_load_dwordx4 v[28:31], v2, s[40:43], s70 offen offset:3072\n"
  "v_cvt_pk_bf16_f32 v56, v48, v49\n"
  "v_exp_f32 v52, v132\n"
  "v_exp_f32 v53, v133\n"
  "v_cvt_pk_bf16_f32 v57, v50, v51\n"
  "v_exp_f32 v54, v134\n"
  "v_exp_f32 v55, v135\n"
  "v_mfma_f32_32x32x16_bf16 v[144:159], v[212:215], v[188:191], v[144:159]\n"
  "v_cvt_pk_bf16_f32 v58, v52, v53\n"
  "v_exp_f32 v48, v136\n"
  "v_exp_f32 v49, v137\n"
  "v_cvt_pk_bf16_f32 v59, v54, v55\n"
  "v_exp_f32 v50, v138\n"
  "v_exp_f32 v51, v139\n"
  "v_mfma_f32_32x32x16_bf16 v[96:111], v[216:219], v[56:59], v[96:111]\n"
  "v_cvt_pk_bf16_f32 v60, v48, v49\n"
  "v_exp_f32 v52, v140\n"
  "v_exp_f32 v53, v141\n"
  "v_cvt_pk_bf16_f32 v61, v50, v51\n"
  "v_exp_f32 v54, v142\n"
  "v_exp_f32 v55, v143\n"
  "s_waitcnt vmcnt(13)\n"
  "v_mfma_f32_32x32x16_bf16 v[128:143], v[224:227], v[160:163], 0\n"
  "v_cvt_pk_bf16_f32 v62, v52, v53\n"
  "v_exp_f32 v48, v144\n"
  "v_exp_f32 v49, v145\n"
  "v_cvt_pk_bf16_f32 v63, v54, v55\n"
  "v_exp_f32 v50, v146\n"
  "v_exp_f32 v51, v147\n"
  "v_mfma_f32_32x32x16_bf16 v[96:111], v[220:223], v[60:63], v[96:111]\n"
  "buffer_load_dwordx4 v[208:211], v2, s[40:43], s71 offen offset:0\n"
  "v_cvt_pk_bf16_f32 v56, v48, v49\n"
  "v_exp_f32 v52, v148\n"
  "v_exp_f32 v53, v149\n"
  "v_cvt_pk_bf16_f32 v57, v50, v51\n"
  "v_exp_f32 v54, v150\n"
  "v_exp_f32 v55, v151\n"
  "v_mfma_f32_32x32x16_bf16 v[128:143], v[228:231], v[164:167], v[128:143]\n"
  "v_cvt_pk_bf16_f32 v58, v52, v53\n"
  "v_exp_f32 v48, v152\n"
  "v_exp_f32 v49, v153\n"
  "v_cvt_pk_bf16_f32 v59, v54, v55\n"
  "v_exp_f32 v50, v154\n"
  "v_exp_f32 v51, v155\n"
  "v_mfma_f32_32x32x16_bf16 v[112:127], v[216:219], v[56:59], v[112:127]\n"
  "v_cvt_pk_bf16_f32 v60, v48, v49\n"
  "v_exp_f32 v52, v156\n"
  "v_exp_f32 v53, v157\n"
  "v_cvt_pk_bf16_f32 v61, v50, v51\n"
  "v_exp_f32 v54, v158\n"
  "v_exp_f32 v55, v159\n"
  "v_mfma_f32_32x32x16_bf16 v[144:159], v[224:227], v[168:171], 0\n"
  "v_cvt_pk_bf16_f32 v62, v52, v53\n"
  "v_exp_f32 v48, v128\n"
  "v_exp_f32 v49, v129\n"
  "v_cvt_pk_bf16_f32 v63, v54, v55\n"
  "v_exp_f32 v50, v130\n"
  "v_exp_f32 v51, v131\n"
  "v_mfma_f32_32x32x16_bf16 v[112:127], v[220:223], v[60:63], v[112:127]\n"
  "buffer_load_dwordx4 v[212:215], v2, s[40:43], s71 offen offset:1024\n"
  "v_cvt_pk_bf16_f32 v56, v48, v49\n"
  "v_exp_f32 v52, v132\n"
  "v_exp_f32 v53, v133\n"
  "v_cvt_pk_bf16_f32 v57, v50, v51\n"
  "v_exp_f32 v54, v134\n"
  "v_exp_f32 v55, v135\n"
  "v_mfma_f32_32x32x16_bf16 v[144:159], v[228:231], v[172:175], v[144:159]\n"
  "v_cvt_pk_bf16_f32 v58, v52, v53\n"
  "v_exp_f32 v48, v136\n"
  "v_exp_f32 v49, v137\n"
  "v_cvt_pk_bf16_f32 v59, v54, v55\n"
  "v_exp_f32 v50, v138\n"
  "v_exp_f32 v51, v139\n"
  "s_waitcnt vmcnt(12)\n"
  "v_mfma_f32_32x32x16_bf16 v[64:79], v[232:235], v[56:59], v[64:79]\n"
  "v_cvt_pk_bf16_f32 v60, v48, v49\n"
  "v_exp_f32 v52, v140\n"
  "v_exp_f32 v53, v141\n"
  "v_cvt_pk_bf16_f32 v61, v50, v51\n"
  "v_exp_f32 v54, v142\n"
  "v_exp_f32 v55, v143\n"
  "v_mfma_f32_32x32x16_bf16 v[128:143], v[224:227], v[176:179], 0\n"
  "v_cvt_pk_bf16_f32 v62, v52, v53\n"
  "v_exp_f32 v48, v144\n"
  "v_exp_f32 v49, v145\n"
  "v_cvt_pk_bf16_f32 v63, v54, v55\n"
  "v_exp_f32 v50, v146\n"
  "v_exp_f32 v51, v147\n"
  "v_mfma_f32_32x32x16_bf16 v[64:79], v[236:239], v[60:63], v[64:79]\n"
  "buffer_load_dwordx4 v[216:219], v2, s[40:43], s71 offen offset:2048\n"
  "v_cvt_pk_bf16_f32 v56, v48, v49\n"
  "v_exp_f32 v52, v148\n"
  "v_exp_f32 v53, v149\n"
  "v_cvt_pk_bf16_f32 v57, v50, v51\n"
  "v_exp_f32 v54, v150\n"
  "v_exp_f32 v55, v151\n"
  "v_mfma_f32_32x32x16_bf16 v[128:143], v[228:231], v[180:183], v[128:143]\n"
  "v_cvt_pk_bf16_f32 v58, v52, v53\n"
  "v_exp_f32 v48, v152\n"
  "v_exp_f32 v49, v153\n"
  "v_cvt_pk_bf16_f32 v59, v54, v55\n"
  "v_exp_f32 v50, v154\n"
  "v_exp_f32 v51, v155\n"
  "v_mfma_f32_32x32x16_bf16 v[80:95], v[232:235], v[56:59], v[80:95]\n"
  "v_cvt_pk_bf16_f32 v60, v48, v49\n"
  "v_exp_f32 v52, v156\n"
  "v_exp_f32 v53, v157\n"
  "v_cvt_pk_bf16_f32 v61, v50, v51\n"
  "v_exp_f32 v54, v158\n"
  "v_exp_f32 v55, v159\n"
  "v_mfma_f32_32x32x16_bf16 v[144:159], v[224:227], v[184:187], 0\n"
  "v_cvt_pk_bf16_f32 v62, v52, v53\n"
  "v_exp_f32 v48, v128\n"
  "v_exp_f32 v49, v129\n"
  "v_cvt_pk_bf16_f32 v63, v54, v55\n"
  "v_exp_f32 v50, v130\n"
  "v_exp_f32 v51, v131\n"
  "v_mfma_f32_32x32x16_bf16 v[80:95], v[236:239], v[60:63], v[80:95]\n"
  "buffer_load_dwordx4 v[220:223], v2, s[40:43], s71 offen offset:3072\n"
  "v_cvt_pk_bf16_f32 v56, v48, v49\n"
  "v_exp_f32 v52, v132\n"
  "v_exp_f32 v53, v133\n"
  "v_cvt_pk_bf16_f32 v57, v50, v51\n"
  "v_exp_f32 v54, v134\n"
  "v_exp_f32 v55, v135\n"
  "v_mfma_f32_32x32x16_bf16 v[144:159], v[228:231], v[188:191], v[144:159]\n"
  "v_cvt_pk_bf16_f32 v58, v52, v53\n"
  "v_exp_f32 v48, v136\n"
  "v_exp_f32 v49, v137\n"
  "v_cvt_pk_bf16_f32 v59, v54, v55\n"
  "v_exp_f32 v50, v138\n"
  "v_exp_f32 v51, v139\n"
  "v_mfma_f32_32x32x16_bf16 v[96:111], v[232:235], v[56:59], v[96:111]\n"
  "v_cvt_pk_bf16_f32 v60, v48, v49\n"
  "v_exp_f32 v52, v140\n"
  "v_exp_f32 v53, v141\n"
  "v_cvt_pk_bf16_f32 v61, v50, v51\n"
  "v_exp_f32 v54, v142\n"
  "v_exp_f32 v55, v143\n"
  "s_waitcnt vmcnt(11)\n"
  "v_mfma_f32_32x32x16_bf16 v[128:143], v[240:243], v[160:163], 0\n"
  "v_cvt_pk_bf16_f32 v62, v52, v53\n"
  "v_exp_f32 v48, v144\n"
  "v_exp_f32 v49, v145\n"
  "v_cvt_pk_bf16_f32 v63, v54, v55\n"
  "v_exp_f32 v50, v146\n"
  "v_exp_f32 v51, v147\n"
  "v_mfma_f32_32x32x16_bf16 v[96:111], v[236:239], v[60:63], v[96:111]\n"
  "buffer_load_dwordx4 v[224:227], v2, s[40:43], s72 offen offset:0\n"
  "v_cvt_pk_bf16_f32 v56, v48, v49\n"
  "v_exp_f32 v52, v148\n"
  "v_exp_f32 v53, v149\n"
  "v_cvt_pk_bf16_f32 v57, v50, v51\n"
  "v_exp_f32 v54, v150\n"
  "v_exp_f32 v55, v151\n"
  "v_mfma_f32_32x32x16_bf16 v[128:143], v[244:247], v[164:167], v[128:143]\n"
  "v_cvt_pk_bf16_f32 v58, v52, v53\n"
  "v_exp_f32 v48, v152\n"
  "v_exp_f32 v49, v153\n"
  "v_cvt_pk_bf16_f32 v59, v54, v55\n"
  "v_exp_f32 v50, v154\n"
  "v_exp_f32 v51, v155\n"
  "v_mfma_f32_32x32x16_bf16 v[112:127], v[232:235], v[56:59], v[112:127]\n"
  "v_cvt_pk_bf16_f32 v60, v48, v49\n"
  "v_exp_f32 v52, v156\n"
  "v_exp_f32 v53, v157\n"
  "v_cvt_pk_bf16_f32 v61, v50, v51\n"
  "v_exp_f32 v54, v158\n"
  "v_exp_f32 v55, v159\n"
  "v_mfma_f32_32x32x16_bf16 v[144:159], v[240:243], v[168:171], 0\n"
  "v_cvt_pk_bf16_f32 v62, v52, v53\n"
  "v_exp_f32 v48, v128\n"
  "v_exp_f32 v49, v129\n"
  "v_cvt_pk_bf16_f32 v63, v54, v55\n"
  "v_exp_f32 v50, v130\n"
  "v_exp_f32 v51, v131\n"
  "v_mfma_f32_32x32x16_bf16 v[112:127], v[236:239], v[60:63], v[112:127]\n"
  "buffer_load_dwordx4 v[228:231], v2, s[40:43], s72 offen offset:1024\n"
  "v_cvt_pk_bf16_f32 v56, v48, v49\n"
  "v_exp_f32 v52, v132\n"
  "v_exp_f32 v53, v133\n"
  "v_cvt_pk_bf16_f32 v57, v50, v51\n"
  "v_exp_f32 v54, v134\n"
  "v_exp_f32 v55, v135\n"
  "v_mfma_f32_32x32x16_bf16 v[144:159], v[244:247], v[172:175], v[144:159]\n"
  "v_cvt_pk_bf16_f32 v58, v52, v53\n"
  "v_exp_f32 v48, v136\n"
  "v_exp_f32 v49, v137\n"
  "v_cvt_pk_bf16_f32 v59, v54, v55\n"
  "v_exp_f32 v50, v138\n"
  "v_exp_f32 v51, v139\n"
  "s_waitcnt vmcnt(9)\n"
  "v_mfma_f32_32x32x16_bf16 v[64:79], v[248:251], v[56:59], v[64:79]\n"
  "v_cvt_pk_bf16_f32 v60, v48, v49\n"
  "v_exp_f32 v52, v140\n"
  "v_exp_f32 v53, v141\n"
  "v_cvt_pk_bf16_f32 v61, v50, v51\n"
  "v_exp_f32 v54, v142\n"
  "v_exp_f32 v55, v143\n"
  "v_mfma_f32_32x32x16_bf16 v[128:143], v[240:243], v[176:179], 0\n"
  "v_cvt_pk_bf16_f32 v62, v52, v53\n"
  "v_exp_f32 v48, v144\n"
  "v_exp_f32 v49, v145\n"
  "v_cvt_pk_bf16_f32 v63, v54, v55\n"
  "v_exp_f32 v50, v146\n"
  "v_exp_f32 v51, v147\n"
  "v_mfma_f32_32x32x16_bf16 v[64:79], v[252:255], v[60:63], v[64:79]\n"
  "buffer_load_dwordx4 v[232:235], v2, s[40:43], s72 offen offset:2048\n"
  "v_cvt_pk_bf16_f32 v56, v48, v49\n"
  "v_exp_f32 v52, v148\n"
  "v_exp_f32 v53, v149\n"
  "v_cvt_pk_bf16_f32 v57, v50, v51\n"
  "v_exp_f32 v54, v150\n"
  "v_exp_f32 v55, v151\n"
  "v_mfma_f32_32x32x16_bf16 v[128:143], v[244:247], v[180:183], v[128:143]\n"
  "v_cvt_pk_bf16_f32 v58, v52, v53\n"
  "v_exp_f32 v48, v152\n"
  "v_exp_f32 v49, v153\n"
  "v_cvt_pk_bf16_f32 v59, v54, v55\n"
  "v_exp_f32 v50, v154\n"
  "v_exp_f32 v51, v155\n"
  "v_mfma_f32_32x32x16_bf16 v[80:95], v[248:251], v[56:59], v[80:95]\n"
  "v_cvt_pk_bf16_f32 v60, v48, v49\n"
  "v_exp_f32 v52, v156\n"
  "v_exp_f32 v53, v157\n"
  "v_cvt_pk_bf16_f32 v61, v50, v51\n"
  "v_exp_f32 v54, v158\n"
  "v_exp_f32 v55, v159\n"
  "v_mfma_f32_32x32x16_bf16 v[144:159], v[240:243], v[184:187], 0\n"
  "v_cvt_pk_bf16_f32 v62, v52, v53\n"
  "v_exp_f32 v48, v128\n"
  "v_exp_f32 v49, v129\n"
  "v_cvt_pk_bf16_f32 v63, v54, v55\n"
  "v_exp_f32 v50, v130\n"
  "v_exp_f32 v51, v131\n"
  "v_mfma_f32_32x32x16_bf16 v[80:95], v[252:255], v[60:63], v[80:95]\n"
  "buffer_load_dwordx4 v[236:239], v2, s[40:43], s72 offen offset:3072\n"
  "v_cvt_pk_bf16_f32 v56, v48, v49\n"
  "v_exp_f32 v52, v132\n"
  "v_exp_f32 v53, v133\n"
  "v_cvt_pk_bf16_f32 v57, v50, v51\n"
  "v_exp_f32 v54, v134\n"
  "v_exp_f32 v55, v135\n"
  "v_mfma_f32_32x32x16_bf16 v[144:159], v[244:247], v[188:191], v[144:159]\n"
  "v_cvt_pk_bf16_f32 v58, v52, v53\n"
  "v_exp_f32 v48, v136\n"
  "v_exp_f32 v49, v137\n"
  "v_cvt_pk_bf16_f32 v59, v54, v55\n"
  "v_exp_f32 v50, v138\n"
  "v_exp_f32 v51, v139\n"
  "v_mfma_f32_32x32x16_bf16 v[96:111], v[248:251], v[56:59], v[96:111]\n"
  "v_cvt_pk_bf16_f32 v60, v48, v49\n"
  "v_exp_f32 v52, v140\n"
  "v_exp_f32 v53, v141\n"
  "v_cvt_pk_bf16_f32 v61, v50, v51\n"
  "v_exp_f32 v54, v142\n"
  "v_exp_f32 v55, v143\n"
  "v_mfma_f32_32x32x16_bf16 v[128:143], v[192:195], v[160:163], 0\n"
  "v_cvt_pk_bf16_f32 v62, v52, v53\n"
  "v_exp_f32 v48, v144\n"
  "v_exp_f32 v49, v145\n"
  "v_cvt_pk_bf16_f32 v63, v54, v55\n"
  "v_exp_f32 v50, v146\n"
  "v_exp_f32 v51, v147\n"
  "v_mfma_f32_32x32x16_bf16 v[96:111], v[252:255], v[60:63], v[96:111]\n"
  "buffer_load_dwordx4 v[240:243], v2, s[40:43], s73 offen offset:0\n"
  "v_cvt_pk_bf16_f32 v56, v48, v49\n"
  "v_exp_f32 v52, v148\n"
  "v_exp_f32 v53, v149\n"
  "v_cvt_pk_bf16_f32 v57, v50, v51\n"
  "v_exp_f32 v54, v150\n"
  "v_exp_f32 v55, v151\n"
  "v_mfma_f32_32x32x16_bf16 v[128:143], v[196:199], v[164:167], v[128:143]\n"
  "v_cvt_pk_bf16_f32 v58, v52, v53\n"
  "v_exp_f32 v48, v152\n"
  "v_exp_f32 v49, v153\n"
  "v_cvt_pk_bf16_f32 v59, v54, v55\n"
  "v_exp_f32 v50, v154\n"
  "v_exp_f32 v51, v155\n"
  "v_mfma_f32_32x32x16_bf16 v[112:127], v[248:251], v[56:59], v[112:127]\n"
  "v_cvt_pk_bf16_f32 v60, v48, v49\n"
  "v_exp_f32 v52, v156\n"
  "v_exp_f32 v53, v157\n"
  "v_cvt_pk_bf16_f32 v61, v50, v51\n"
  "v_exp_f32 v54, v158\n"
  "v_exp_f32 v55, v159\n"
  "v_mfma_f32_32x32x16_bf16 v[144:159], v[192:195], v[168:171], 0\n"
  "v_cvt_pk_bf16_f32 v62, v52, v53\n"
  "v_exp_f32 v48, v128\n"
  "v_exp_f32 v49, v129\n"
  "v_cvt_pk_bf16_f32 v63, v54, v55\n"
  "v_exp_f32 v50, v130\n"
  "v_exp_f32 v51, v131\n"
  "v_mfma_f32_32x32x16_bf16 v[112:127], v[252:255], v[60:63], v[112:127]\n"
  "buffer_load_dwordx4 v[244:247], v2, s[40:43], s73 offen offset:1024\n"
  "v_cvt_pk_bf16_f32 v56, v48, v49\n"
  "v_exp_f32 v52, v132\n"
  "v_exp_f32 v53, v133\n"
  "v_cvt_pk_bf16_f32 v57, v50, v51\n"
  "v_exp_f32 v54, v134\n"
  "v_exp_f32 v55, v135\n"
  "v_mfma_f32_32x32x16_bf16 v[144:159], v[196:199], v[172:175], v[144:159]\n"
  "v_cvt_pk_bf16_f32 v58, v52, v53\n"
  "v_exp_f32 v48, v136\n"
  "v_exp_f32 v49, v137\n"
  "v_cvt_pk_bf16_f32 v59, v54, v55\n"
  "v_exp_f32 v50, v138\n"
  "v_exp_f32 v51, v139\n"
  "v_mfma_f32_32x32x16_bf16 v[64:79], v[200:203], v[56:59], v[64:79]\n"
  "v_cvt_pk_bf16_f32 v60, v48, v49\n"
  "v_exp_f32 v52, v140\n"
  "v_exp_f32 v53, v141\n"
  "v_cvt_pk_bf16_f32 v61, v50, v51\n"
  "v_exp_f32 v54, v142\n"
  "v_exp_f32 v55, v143\n"
  "v_mfma_f32_32x32x16_bf16 v[128:143], v[192:195], v[176:179], 0\n"
  "v_cvt_pk_bf16_f32 v62, v52, v53\n"
  "v_exp_f32 v48, v144\n"
  "v_exp_f32 v49, v145\n"
  "v_cvt_pk_bf16_f32 v63, v54, v55\n"
  "v_exp_f32 v50, v146\n"
  "v_exp_f32 v51, v147\n"
  "v_mfma_f32_32x32x16_bf16 v[64:79], v[204:207], v[60:63], v[64:79]\n"
  "buffer_load_dwordx4 v[248:251], v2, s[40:43], s73 offen offset:2048\n"
  "v_cvt_pk_bf16_f32 v56, v48, v49\n"
  "v_exp_f32 v52, v148\n"
  "v_exp_f32 v53, v149\n"
  "v_cvt_pk_bf16_f32 v57, v50, v51\n"
  "v_exp_f32 v54, v150\n"
  "v_exp_f32 v55, v151\n"
  "v_mfma_f32_32x32x16_bf16 v[128:143], v[196:199], v[180:183], v[128:143]\n"
  "v_cvt_pk_bf16_f32 v58, v52, v53\n"
  "v_exp_f32 v48, v152\n"
  "v_exp_f32 v49, v153\n"
  "v_cvt_pk_bf16_f32 v59, v54, v55\n"
  "v_exp_f32 v50, v154\n"
  "v_exp_f32 v51, v155\n"
  "v_mfma_f32_32x32x16_bf16 v[80:95], v[200:203], v[56:59], v[80:95]\n"
  "v_cvt_pk_bf16_f32 v60, v48, v49\n"
  "v_exp_f32 v52, v156\n"
  "v_exp_f32 v53, v157\n"
  "v_cvt_pk_bf16_f32 v61, v50, v51\n"
  "v_exp_f32 v54, v158\n"
  "v_exp_f32 v55, v159\n"
  "v_mfma_f32_32x32x16_bf16 v[144:159], v[192:195], v[184:187], 0\n"
  "v_cvt_pk_bf16_f32 v62, v52, v53\n"
  "v_exp_f32 v48, v128\n"
  "v_exp_f32 v49, v129\n"
  "v_cvt_pk_bf16_f32 v63, v54, v55\n"
  "v_exp_f32 v50, v130\n"
  "v_exp_f32 v51, v131\n"
  "v_mfma_f32_32x32x16_bf16 v[80:95], v[204:207], v[60:63], v[80:95]\n"
  "buffer_load_dwordx4 v[252:255], v2, s[40:43], s73 offen offset:3072\n"
  "v_cvt_pk_bf16_f32 v56, v48, v49\n"
  "v_exp_f32 v52, v132\n"
  "v_exp_f32 v53, v133\n"
  "v_cvt_pk_bf16_f32 v57, v50, v51\n"
  "v_exp_f32 v54, v134\n"
  "v_exp_f32 v55, v135\n"
  "v_mfma_f32_32x32x16_bf16 v[144:159], v[196:199], v[188:191], v[144:159]\n"
  "v_cvt_pk_bf16_f32 v58, v52, v53\n"
  "v_exp_f32 v48, v136\n"
  "v_exp_f32 v49, v137\n"
  "v_cvt_pk_bf16_f32 v59, v54, v55\n"
  "v_exp_f32 v50, v138\n"
  "v_exp_f32 v51, v139\n"
  "v_mfma_f32_32x32x16_bf16 v[96:111], v[200:203], v[56:59], v[96:111]\n"
  "v_cvt_pk_bf16_f32 v60, v48, v49\n"
  "v_exp_f32 v52, v140\n"
  "v_exp_f32 v53, v141\n"
  "v_cvt_pk_bf16_f32 v61, v50, v51\n"
  "v_exp_f32 v54, v142\n"
  "v_exp_f32 v55, v143\n"
  "s_waitcnt vmcnt(14)\n"
  "v_mfma_f32_32x32x16_bf16 v[128:143], v[16:19], v[160:163], 0\n"
  "v_cvt_pk_bf16_f32 v62, v52, v53\n"
  "v_exp_f32 v48, v144\n"
  "v_exp_f32 v49, v145\n"
  "v_cvt_pk_bf16_f32 v63, v54, v55\n"
  "v_exp_f32 v50, v146\n"
  "v_exp_f32 v51, v147\n"
  "v_mfma_f32_32x32x16_bf16 v[96:111], v[204:207], v[60:63], v[96:111]\n"
  "buffer_load_dwordx4 v[192:195], v2, s[40:43], s74 offen offset:0\n"
  "v_cvt_pk_bf16_f32 v56, v48, v49\n"
  "v_exp_f32 v52, v148\n"
  "v_exp_f32 v53, v149\n"
  "v_cvt_pk_bf16_f32 v57, v50, v51\n"
  "v_exp_f32 v54, v150\n"
  "v_exp_f32 v55, v151\n"
  "v_mfma_f32_32x32x16_bf16 v[128:143], v[20:23], v[164:167], v[128:143]\n"
  "v_cvt_pk_bf16_f32 v58, v52, v53\n"
  "v_exp_f32 v48, v152\n"
  "v_exp_f32 v49, v153\n"
  "v_cvt_pk_bf16_f32 v59, v54, v55\n"
  "v_exp_f32 v50, v154\n"
  "v_exp_f32 v51, v155\n"
  "v_mfma_f32_32x32x16_bf16 v[112:127], v[200:203], v[56:59], v[112:127]\n"
  "v_cvt_pk_bf16_f32 v60, v48, v49\n"
  "v_exp_f32 v52, v156\n"
  "v_exp_f32 v53, v157\n"
  "v_cvt_pk_bf16_f32 v61, v50, v51\n"
  "v_exp_f32 v54, v158\n"
  "v_exp_f32 v55, v159\n"
  "v_mfma_f32_32x32x16_bf16 v[144:159], v[16:19], v[168:171], 0\n"
  "v_cvt_pk_bf16_f32 v62, v52, v53\n"
  "v_exp_f32 v48, v128\n"
  "v_exp_f32 v49, v129\n"
  "v_cvt_pk_bf16_f32 v63, v54, v55\n"
  "v_exp_f32 v50, v130\n"
  "v_exp_f32 v51, v131\n"
  "v_mfma_f32_32x32x16_bf16 v[112:127], v[204:207], v[60:63], v[112:127]\n"
  "buffer_load_dwordx4 v[196:199], v2, s[40:43], s74 offen offset:1024\n"
  "v_cvt_pk_bf16_f32 v56, v48, v49\n"
  "v_exp_f32 v52, v132\n"
  "v_exp_f32 v53, v133\n"
  "v_cvt_pk_bf16_f32 v57, v50, v51\n"
  "v_exp_f32 v54, v134\n"
  "v_exp_f32 v55, v135\n"
  "v_mfma_f32_32x32x16_bf16 v[144:159], v[20:23], v[172:175], v[144:159]\n"
  "v_cvt_pk_bf16_f32 v58, v52, v53\n"
  "v_exp_f32 v48, v136\n"
  "v_exp_f32 v49, v137\n"
  "v_cvt_pk_bf16_f32 v59, v54, v55\n"
  "v_exp_f32 v50, v138\n"
  "v_exp_f32 v51, v139\n"
  "s_waitcnt vmcnt(14)\n"
  "v_mfma_f32_32x32x16_bf16 v[64:79], v[24:27], v[56:59], v[64:79]\n"
  "v_cvt_pk_bf16_f32 v60, v48, v49\n"
  "v_exp_f32 v52, v140\n"
  "v_exp_f32 v53, v141\n"
  "v_cvt_pk_bf16_f32 v61, v50, v51\n"
  "v_exp_f32 v54, v142\n"
  "v_exp_f32 v55, v143\n"
  "v_mfma_f32_32x32x16_bf16 v[128:143], v[16:19], v[176:179], 0\n"
  "v_cvt_pk_bf16_f32 v62, v52, v53\n"
  "v_exp_f32 v48, v144\n"
  "v_exp_f32 v49, v145\n"
  "v_cvt_pk_bf16_f32 v63, v54, v55\n"
  "v_exp_f32 v50, v146\n"
  "v_exp_f32 v51, v147\n"
  "v_mfma_f32_32x32x16_bf16 v[64:79], v[28:31], v[60:63], v[64:79]\n"
  "buffer_load_dwordx4 v[200:203], v2, s[40:43], s74 offen offset:2048\n"
  "v_cvt_pk_bf16_f32 v56, v48, v49\n"
  "v_exp_f32 v52, v148\n"
  "v_exp_f32 v53, v149\n"
  "v_cvt_pk_bf16_f32 v57, v50, v51\n"
  "v_exp_f32 v54, v150\n"
  "v_exp_f32 v55, v151\n"
  "v_mfma_f32_32x32x16_bf16 v[128:143], v[20:23], v[180:183], v[128:143]\n"
  "v_cvt_pk_bf16_f32 v58, v52, v53\n"
  "v_exp_f32 v48, v152\n"
  "v_exp_f32 v49, v153\n"
  "v_cvt_pk_bf16_f32 v59, v54, v55\n"
  "v_exp_f32 v50, v154\n"
  "v_exp_f32 v51, v155\n"
  "v_mfma_f32_32x32x16_bf16 v[80:95], v[24:27], v[56:59], v[80:95]\n"
  "v_cvt_pk_bf16_f32 v60, v48, v49\n"
  "v_exp_f32 v52, v156\n"
  "v_exp_f32 v53, v157\n"
  "v_cvt_pk_bf16_f32 v61, v50, v51\n"
  "v_exp_f32 v54, v158\n"
  "v_exp_f32 v55, v159\n"
  "v_mfma_f32_32x32x16_bf16 v[144:159], v[16:19], v[184:187], 0\n"
  "v_cvt_pk_bf16_f32 v62, v52, v53\n"
  "v_exp_f32 v48, v128\n"
  "v_exp_f32 v49, v129\n"
  "v_cvt_pk_bf16_f32 v63, v54, v55\n"
  "v_exp_f32 v50, v130\n"
  "v_exp_f32 v51, v131\n"
  "v_mfma_f32_32x32x16_bf16 v[80:95], v[28:31], v[60:63], v[80:95]\n"
  "buffer_load_dwordx4 v[204:207], v2, s[40:43], s74 offen offset:3072\n"
  "v_cvt_pk_bf16_f32 v56, v48, v49\n"
  "v_exp_f32 v52, v132\n"
  "v_exp_f32 v53, v133\n"
  "v_cvt_pk_bf16_f32 v57, v50, v51\n"
  "v_exp_f32 v54, v134\n"
  "v_exp_f32 v55, v135\n"
  "v_mfma_f32_32x32x16_bf16 v[144:159], v[20:23], v[188:191], v[144:159]\n"
  "v_cvt_pk_bf16_f32 v58, v52, v53\n"
  "v_exp_f32 v48, v136\n"
  "v_exp_f32 v49, v137\n"
  "v_cvt_pk_bf16_f32 v59, v54, v55\n"
  "v_exp_f32 v50, v138\n"
  "v_exp_f32 v51, v139\n"
  "v_mfma_f32_32x32x16_bf16 v[96:111], v[24:27], v[56:59], v[96:111]\n"
  "v_cvt_pk_bf16_f32 v60, v48, v49\n"
  "v_exp_f32 v52, v140\n"
  "v_exp_f32 v53, v141\n"
  "v_cvt_pk_bf16_f32 v61, v50, v51\n"
  "v_exp_f32 v54, v142\n"
  "v_exp_f32 v55, v143\n"
  "s_waitcnt vmcnt(14)\n"
  "v_mfma_f32_32x32x16_bf16 v[128:143], v[208:211], v[160:163], 0\n"
  "v_cvt_pk_bf16_f32 v62, v52, v53\n"
  "v_exp_f32 v48, v144\n"
  "v_exp_f32 v49, v145\n"
  "v_cvt_pk_bf16_f32 v63, v54, v55\n"
  "v_exp_f32 v50, v146\n"
  "v_exp_f32 v51, v147\n"
  "v_mfma_f32_32x32x16_bf16 v[96:111], v[28:31], v[60:63], v[96:111]\n"
  "buffer_load_dwordx4 v[16:19], v2, s[40:43], s75 offen offset:0\n"
  "v_cvt_pk_bf16_f32 v56, v48, v49\n"
  "v_exp_f32 v52, v148\n"
  "v_exp_f32 v53, v149\n"
  "v_cvt_pk_bf16_f32 v57, v50, v51\n"
  "v_exp_f32 v54, v150\n"
  "v_exp_f32 v55, v151\n"
  "v_mfma_f32_32x32x16_bf16 v[128:143], v[212:215], v[164:167], v[128:143]\n"
  "v_cvt_pk_bf16_f32 v58, v52, v53\n"
  "v_exp_f32 v48, v152\n"
  "v_exp_f32 v49, v153\n"
  "v_cvt_pk_bf16_f32 v59, v54, v55\n"
  "v_exp_f32 v50, v154\n"
  "v_exp_f32 v51, v155\n"
  "v_mfma_f32_32x32x16_bf16 v[112:127], v[24:27], v[56:59], v[112:127]\n"
  "v_cvt_pk_bf16_f32 v60, v48, v49\n"
  "v_exp_f32 v52, v156\n"
  "v_exp_f32 v53, v157\n"
  "v_cvt_pk_bf16_f32 v61, v50, v51\n"
  "v_exp_f32 v54, v158\n"
  "v_exp_f32 v55, v159\n"
  "v_mfma_f32_32x32x16_bf16 v[144:159], v[208:211], v[168:171], 0\n"
  "v_cvt_pk_bf16_f32 v62, v52, v53\n"
  "v_exp_f32 v48, v128\n"
  "v_exp_f32 v49, v129\n"
  "v_cvt_pk_bf16_f32 v63, v54, v55\n"
  "v_exp_f32 v50, v130\n"
  "v_exp_f32 v51, v131\n"
  "v_mfma_f32_32x32x16_bf16 v[112:127], v[28:31], v[60:63], v[112:127]\n"
  "buffer_load_dwordx4 v[20:23], v2, s[40:43], s75 offen offset:1024\n"
  "v_cvt_pk_bf16_f32 v56, v48, v49\n"
  "v_exp_f32 v52, v132\n"
  "v_exp_f32 v53, v133\n"
  "v_cvt_pk_bf16_f32 v57, v50, v51\n"
  "v_exp_f32 v54, v134\n"
  "v_exp_f32 v55, v135\n"
  "v_mfma_f32_32x32x16_bf16 v[144:159], v[212:215], v[172:175], v[144:159]\n"
  "v_cvt_pk_bf16_f32 v58, v52, v53\n"
  "v_exp_f32 v48, v136\n"
  "v_exp_f32 v49, v137\n"
  "v_cvt_pk_bf16_f32 v59, v54, v55\n"
  "v_exp_f32 v50, v138\n"
  "v_exp_f32 v51, v139\n"
  "s_waitcnt vmcnt(14)\n"
  "v_mfma_f32_32x32x16_bf16 v[64:79], v[216:219], v[56:59], v[64:79]\n"
  "v_cvt_pk_bf16_f32 v60, v48, v49\n"
  "v_exp_f32 v52, v140\n"
  "v_exp_f32 v53, v141\n"
  "v_cvt_pk_bf16_f32 v61, v50, v51\n"
  "v_exp_f32 v54, v142\n"
  "v_exp_f32 v55, v143\n"
  "v_mfma_f32_32x32x16_bf16 v[128:143], v[208:211], v[176:179], 0\n"
  "v_cvt_pk_bf16_f32 v62, v52, v53\n"
  "v_exp_f32 v48, v144\n"
  "v_exp_f32 v49, v145\n"
  "v_cvt_pk_bf16_f32 v63, v54, v55\n"
  "v_exp_f32 v50, v146\n"
  "v_exp_f32 v51, v147\n"
  "v_mfma_f32_32x32x16_bf16 v[64:79], v[220:223], v[60:63], v[64:79]\n"
  "buffer_load_dwordx4 v[24:27], v2, s[40:43], s75 offen offset:2048\n"
  "v_cvt_pk_bf16_f32 v56, v48, v49\n"
  "v_exp_f32 v52, v148\n"
  "v_exp_f32 v53, v149\n"
  "v_cvt_pk_bf16_f32 v57, v50, v51\n"
  "v_exp_f32 v54, v150\n"
  "v_exp_f32 v55, v151\n"
  "v_mfma_f32_32x32x16_bf16 v[128:143], v[212:215], v[180:183], v[128:143]\n"
  "v_cvt_pk_bf16_f32 v58, v52, v53\n"
  "v_exp_f32 v48, v152\n"
  "v_exp_f32 v49, v153\n"
  "v_cvt_pk_bf16_f32 v59, v54, v55\n"
  "v_exp_f32 v50, v154\n"
  "v_exp_f32 v51, v155\n"
  "v_mfma_f32_32x32x16_bf16 v[80:95], v[216:219], v[56:59], v[80:95]\n"
  "v_cvt_pk_bf16_f32 v60, v48, v49\n"
  "v_exp_f32 v52, v156\n"
  "v_exp_f32 v53, v157\n"
  "v_cvt_pk_bf16_f32 v61, v50, v51\n"
  "v_exp_f32 v54, v158\n"
  "v_exp_f32 v55, v159\n"
  "v_mfma_f32_32x32x16_bf16 v[144:159], v[208:211], v[184:187], 0\n"
  "v_cvt_pk_bf16_f32 v62, v52, v53\n"
  "v_exp_f32 v48, v128\n"
  "v_exp_f32 v49, v129\n"
  "v_cvt_pk_bf16_f32 v63, v54, v55\n"
  "v_exp_f32 v50, v130\n"
  "v_exp_f32 v51, v131\n"
  "v_mfma_f32_32x32x16_bf16 v[80:95], v[220:223], v[60:63], v[80:95]\n"
  "buffer_load_dwordx4 v[28:31], v2, s[40:43], s75 offen offset:3072\n"
  "v_cvt_pk_bf16_f32 v56, v48, v49\n"
  "v_exp_f32 v52, v132\n"
  "v_exp_f32 v53, v133\n"
  "v_cvt_pk_bf16_f32 v57, v50, v51\n"
  "v_exp_f32 v54, v134\n"
  "v_exp_f32 v55, v135\n"
  "v_mfma_f32_32x32x16_bf16 v[144:159], v[212:215], v[188:191], v[144:159]\n"
  "v_cvt_pk_bf16_f32 v58, v52, v53\n"
  "v_exp_f32 v48, v136\n"
  "v_exp_f32 v49, v137\n"
  "v_cvt_pk_bf16_f32 v59, v54, v55\n"
  "v_exp_f32 v50, v138\n"
  "v_exp_f32 v51, v139\n"
  "v_mfma_f32_32x32x16_bf16 v[96:111], v[216:219], v[56:59], v[96:111]\n"
  "v_cvt_pk_bf16_f32 v60, v48, v49\n"
  "v_exp_f32 v52, v140\n"
  "v_exp_f32 v53, v141\n"
  "v_cvt_pk_bf16_f32 v61, v50, v51\n"
  "v_exp_f32 v54, v142\n"
  "v_exp_f32 v55, v143\n"
  "s_waitcnt vmcnt(14)\n"
  "v_mfma_f32_32x32x16_bf16 v[128:143], v[224:227], v[160:163], 0\n"
  "v_cvt_pk_bf16_f32 v62, v52, v53\n"
  "v_exp_f32 v48, v144\n"
  "v_exp_f32 v49, v145\n"
  "v_cvt_pk_bf16_f32 v63, v54, v55\n"
  "v_exp_f32 v50, v146\n"
  "v_exp_f32 v51, v147\n"
  "v_mfma_f32_32x32x16_bf16 v[96:111], v[220:223], v[60:63], v[96:111]\n"
  "buffer_load_dwordx4 v[208:211], v2, s[40:43], s76 offen offset:0\n"
  "v_cvt_pk_bf16_f32 v56, v48, v49\n"
  "v_exp_f32 v52, v148\n"
  "v_exp_f32 v53, v149\n"
  "v_cvt_pk_bf16_f32 v57, v50, v51\n"
  "v_exp_f32 v54, v150\n"
  "v_exp_f32 v55, v151\n"
  "v_mfma_f32_32x32x16_bf16 v[128:143], v[228:231], v[164:167], v[128:143]\n"
  "v_cvt_pk_bf16_f32 v58, v52, v53\n"
  "v_exp_f32 v48, v152\n"
  "v_exp_f32 v49, v153\n"
  "v_cvt_pk_bf16_f32 v59, v54, v55\n"
  "v_exp_f32 v50, v154\n"
  "v_exp_f32 v51, v155\n"
  "v_mfma_f32_32x32x16_bf16 v[112:127], v[216:219], v[56:59], v[112:127]\n"
  "v_cvt_pk_bf16_f32 v60, v48, v49\n"
  "v_exp_f32 v52, v156\n"
  "v_exp_f32 v53, v157\n"
  "v_cvt_pk_bf16_f32 v61, v50, v51\n"
  "v_exp_f32 v54, v158\n"
  "v_exp_f32 v55, v159\n"
  "v_mfma_f32_32x32x16_bf16 v[144:159], v[224:227], v[168:171], 0\n"
  "v_cvt_pk_bf16_f32 v62, v52, v53\n"
  "v_exp_f32 v48, v128\n"
  "v_exp_f32 v49, v129\n"
  "v_cvt_pk_bf16_f32 v63, v54, v55\n"
  "v_exp_f32 v50, v130\n"
  "v_exp_f32 v51, v131\n"
  "v_mfma_f32_32x32x16_bf16 v[112:127], v[220:223], v[60:63], v[112:127]\n"
  "buffer_load_dwordx4 v[212:215], v2, s[40:43], s76 offen offset:1024\n"
  "v_cvt_pk_bf16_f32 v56, v48, v49\n"
  "v_exp_f32 v52, v132\n"
  "v_exp_f32 v53, v133\n"
  "v_cvt_pk_bf16_f32 v57, v50, v51\n"
  "v_exp_f32 v54, v134\n"
  "v_exp_f32 v55, v135\n"
  "v_mfma_f32_32x32x16_bf16 v[144:159], v[228:231], v[172:175], v[144:159]\n"
  "v_cvt_pk_bf16_f32 v58, v52, v53\n"
  "v_exp_f32 v48, v136\n"
  "v_exp_f32 v49, v137\n"
  "v_cvt_pk_bf16_f32 v59, v54, v55\n"
  "v_exp_f32 v50, v138\n"
  "v_exp_f32 v51, v139\n"
  "s_waitcnt vmcnt(14)\n"
  "v_mfma_f32_32x32x16_bf16 v[64:79], v[232:235], v[56:59], v[64:79]\n"
  "v_cvt_pk_bf16_f32 v60, v48, v49\n"
  "v_exp_f32 v52, v140\n"
  "v_exp_f32 v53, v141\n"
  "v_cvt_pk_bf16_f32 v61, v50, v51\n"
  "v_exp_f32 v54, v142\n"
  "v_exp_f32 v55, v143\n"
  "v_mfma_f32_32x32x16_bf16 v[128:143], v[224:227], v[176:179], 0\n"
  "v_cvt_pk_bf16_f32 v62, v52, v53\n"
  "v_exp_f32 v48, v144\n"
  "v_exp_f32 v49, v145\n"
  "v_cvt_pk_bf16_f32 v63, v54, v55\n"
  "v_exp_f32 v50, v146\n"
  "v_exp_f32 v51, v147\n"
  "v_mfma_f32_32x32x16_bf16 v[64:79], v[236:239], v[60:63], v[64:79]\n"
  "buffer_load_dwordx4 v[216:219], v2, s[40:43], s76 offen offset:2048\n"
  "v_cvt_pk_bf16_f32 v56, v48, v49\n"
  "v_exp_f32 v52, v148\n"
  "v_exp_f32 v53, v149\n"
  "v_cvt_pk_bf16_f32 v57, v50, v51\n"
  "v_exp_f32 v54, v150\n"
  "v_exp_f32 v55, v151\n"
  "v_mfma_f32_32x32x16_bf16 v[128:143], v[228:231], v[180:183], v[128:143]\n"
  "v_cvt_pk_bf16_f32 v58, v52, v53\n"
  "v_exp_f32 v48, v152\n"
  "v_exp_f32 v49, v153\n"
  "v_cvt_pk_bf16_f32 v59, v54, v55\n"
  "v_exp_f32 v50, v154\n"
  "v_exp_f32 v51, v155\n"
  "v_mfma_f32_32x32x16_bf16 v[80:95], v[232:235], v[56:59], v[80:95]\n"
  "v_cvt_pk_bf16_f32 v60, v48, v49\n"
  "v_exp_f32 v52, v156\n"
  "v_exp_f32 v53, v157\n"
  "v_cvt_pk_bf16_f32 v61, v50, v51\n"
  "v_exp_f32 v54, v158\n"
  "v_exp_f32 v55, v159\n"
  "v_mfma_f32_32x32x16_bf16 v[144:159], v[224:227], v[184:187], 0\n"
  "v_cvt_pk_bf16_f32 v62, v52, v53\n"
  "v_exp_f32 v48, v128\n"
  "v_exp_f32 v49, v129\n"
  "v_cvt_pk_bf16_f32 v63, v54, v55\n"
  "v_exp_f32 v50, v130\n"
  "v_exp_f32 v51, v131\n"
  "v_mfma_f32_32x32x16_bf16 v[80:95], v[236:239], v[60:63], v[80:95]\n"
  "buffer_load_dwordx4 v[220:223], v2, s[40:43], s76 offen offset:3072\n"
  "v_cvt_pk_bf16_f32 v56, v48, v49\n"
  "v_exp_f32 v52, v132\n"
  "v_exp_f32 v53, v133\n"
  "v_cvt_pk_bf16_f32 v57, v50, v51\n"
  "v_exp_f32 v54, v134\n"
  "v_exp_f32 v55, v135\n"
  "v_mfma_f32_32x32x16_bf16 v[144:159], v[228:231], v[188:191], v[144:159]\n"
  "v_cvt_pk_bf16_f32 v58, v52, v53\n"
  "v_exp_f32 v48, v136\n"
  "v_exp_f32 v49, v137\n"
  "v_cvt_pk_bf16_f32 v59, v54, v55\n"
  "v_exp_f32 v50, v138\n"
  "v_exp_f32 v51, v139\n"
  "v_mfma_f32_32x32x16_bf16 v[96:111], v[232:235], v[56:59], v[96:111]\n"
  "v_cvt_pk_bf16_f32 v60, v48, v49\n"
  "v_exp_f32 v52, v140\n"
  "v_exp_f32 v53, v141\n"
  "v_cvt_pk_bf16_f32 v61, v50, v51\n"
  "v_exp_f32 v54, v142\n"
  "v_exp_f32 v55, v143\n"
  "s_waitcnt vmcnt(14)\n"
  "v_mfma_f32_32x32x16_bf16 v[128:143], v[240:243], v[160:163], 0\n"
  "v_cvt_pk_bf16_f32 v62, v52, v53\n"
  "v_exp_f32 v48, v144\n"
  "v_exp_f32 v49, v145\n"
  "v_cvt_pk_bf16_f32 v63, v54, v55\n"
  "v_exp_f32 v50, v146\n"
  "v_exp_f32 v51, v147\n"
  "v_mfma_f32_32x32x16_bf16 v[96:111], v[236:239], v[60:63], v[96:111]\n"
  "buffer_load_dwordx4 v[224:227], v2, s[40:43], s77 offen offset:0\n"
  "v_cvt_pk_bf16_f32 v56, v48, v49\n"
  "v_exp_f32 v52, v148\n"
  "v_exp_f32 v53, v149\n"
  "v_cvt_pk_bf16_f32 v57, v50, v51\n"
  "v_exp_f32 v54, v150\n"
  "v_exp_f32 v55, v151\n"
  "v_mfma_f32_32x32x16_bf16 v[128:143], v[244:247], v[164:167], v[128:143]\n"
  "v_cvt_pk_bf16_f32 v58, v52, v53\n"
  "v_exp_f32 v48, v152\n"
  "v_exp_f32 v49, v153\n"
  "v_cvt_pk_bf16_f32 v59, v54, v55\n"
  "v_exp_f32 v50, v154\n"
  "v_exp_f32 v51, v155\n"
  "v_mfma_f32_32x32x16_bf16 v[112:127], v[232:235], v[56:59], v[112:127]\n"
  "v_cvt_pk_bf16_f32 v60, v48, v49\n"
  "v_exp_f32 v52, v156\n"
  "v_exp_f32 v53, v157\n"
  "v_cvt_pk_bf16_f32 v61, v50, v51\n"
  "v_exp_f32 v54, v158\n"
  "v_exp_f32 v55, v159\n"
  "v_mfma_f32_32x32x16_bf16 v[144:159], v[240:243], v[168:171], 0\n"
  "v_cvt_pk_bf16_f32 v62, v52, v53\n"
  "v_exp_f32 v48, v128\n"
  "v_exp_f32 v49, v129\n"
  "v_cvt_pk_bf16_f32 v63, v54, v55\n"
  "v_exp_f32 v50, v130\n"
  "v_exp_f32 v51, v131\n"
  "v_mfma_f32_32x32x16_bf16 v[112:127], v[236:239], v[60:63], v[112:127]\n"
  "buffer_load_dwordx4 v[228:231], v2, s[40:43], s77 offen offset:1024\n"
  "v_cvt_pk_bf16_f32 v56, v48, v49\n"
  "v_exp_f32 v52, v132\n"
  "v_exp_f32 v53, v133\n"
  "v_cvt_pk_bf16_f32 v57, v50, v51\n"
  "v_exp_f32 v54, v134\n"
  "v_exp_f32 v55, v135\n"
  "v_mfma_f32_32x32x16_bf16 v[144:159], v[244:247], v[172:175], v[144:159]\n"
  "v_cvt_pk_bf16_f32 v58, v52, v53\n"
  "v_exp_f32 v48, v136\n"
  "v_exp_f32 v49, v137\n"
  "v_cvt_pk_bf16_f32 v59, v54, v55\n"
  "v_exp_f32 v50, v138\n"
  "v_exp_f32 v51, v139\n"
  "s_waitcnt vmcnt(14)\n"
  "v_mfma_f32_32x32x16_bf16 v[64:79], v[248:251], v[56:59], v[64:79]\n"
  "v_cvt_pk_bf16_f32 v60, v48, v49\n"
  "v_exp_f32 v52, v140\n"
  "v_exp_f32 v53, v141\n"
  "v_cvt_pk_bf16_f32 v61, v50, v51\n"
  "v_exp_f32 v54, v142\n"
  "v_exp_f32 v55, v143\n"
  "v_mfma_f32_32x32x16_bf16 v[128:143], v[240:243], v[176:179], 0\n"
  "v_cvt_pk_bf16_f32 v62, v52, v53\n"
  "v_exp_f32 v48, v144\n"
  "v_exp_f32 v49, v145\n"
  "v_cvt_pk_bf16_f32 v63, v54, v55\n"
  "v_exp_f32 v50, v146\n"
  "v_exp_f32 v51, v147\n"
  "v_mfma_f32_32x32x16_bf16 v[64:79], v[252:255], v[60:63], v[64:79]\n"
  "buffer_load_dwordx4 v[232:235], v2, s[40:43], s77 offen offset:2048\n"
  "v_cvt_pk_bf16_f32 v56, v48, v49\n"
  "v_exp_f32 v52, v148\n"
  "v_exp_f32 v53, v149\n"
  "v_cvt_pk_bf16_f32 v57, v50, v51\n"
  "v_exp_f32 v54, v150\n"
  "v_exp_f32 v55, v151\n"
  "v_mfma_f32_32x32x16_bf16 v[128:143], v[244:247], v[180:183], v[128:143]\n"
  "v_cvt_pk_bf16_f32 v58, v52, v53\n"
  "v_exp_f32 v48, v152\n"
  "v_exp_f32 v49, v153\n"
  "v_cvt_pk_bf16_f32 v59, v54, v55\n"
  "v_exp_f32 v50, v154\n"
  "v_exp_f32 v51, v155\n"
  "v_mfma_f32_32x32x16_bf16 v[80:95], v[248:251], v[56:59], v[80:95]\n"
  "v_cvt_pk_bf16_f32 v60, v48, v49\n"
  "v_exp_f32 v52, v156\n"
  "v_exp_f32 v53, v157\n"
  "v_cvt_pk_bf16_f32 v61, v50, v51\n"
  "v_exp_f32 v54, v158\n"
  "v_exp_f32 v55, v159\n"
  "v_mfma_f32_32x32x16_bf16 v[144:159], v[240:243], v[184:187], 0\n"
  "v_cvt_pk_bf16_f32 v62, v52, v53\n"
  "v_exp_f32 v48, v128\n"
  "v_exp_f32 v49, v129\n"
  "v_cvt_pk_bf16_f32 v63, v54, v55\n"
  "v_exp_f32 v50, v130\n"
  "v_exp_f32 v51, v131\n"
  "v_mfma_f32_32x32x16_bf16 v[80:95], v[252:255], v[60:63], v[80:95]\n"
  "buffer_load_dwordx4 v[236:239], v2, s[40:43], s77 offen offset:3072\n"
  "v_cvt_pk_bf16_f32 v56, v48, v49\n"
  "v_exp_f32 v52, v132\n"
  "v_exp_f32 v53, v133\n"
  "v_cvt_pk_bf16_f32 v57, v50, v51\n"
  "v_exp_f32 v54, v134\n"
  "v_exp_f32 v55, v135\n"
  "v_mfma_f32_32x32x16_bf16 v[144:159], v[244:247], v[188:191], v[144:159]\n"
  "v_cvt_pk_bf16_f32 v58, v52, v53\n"
  "v_exp_f32 v48, v136\n"
  "v_exp_f32 v49, v137\n"
  "v_cvt_pk_bf16_f32 v59, v54, v55\n"
  "v_exp_f32 v50, v138\n"
  "v_exp_f32 v51, v139\n"
  "v_mfma_f32_32x32x16_bf16 v[96:111], v[248:251], v[56:59], v[96:111]\n"
  "v_cvt_pk_bf16_f32 v60, v48, v49\n"
  "v_exp_f32 v52, v140\n"
  "v_exp_f32 v53, v141\n"
  "v_cvt_pk_bf16_f32 v61, v50, v51\n"
  "v_exp_f32 v54, v142\n"
  "v_exp_f32 v55, v143\n"
  "s_waitcnt vmcnt(14)\n"
  "v_mfma_f32_32x32x16_bf16 v[128:143], v[192:195], v[160:163], 0\n"
  "v_cvt_pk_bf16_f32 v62, v52, v53\n"
  "v_exp_f32 v48, v144\n"
  "v_exp_f32 v49, v145\n"
  "v_cvt_pk_bf16_f32 v63, v54, v55\n"
  "v_exp_f32 v50, v146\n"
  "v_exp_f32 v51, v147\n"
  "v_mfma_f32_32x32x16_bf16 v[96:111], v[252:255], v[60:63], v[96:111]\n"
  "buffer_load_dwordx4 v[240:243], v2, s[40:43], s78 offen offset:0\n"
  "v_cvt_pk_bf16_f32 v56, v48, v49\n"
  "v_exp_f32 v52, v148\n"
  "v_exp_f32 v53, v149\n"
  "v_cvt_pk_bf16_f32 v57, v50, v51\n"
  "v_exp_f32 v54, v150\n"
  "v_exp_f32 v55, v151\n"
  "v_mfma_f32_32x32x16_bf16 v[128:143], v[196:199], v[164:167], v[128:143]\n"
  "v_cvt_pk_bf16_f32 v58, v52, v53\n"
  "v_exp_f32 v48, v152\n"
  "v_exp_f32 v49, v153\n"
  "v_cvt_pk_bf16_f32 v59, v54, v55\n"
  "v_exp_f32 v50, v154\n"
  "v_exp_f32 v51, v155\n"
  "v_mfma_f32_32x32x16_bf16 v[112:127], v[248:251], v[56:59], v[112:127]\n"
  "v_cvt_pk_bf16_f32 v60, v48, v49\n"
  "v_exp_f32 v52, v156\n"
  "v_exp_f32 v53, v157\n"
  "v_cvt_pk_bf16_f32 v61, v50, v51\n"
  "v_exp_f32 v54, v158\n"
  "v_exp_f32 v55, v159\n"
  "v_mfma_f32_32x32x16_bf16 v[144:159], v[192:195], v[168:171], 0\n"
  "v_cvt_pk_bf16_f32 v62, v52, v53\n"
  "v_exp_f32 v48, v128\n"
  "v_exp_f32 v49, v129\n"
  "v_cvt_pk_bf16_f32 v63, v54, v55\n"
  "v_exp_f32 v50, v130\n"
  "v_exp_f32 v51, v131\n"
  "v_mfma_f32_32x32x16_bf16 v[112:127], v[252:255], v[60:63], v[112:127]\n"
  "buffer_load_dwordx4 v[244:247], v2, s[40:43], s78 offen offset:1024\n"
  "v_cvt_pk_bf16_f32 v56, v48, v49\n"
  "v_exp_f32 v52, v132\n"
  "v_exp_f32 v53, v133\n"
  "v_cvt_pk_bf16_f32 v57, v50, v51\n"
  "v_exp_f32 v54, v134\n"
  "v_exp_f32 v55, v135\n"
  "v_mfma_f32_32x32x16_bf16 v[144:159], v[196:199], v[172:175], v[144:159]\n"
  "v_cvt_pk_bf16_f32 v58, v52, v53\n"
  "v_exp_f32 v48, v136\n"
  "v_exp_f32 v49, v137\n"
  "v_cvt_pk_bf16_f32 v59, v54, v55\n"
  "v_exp_f32 v50, v138\n"
  "v_exp_f32 v51, v139\n"
  "s_waitcnt vmcnt(14)\n"
  "v_mfma_f32_32x32x16_bf16 v[64:79], v[200:203], v[56:59], v[64:79]\n"
  "v_cvt_pk_bf16_f32 v60, v48, v49\n"
  "v_exp_f32 v52, v140\n"
  "v_exp_f32 v53, v141\n"
  "v_cvt_pk_bf16_f32 v61, v50, v51\n"
  "v_exp_f32 v54, v142\n"
  "v_exp_f32 v55, v143\n"
  "v_mfma_f32_32x32x16_bf16 v[128:143], v[192:195], v[176:179], 0\n"
  "v_cvt_pk_bf16_f32 v62, v52, v53\n"
  "v_exp_f32 v48, v144\n"
  "v_exp_f32 v49, v145\n"
  "v_cvt_pk_bf16_f32 v63, v54, v55\n"
  "v_exp_f32 v50, v146\n"
  "v_exp_f32 v51, v147\n"
  "v_mfma_f32_32x32x16_bf16 v[64:79], v[204:207], v[60:63], v[64:79]\n"
  "buffer_load_dwordx4 v[248:251], v2, s[40:43], s78 offen offset:2048\n"
  "v_cvt_pk_bf16_f32 v56, v48, v49\n"
  "v_exp_f32 v52, v148\n"
  "v_exp_f32 v53, v149\n"
  "v_cvt_pk_bf16_f32 v57, v50, v51\n"
  "v_exp_f32 v54, v150\n"
  "v_exp_f32 v55, v151\n"
  "v_mfma_f32_32x32x16_bf16 v[128:143], v[196:199], v[180:183], v[128:143]\n"
  "v_cvt_pk_bf16_f32 v58, v52, v53\n"
  "v_exp_f32 v48, v152\n"
  "v_exp_f32 v49, v153\n"
  "v_cvt_pk_bf16_f32 v59, v54, v55\n"
  "v_exp_f32 v50, v154\n"
  "v_exp_f32 v51, v155\n"
  "v_mfma_f32_32x32x16_bf16 v[80:95], v[200:203], v[56:59], v[80:95]\n"
  "v_cvt_pk_bf16_f32 v60, v48, v49\n"
  "v_exp_f32 v52, v156\n"
  "v_exp_f32 v53, v157\n"
  "v_cvt_pk_bf16_f32 v61, v50, v51\n"
  "v_exp_f32 v54, v158\n"
  "v_exp_f32 v55, v159\n"
  "v_mfma_f32_32x32x16_bf16 v[144:159], v[192:195], v[184:187], 0\n"
  "v_cvt_pk_bf16_f32 v62, v52, v53\n"
  "v_exp_f32 v48, v128\n"
  "v_exp_f32 v49, v129\n"
  "v_cvt_pk_bf16_f32 v63, v54, v55\n"
  "v_exp_f32 v50, v130\n"
  "v_exp_f32 v51, v131\n"
  "v_mfma_f32_32x32x16_bf16 v[80:95], v[204:207], v[60:63], v[80:95]\n"
  "buffer_load_dwordx4 v[252:255], v2, s[40:43], s78 offen offset:3072\n"
  "v_cvt_pk_bf16_f32 v56, v48, v49\n"
  "v_exp_f32 v52, v132\n"
  "v_exp_f32 v53, v133\n"
  "v_cvt_pk_bf16_f32 v57, v50, v51\n"
  "v_exp_f32 v54, v134\n"
  "v_exp_f32 v55, v135\n"
  "v_mfma_f32_32x32x16_bf16 v[144:159], v[196:199], v[188:191], v[144:159]\n"
  "v_cvt_pk_bf16_f32 v58, v52, v53\n"
  "v_exp_f32 v48, v136\n"
  "v_exp_f32 v49, v137\n"
  "v_cvt_pk_bf16_f32 v59, v54, v55\n"
  "v_exp_f32 v50, v138\n"
  "v_exp_f32 v51, v139\n"
  "v_mfma_f32_32x32x16_bf16 v[96:111], v[200:203], v[56:59], v[96:111]\n"
  "v_cvt_pk_bf16_f32 v60, v48, v49\n"
  "v_exp_f32 v52, v140\n"
  "v_exp_f32 v53, v141\n"
  "v_cvt_pk_bf16_f32 v61, v50, v51\n"
  "v_exp_f32 v54, v142\n"
  "v_exp_f32 v55, v143\n"
  "s_waitcnt vmcnt(14)\n"
  "v_mfma_f32_32x32x16_bf16 v[128:143], v[16:19], v[160:163], 0\n"
  "v_cvt_pk_bf16_f32 v62, v52, v53\n"
  "v_exp_f32 v48, v144\n"
  "v_exp_f32 v49, v145\n"
  "v_cvt_pk_bf16_f32 v63, v54, v55\n"
  "v_exp_f32 v50, v146\n"
  "v_exp_f32 v51, v147\n"
  "v_mfma_f32_32x32x16_bf16 v[96:111], v[204:207], v[60:63], v[96:111]\n"
  "buffer_load_dwordx4 v[192:195], v2, s[40:43], s79 offen offset:0\n"
  "v_cvt_pk_bf16_f32 v56, v48, v49\n"
  "v_exp_f32 v52, v148\n"
  "v_exp_f32 v53, v149\n"
  "v_cvt_pk_bf16_f32 v57, v50, v51\n"
  "v_exp_f32 v54, v150\n"
  "v_exp_f32 v55, v151\n"
  "v_mfma_f32_32x32x16_bf16 v[128:143], v[20:23], v[164:167], v[128:143]\n"
  "v_cvt_pk_bf16_f32 v58, v52, v53\n"
  "v_exp_f32 v48, v152\n"
  "v_exp_f32 v49, v153\n"
  "v_cvt_pk_bf16_f32 v59, v54, v55\n"
  "v_exp_f32 v50, v154\n"
  "v_exp_f32 v51, v155\n"
  "v_mfma_f32_32x32x16_bf16 v[112:127], v[200:203], v[56:59], v[112:127]\n"
  "v_cvt_pk_bf16_f32 v60, v48, v49\n"
  "v_exp_f32 v52, v156\n"
  "v_exp_f32 v53, v157\n"
  "v_cvt_pk_bf16_f32 v61, v50, v51\n"
  "v_exp_f32 v54, v158\n"
  "v_exp_f32 v55, v159\n"
  "v_mfma_f32_32x32x16_bf16 v[144:159], v[16:19], v[168:171], 0\n"
  "v_cvt_pk_bf16_f32 v62, v52, v53\n"
  "v_exp_f32 v48, v128\n"
  "v_exp_f32 v49, v129\n"
  "v_cvt_pk_bf16_f32 v63, v54, v55\n"
  "v_exp_f32 v50, v130\n"
  "v_exp_f32 v51, v131\n"
  "v_mfma_f32_32x32x16_bf16 v[112:127], v[204:207], v[60:63], v[112:127]\n"
  "buffer_load_dwordx4 v[196:199], v2, s[40:43], s79 offen offset:1024\n"
  "v_cvt_pk_bf16_f32 v56, v48, v49\n"
  "v_exp_f32 v52, v132\n"
  "v_exp_f32 v53, v133\n"
  "v_cvt_pk_bf16_f32 v57, v50, v51\n"
  "v_exp_f32 v54, v134\n"
  "v_exp_f32 v55, v135\n"
  "v_mfma_f32_32x32x16_bf16 v[144:159], v[20:23], v[172:175], v[144:159]\n"
  "v_cvt_pk_bf16_f32 v58, v52, v53\n"
  "v_exp_f32 v48, v136\n"
  "v_exp_f32 v49, v137\n"
  "v_cvt_pk_bf16_f32 v59, v54, v55\n"
  "v_exp_f32 v50, v138\n"
  "v_exp_f32 v51, v139\n"
  "s_waitcnt vmcnt(14)\n"
  "v_mfma_f32_32x32x16_bf16 v[64:79], v[24:27], v[56:59], v[64:79]\n"
  "v_cvt_pk_bf16_f32 v60, v48, v49\n"
  "v_exp_f32 v52, v140\n"
  "v_exp_f32 v53, v141\n"
  "v_cvt_pk_bf16_f32 v61, v50, v51\n"
  "v_exp_f32 v54, v142\n"
  "v_exp_f32 v55, v143\n"
  "v_mfma_f32_32x32x16_bf16 v[128:143], v[16:19], v[176:179], 0\n"
  "v_cvt_pk_bf16_f32 v62, v52, v53\n"
  "v_exp_f32 v48, v144\n"
  "v_exp_f32 v49, v145\n"
  "v_cvt_pk_bf16_f32 v63, v54, v55\n"
  "v_exp_f32 v50, v146\n"
  "v_exp_f32 v51, v147\n"
  "v_mfma_f32_32x32x16_bf16 v[64:79], v[28:31], v[60:63], v[64:79]\n"
  "buffer_load_dwordx4 v[200:203], v2, s[40:43], s79 offen offset:2048\n"
  "v_cvt_pk_bf16_f32 v56, v48, v49\n"
  "v_exp_f32 v52, v148\n"
  "v_exp_f32 v53, v149\n"
  "v_cvt_pk_bf16_f32 v57, v50, v51\n"
  "v_exp_f32 v54, v150\n"
  "v_exp_f32 v55, v151\n"
  "v_mfma_f32_32x32x16_bf16 v[128:143], v[20:23], v[180:183], v[128:143]\n"
  "v_cvt_pk_bf16_f32 v58, v52, v53\n"
  "v_exp_f32 v48, v152\n"
  "v_exp_f32 v49, v153\n"
  "v_cvt_pk_bf16_f32 v59, v54, v55\n"
  "v_exp_f32 v50, v154\n"
  "v_exp_f32 v51, v155\n"
  "v_mfma_f32_32x32x16_bf16 v[80:95], v[24:27], v[56:59], v[80:95]\n"
  "v_cvt_pk_bf16_f32 v60, v48, v49\n"
  "v_exp_f32 v52, v156\n"
  "v_exp_f32 v53, v157\n"
  "v_cvt_pk_bf16_f32 v61, v50, v51\n"
  "v_exp_f32 v54, v158\n"
  "v_exp_f32 v55, v159\n"
  "v_mfma_f32_32x32x16_bf16 v[144:159], v[16:19], v[184:187], 0\n"
  "v_cvt_pk_bf16_f32 v62, v52, v53\n"
  "v_exp_f32 v48, v128\n"
  "v_exp_f32 v49, v129\n"
  "v_cvt_pk_bf16_f32 v63, v54, v55\n"
  "v_exp_f32 v50, v130\n"
  "v_exp_f32 v51, v131\n"
  "v_mfma_f32_32x32x16_bf16 v[80:95], v[28:31], v[60:63], v[80:95]\n"
  "buffer_load_dwordx4 v[204:207], v2, s[40:43], s79 offen offset:3072\n"
  "v_cvt_pk_bf16_f32 v56, v48, v49\n"
  "v_exp_f32 v52, v132\n"
  "v_exp_f32 v53, v133\n"
  "v_cvt_pk_bf16_f32 v57, v50, v51\n"
  "v_exp_f32 v54, v134\n"
  "v_exp_f32 v55, v135\n"
  "v_mfma_f32_32x32x16_bf16 v[144:159], v[20:23], v[188:191], v[144:159]\n"
  "v_cvt_pk_bf16_f32 v58, v52, v53\n"
  "v_exp_f32 v48, v136\n"
  "v_exp_f32 v49, v137\n"
  "v_cvt_pk_bf16_f32 v59, v54, v55\n"
  "v_exp_f32 v50, v138\n"
  "v_exp_f32 v51, v139\n"
  "v_mfma_f32_32x32x16_bf16 v[96:111], v[24:27], v[56:59], v[96:111]\n"
  "v_cvt_pk_bf16_f32 v60, v48, v49\n"
  "v_exp_f32 v52, v140\n"
  "v_exp_f32 v53, v141\n"
  "v_cvt_pk_bf16_f32 v61, v50, v51\n"
  "v_exp_f32 v54, v142\n"
  "v_exp_f32 v55, v143\n"
  "s_waitcnt vmcnt(14)\n"
  "v_mfma_f32_32x32x16_bf16 v[128:143], v[208:211], v[160:163], 0\n"
  "v_cvt_pk_bf16_f32 v62, v52, v53\n"
  "v_exp_f32 v48, v144\n"
  "v_exp_f32 v49, v145\n"
  "v_cvt_pk_bf16_f32 v63, v54, v55\n"
  "v_exp_f32 v50, v146\n"
  "v_exp_f32 v51, v147\n"
  "v_mfma_f32_32x32x16_bf16 v[96:111], v[28:31], v[60:63], v[96:111]\n"
  "v_cvt_pk_bf16_f32 v56, v48, v49\n"
  "v_exp_f32 v52, v148\n"
  "v_exp_f32 v53, v149\n"
  "v_cvt_pk_bf16_f32 v57, v50, v51\n"
  "v_exp_f32 v54, v150\n"
  "v_exp_f32 v55, v151\n"
  "v_mfma_f32_32x32x16_bf16 v[128:143], v[212:215], v[164:167], v[128:143]\n"
  "v_cvt_pk_bf16_f32 v58, v52, v53\n"
  "v_exp_f32 v48, v152\n"
  "v_exp_f32 v49, v153\n"
  "v_cvt_pk_bf16_f32 v59, v54, v55\n"
  "v_exp_f32 v50, v154\n"
  "v_exp_f32 v51, v155\n"
  "v_mfma_f32_32x32x16_bf16 v[112:127], v[24:27], v[56:59], v[112:127]\n"
  "v_cvt_pk_bf16_f32 v60, v48, v49\n"
  "v_exp_f32 v52, v156\n"
  "v_exp_f32 v53, v157\n"
  "v_cvt_pk_bf16_f32 v61, v50, v51\n"
  "v_exp_f32 v54, v158\n"
  "v_exp_f32 v55, v159\n"
  "v_mfma_f32_32x32x16_bf16 v[144:159], v[208:211], v[168:171], 0\n"
  "v_cvt_pk_bf16_f32 v62, v52, v53\n"
  "v_exp_f32 v48, v128\n"
  "v_exp_f32 v49, v129\n"
  "v_cvt_pk_bf16_f32 v63, v54, v55\n"
  "v_exp_f32 v50, v130\n"
  "v_exp_f32 v51, v131\n"
  "v_mfma_f32_32x32x16_bf16 v[112:127], v[28:31], v[60:63], v[112:127]\n"
  "v_cvt_pk_bf16_f32 v56, v48, v49\n"
  "v_exp_f32 v52, v132\n"
  "v_exp_f32 v53, v133\n"
  "v_cvt_pk_bf16_f32 v57, v50, v51\n"
  "v_exp_f32 v54, v134\n"
  "v_exp_f32 v55, v135\n"
  "v_mfma_f32_32x32x16_bf16 v[144:159], v[212:215], v[172:175], v[144:159]\n"
  "v_cvt_pk_bf16_f32 v58, v52, v53\n"
  "v_exp_f32 v48, v136\n"
  "v_exp_f32 v49, v137\n"
  "v_cvt_pk_bf16_f32 v59, v54, v55\n"
  "v_exp_f32 v50, v138\n"
  "v_exp_f32 v51, v139\n"
  "s_waitcnt vmcnt(12)\n"
  "v_mfma_f32_32x32x16_bf16 v[64:79], v[216:219], v[56:59], v[64:79]\n"
  "v_cvt_pk_bf16_f32 v60, v48, v49\n"
  "v_exp_f32 v52, v140\n"
  "v_exp_f32 v53, v141\n"
  "v_cvt_pk_bf16_f32 v61, v50, v51\n"
  "v_exp_f32 v54, v142\n"
  "v_exp_f32 v55, v143\n"
  "v_mfma_f32_32x32x16_bf16 v[128:143], v[208:211], v[176:179], 0\n"
  "v_cvt_pk_bf16_f32 v62, v52, v53\n"
  "v_exp_f32 v48, v144\n"
  "v_exp_f32 v49, v145\n"
  "v_cvt_pk_bf16_f32 v63, v54, v55\n"
  "v_exp_f32 v50, v146\n"
  "v_exp_f32 v51, v147\n"
  "v_mfma_f32_32x32x16_bf16 v[64:79], v[220:223], v[60:63], v[64:79]\n"
  "v_cvt_pk_bf16_f32 v56, v48, v49\n"
  "v_exp_f32 v52, v148\n"
  "v_exp_f32 v53, v149\n"
  "v_cvt_pk_bf16_f32 v57, v50, v51\n"
  "v_exp_f32 v54, v150\n"
  "v_exp_f32 v55, v151\n"
  "v_mfma_f32_32x32x16_bf16 v[128:143], v[212:215], v[180:183], v[128:143]\n"
  "v_cvt_pk_bf16_f32 v58, v52, v53\n"
  "v_exp_f32 v48, v152\n"
  "v_exp_f32 v49, v153\n"
  "v_cvt_pk_bf16_f32 v59, v54, v55\n"
  "v_exp_f32 v50, v154\n"
  "v_exp_f32 v51, v155\n"
  "v_mfma_f32_32x32x16_bf16 v[80:95], v[216:219], v[56:59], v[80:95]\n"
  "v_cvt_pk_bf16_f32 v60, v48, v49\n"
  "v_exp_f32 v52, v156\n"
  "v_exp_f32 v53, v157\n"
  "v_cvt_pk_bf16_f32 v61, v50, v51\n"
  "v_exp_f32 v54, v158\n"
  "v_exp_f32 v55, v159\n"
  "v_mfma_f32_32x32x16_bf16 v[144:159], v[208:211], v[184:187], 0\n"
  "v_cvt_pk_bf16_f32 v62, v52, v53\n"
  "v_exp_f32 v48, v128\n"
  "v_exp_f32 v49, v129\n"
  "v_cvt_pk_bf16_f32 v63, v54, v55\n"
  "v_exp_f32 v50, v130\n"
  "v_exp_f32 v51, v131\n"
  "v_mfma_f32_32x32x16_bf16 v[80:95], v[220:223], v[60:63], v[80:95]\n"
  "v_cvt_pk_bf16_f32 v56, v48, v49\n"
  "v_exp_f32 v52, v132\n"
  "v_exp_f32 v53, v133\n"
  "v_cvt_pk_bf16_f32 v57, v50, v51\n"
  "v_exp_f32 v54, v134\n"
  "v_exp_f32 v55, v135\n"
  "v_mfma_f32_32x32x16_bf16 v[144:159], v[212:215], v[188:191], v[144:159]\n"
  "v_cvt_pk_bf16_f32 v58, v52, v53\n"
  "v_exp_f32 v48, v136\n"
  "v_exp_f32 v49, v137\n"
  "v_cvt_pk_bf16_f32 v59, v54, v55\n"
  "v_exp_f32 v50, v138\n"
  "v_exp_f32 v51, v139\n"
  "v_mfma_f32_32x32x16_bf16 v[96:111], v[216:219], v[56:59], v[96:111]\n"
  "v_cvt_pk_bf16_f32 v60, v48, v49\n"
  "v_exp_f32 v52, v140\n"
  "v_exp_f32 v53, v141\n"
  "v_cvt_pk_bf16_f32 v61, v50, v51\n"
  "v_exp_f32 v54, v142\n"
  "v_exp_f32 v55, v143\n"
  "s_waitcnt vmcnt(10)\n"
  "v_mfma_f32_32x32x16_bf16 v[128:143], v[224:227], v[160:163], 0\n"
  "v_cvt_pk_bf16_f32 v62, v52, v53\n"
  "v_exp_f32 v48, v144\n"
  "v_exp_f32 v49, v145\n"
  "v_cvt_pk_bf16_f32 v63, v54, v55\n"
  "v_exp_f32 v50, v146\n"
  "v_exp_f32 v51, v147\n"
  "v_mfma_f32_32x32x16_bf16 v[96:111], v[220:223], v[60:63], v[96:111]\n"
  "v_cvt_pk_bf16_f32 v56, v48, v49\n"
  "v_exp_f32 v52, v148\n"
  "v_exp_f32 v53, v149\n"
  "v_cvt_pk_bf16_f32 v57, v50, v51\n"
  "v_exp_f32 v54, v150\n"
  "v_exp_f32 v55, v151\n"
  "v_mfma_f32_32x32x16_bf16 v[128:143], v[228:231], v[164:167], v[128:143]\n"
  "buffer_load_dwordx4 v[16:19], v32, s[96:99], s21 offen offset:0\n"
  "v_cvt_pk_bf16_f32 v58, v52, v53\n"
  "v_exp_f32 v48, v152\n"
  "v_exp_f32 v49, v153\n"
  "v_cvt_pk_bf16_f32 v59, v54, v55\n"
  "v_exp_f32 v50, v154\n"
  "v_exp_f32 v51, v155\n"
  "v_mfma_f32_32x32x16_bf16 v[112:127], v[216:219], v[56:59], v[112:127]\n"
  "v_cvt_pk_bf16_f32 v60, v48, v49\n"
  "v_exp_f32 v52, v156\n"
  "v_exp_f32 v53, v157\n"
  "v_cvt_pk_bf16_f32 v61, v50, v51\n"
  "v_exp_f32 v54, v158\n"
  "v_exp_f32 v55, v159\n"
  "v_mfma_f32_32x32x16_bf16 v[144:159], v[224:227], v[168:171], 0\n"
  "v_cvt_pk_bf16_f32 v62, v52, v53\n"
  "v_exp_f32 v48, v128\n"
  "v_exp_f32 v49, v129\n"
  "v_cvt_pk_bf16_f32 v63, v54, v55\n"
  "v_exp_f32 v50, v130\n"
  "v_exp_f32 v51, v131\n"
  "v_mfma_f32_32x32x16_bf16 v[112:127], v[220:223], v[60:63], v[112:127]\n"
  "v_cvt_pk_bf16_f32 v56, v48, v49\n"
  "v_exp_f32 v52, v132\n"
  "v_exp_f32 v53, v133\n"
  "v_cvt_pk_bf16_f32 v57, v50, v51\n"
  "v_exp_f32 v54, v134\n"
  "v_exp_f32 v55, v135\n"
  "v_mfma_f32_32x32x16_bf16 v[144:159], v[228:231], v[172:175], v[144:159]\n"
  "buffer_load_dwordx4 v[20:23], v32, s[96:99], s21 offen offset:32\n"
  "v_cvt_pk_bf16_f32 v58, v52, v53\n"
  "v_exp_f32 v48, v136\n"
  "v_exp_f32 v49, v137\n"
  "v_cvt_pk_bf16_f32 v59, v54, v55\n"
  "v_exp_f32 v50, v138\n"
  "v_exp_f32 v51, v139\n"
  "s_waitcnt vmcnt(10)\n"
  "v_mfma_f32_32x32x16_bf16 v[64:79], v[232:235], v[56:59], v[64:79]\n"
  "v_cvt_pk_bf16_f32 v60, v48, v49\n"
  "v_exp_f32 v52, v140\n"
  "v_exp_f32 v53, v141\n"
  "v_cvt_pk_bf16_f32 v61, v50, v51\n"
  "v_exp_f32 v54, v142\n"
  "v_exp_f32 v55, v143\n"
  "v_mfma_f32_32x32x16_bf16 v[128:143], v[224:227], v[176:179], 0\n"
  "v_cvt_pk_bf16_f32 v62, v52, v53\n"
  "v_exp_f32 v48, v144\n"
  "v_exp_f32 v49, v145\n"
  "v_cvt_pk_bf16_f32 v63, v54, v55\n"
  "v_exp_f32 v50, v146\n"
  "v_exp_f32 v51, v147\n"
  "v_mfma_f32_32x32x16_bf16 v[64:79], v[236:239], v[60:63], v[64:79]\n"
  "v_cvt_pk_bf16_f32 v56, v48, v49\n"
  "v_exp_f32 v52, v148\n"
  "v_exp_f32 v53, v149\n"
  "v_cvt_pk_bf16_f32 v57, v50, v51\n"
  "v_exp_f32 v54, v150\n"
  "v_exp_f32 v55, v151\n"
  "v_mfma_f32_32x32x16_bf16 v[128:143], v[228:231], v[180:183], v[128:143]\n"
  "s_mov_b64 s[96:97], s[10:11]\n"
  "s_movk_i32 s98, 3584\n"
  "v_cvt_pk_bf16_f32 v58, v52, v53\n"
  "v_exp_f32 v48, v152\n"
  "v_exp_f32 v49, v153\n"
  "v_cvt_pk_bf16_f32 v59, v54, v55\n"
  "v_exp_f32 v50, v154\n"
  "v_exp_f32 v51, v155\n"
  "v_mfma_f32_32x32x16_bf16 v[80:95], v[232:235], v[56:59], v[80:95]\n"
  "v_cvt_pk_bf16_f32 v60, v48, v49\n"
  "v_exp_f32 v52, v156\n"
  "v_exp_f32 v53, v157\n"
  "v_cvt_pk_bf16_f32 v61, v50, v51\n"
  "v_exp_f32 v54, v158\n"
  "v_exp_f32 v55, v159\n"
  "v_mfma_f32_32x32x16_bf16 v[144:159], v[224:227], v[184:187], 0\n"
  "v_cvt_pk_bf16_f32 v62, v52, v53\n"
  "v_exp_f32 v48, v128\n"
  "v_exp_f32 v49, v129\n"
  "v_cvt_pk_bf16_f32 v63, v54, v55\n"
  "v_exp_f32 v50, v130\n"
  "v_exp_f32 v51, v131\n"
  "v_mfma_f32_32x32x16_bf16 v[80:95], v[236:239], v[60:63], v[80:95]\n"
  "v_cvt_pk_bf16_f32 v56, v48, v49\n"
  "v_exp_f32 v52, v132\n"
  "v_exp_f32 v53, v133\n"
  "v_cvt_pk_bf16_f32 v57, v50, v51\n"
  "v_exp_f32 v54, v134\n"
  "v_exp_f32 v55, v135\n"
  "v_mfma_f32_32x32x16_bf16 v[144:159], v[228:231], v[188:191], v[144:159]\n"
  "buffer_load_dwordx4 v[24:27], v40, s[96:99], s23 offen offset:0\n"
  "v_cvt_pk_bf16_f32 v58, v52, v53\n"
  "v_exp_f32 v48, v136\n"
  "v_exp_f32 v49, v137\n"
  "v_cvt_pk_bf16_f32 v59, v54, v55\n"
  "v_exp_f32 v50, v138\n"
  "v_exp_f32 v51, v139\n"
  "v_mfma_f32_32x32x16_bf16 v[96:111], v[232:235], v[56:59], v[96:111]\n"
  "v_cvt_pk_bf16_f32 v60, v48, v49\n"
  "v_exp_f32 v52, v140\n"
  "v_exp_f32 v53, v141\n"
  "v_cvt_pk_bf16_f32 v61, v50, v51\n"
  "v_exp_f32 v54, v142\n"
  "v_exp_f32 v55, v143\n"
  "s_waitcnt vmcnt(9)\n"
  "v_mfma_f32_32x32x16_bf16 v[128:143], v[240:243], v[160:163], 0\n"
  "v_cvt_pk_bf16_f32 v62, v52, v53\n"
  "v_exp_f32 v48, v144\n"
  "v_exp_f32 v49, v145\n"
  "v_cvt_pk_bf16_f32 v63, v54, v55\n"
  "v_exp_f32 v50, v146\n"
  "v_exp_f32 v51, v147\n"
  "v_mfma_f32_32x32x16_bf16 v[96:111], v[236:239], v[60:63], v[96:111]\n"
  "v_cvt_pk_bf16_f32 v56, v48, v49\n"
  "v_exp_f32 v52, v148\n"
  "v_exp_f32 v53, v149\n"
  "v_cvt_pk_bf16_f32 v57, v50, v51\n"
  "v_exp_f32 v54, v150\n"
  "v_exp_f32 v55, v151\n"
  "v_mfma_f32_32x32x16_bf16 v[128:143], v[244:247], v[164:167], v[128:143]\n"
  "buffer_load_dwordx4 v[28:31], v40, s[96:99], s23 offen offset:32\n"
  "v_cvt_pk_bf16_f32 v58, v52, v53\n"
  "v_exp_f32 v48, v152\n"
  "v_exp_f32 v49, v153\n"
  "v_cvt_pk_bf16_f32 v59, v54, v55\n"
  "v_exp_f32 v50, v154\n"
  "v_exp_f32 v51, v155\n"
  "v_mfma_f32_32x32x16_bf16 v[112:127], v[232:235], v[56:59], v[112:127]\n"
  "v_cvt_pk_bf16_f32 v60, v48, v49\n"
  "v_exp_f32 v52, v156\n"
  "v_exp_f32 v53, v157\n"
  "v_cvt_pk_bf16_f32 v61, v50, v51\n"
  "v_exp_f32 v54, v158\n"
  "v_exp_f32 v55, v159\n"
  "v_mfma_f32_32x32x16_bf16 v[144:159], v[240:243], v[168:171], 0\n"
  "v_cvt_pk_bf16_f32 v62, v52, v53\n"
  "v_exp_f32 v48, v128\n"
  "v_exp_f32 v49, v129\n"
  "v_cvt_pk_bf16_f32 v63, v54, v55\n"
  "v_exp_f32 v50, v130\n"
  "v_exp_f32 v51, v131\n"
  "v_mfma_f32_32x32x16_bf16 v[112:127], v[236:239], v[60:63], v[112:127]\n"
  "v_cvt_pk_bf16_f32 v56, v48, v49\n"
  "v_exp_f32 v52, v132\n"
  "v_exp_f32 v53, v133\n"
  "v_cvt_pk_bf16_f32 v57, v50, v51\n"
  "v_exp_f32 v54, v134\n"
  "v_exp_f32 v55, v135\n"
  "v_mfma_f32_32x32x16_bf16 v[144:159], v[244:247], v[172:175], v[144:159]\n"
  "s_mov_b64 s[96:97], s[14:15]\n"
  "s_movk_i32 s98, 4096\n"
  "v_cvt_pk_bf16_f32 v58, v52, v53\n"
  "v_exp_f32 v48, v136\n"
  "v_exp_f32 v49, v137\n"
  "v_cvt_pk_bf16_f32 v59, v54, v55\n"
  "v_exp_f32 v50, v138\n"
  "v_exp_f32 v51, v139\n"
  "s_waitcnt vmcnt(8)\n"
  "v_mfma_f32_32x32x16_bf16 v[64:79], v[248:251], v[56:59], v[64:79]\n"
  "v_cvt_pk_bf16_f32 v60, v48, v49\n"
  "v_exp_f32 v52, v140\n"
  "v_exp_f32 v53, v141\n"
  "v_cvt_pk_bf16_f32 v61, v50, v51\n"
  "v_exp_f32 v54, v142\n"
  "v_exp_f32 v55, v143\n"
  "v_mfma_f32_32x32x16_bf16 v[128:143], v[240:243], v[176:179], 0\n"
  "v_cvt_pk_bf16_f32 v62, v52, v53\n"
  "v_exp_f32 v48, v144\n"
  "v_exp_f32 v49, v145\n"
  "v_cvt_pk_bf16_f32 v63, v54, v55\n"
  "v_exp_f32 v50, v146\n"
  "v_exp_f32 v51, v147\n"
  "v_mfma_f32_32x32x16_bf16 v[64:79], v[252:255], v[60:63], v[64:79]\n"
  "v_cvt_pk_bf16_f32 v56, v48, v49\n"
  "v_exp_f32 v52, v148\n"
  "v_exp_f32 v53, v149\n"
  "v_cvt_pk_bf16_f32 v57, v50, v51\n"
  "v_exp_f32 v54, v150\n"
  "v_exp_f32 v55, v151\n"
  "v_mfma_f32_32x32x16_bf16 v[128:143], v[244:247], v[180:183], v[128:143]\n"
  "buffer_load_ushort v33, v41, s[96:99], s24 offen\n"
  "v_cvt_pk_bf16_f32 v58, v52, v53\n"
  "v_exp_f32 v48, v152\n"
  "v_exp_f32 v49, v153\n"
  "v_cvt_pk_bf16_f32 v59, v54, v55\n"
  "v_exp_f32 v50, v154\n"
  "v_exp_f32 v51, v155\n"
  "v_mfma_f32_32x32x16_bf16 v[80:95], v[248:251], v[56:59], v[80:95]\n"
  "v_cvt_pk_bf16_f32 v60, v48, v49\n"
  "v_exp_f32 v52, v156\n"
  "v_exp_f32 v53, v157\n"
  "v_cvt_pk_bf16_f32 v61, v50, v51\n"
  "v_exp_f32 v54, v158\n"
  "v_exp_f32 v55, v159\n"
  "v_mfma_f32_32x32x16_bf16 v[144:159], v[240:243], v[184:187], 0\n"
  "v_cvt_pk_bf16_f32 v62, v52, v53\n"
  "v_exp_f32 v48, v128\n"
  "v_exp_f32 v49, v129\n"
  "v_cvt_pk_bf16_f32 v63, v54, v55\n"
  "v_exp_f32 v50, v130\n"
  "v_exp_f32 v51, v131\n"
  "v_mfma_f32_32x32x16_bf16 v[80:95], v[252:255], v[60:63], v[80:95]\n"
  "v_cvt_pk_bf16_f32 v56, v48, v49\n"
  "v_exp_f32 v52, v132\n"
  "v_exp_f32 v53, v133\n"
  "v_cvt_pk_bf16_f32 v57, v50, v51\n"
  "v_exp_f32 v54, v134\n"
  "v_exp_f32 v55, v135\n"
  "v_mfma_f32_32x32x16_bf16 v[144:159], v[244:247], v[188:191], v[144:159]\n"
  "s_mov_b64 s[96:97], s[12:13]\n"
  "s_mov_b32 s98, 7340032\n"
  "v_cvt_pk_bf16_f32 v58, v52, v53\n"
  "v_exp_f32 v48, v136\n"
  "v_exp_f32 v49, v137\n"
  "v_cvt_pk_bf16_f32 v59, v54, v55\n"
  "v_exp_f32 v50, v138\n"
  "v_exp_f32 v51, v139\n"
  "v_mfma_f32_32x32x16_bf16 v[96:111], v[248:251], v[56:59], v[96:111]\n"
  "v_cvt_pk_bf16_f32 v60, v48, v49\n"
  "v_exp_f32 v52, v140\n"
  "v_exp_f32 v53, v141\n"
  "v_cvt_pk_bf16_f32 v61, v50, v51\n"
  "v_exp_f32 v54, v142\n"
  "v_exp_f32 v55, v143\n"
  "s_waitcnt vmcnt(7)\n"
  "v_mfma_f32_32x32x16_bf16 v[128:143], v[192:195], v[160:163], 0\n"
  "v_cvt_pk_bf16_f32 v62, v52, v53\n"
  "v_exp_f32 v48, v144\n"
  "v_exp_f32 v49, v145\n"
  "v_cvt_pk_bf16_f32 v63, v54, v55\n"
  "v_exp_f32 v50, v146\n"
  "v_exp_f32 v51, v147\n"
  "v_mfma_f32_32x32x16_bf16 v[96:111], v[252:255], v[60:63], v[96:111]\n"
  "v_cvt_pk_bf16_f32 v56, v48, v49\n"
  "v_exp_f32 v52, v148\n"
  "v_exp_f32 v53, v149\n"
  "v_cvt_pk_bf16_f32 v57, v50, v51\n"
  "v_exp_f32 v54, v150\n"
  "v_exp_f32 v55, v151\n"
  "v_mfma_f32_32x32x16_bf16 v[128:143], v[196:199], v[164:167], v[128:143]\n"
  "v_cvt_pk_bf16_f32 v58, v52, v53\n"
  "v_exp_f32 v48, v152\n"
  "v_exp_f32 v49, v153\n"
  "v_cvt_pk_bf16_f32 v59, v54, v55\n"
  "v_exp_f32 v50, v154\n"
  "v_exp_f32 v51, v155\n"
  "v_mfma_f32_32x32x16_bf16 v[112:127], v[248:251], v[56:59], v[112:127]\n"
  "v_cvt_pk_bf16_f32 v60, v48, v49\n"
  "v_exp_f32 v52, v156\n"
  "v_exp_f32 v53, v157\n"
  "v_cvt_pk_bf16_f32 v61, v50, v51\n"
  "v_exp_f32 v54, v158\n"
  "v_exp_f32 v55, v159\n"
  "v_mfma_f32_32x32x16_bf16 v[144:159], v[192:195], v[168:171], 0\n"
  "v_cvt_pk_bf16_f32 v62, v52, v53\n"
  "v_exp_f32 v48, v128\n"
  "v_exp_f32 v49, v129\n"
  "v_cvt_pk_bf16_f32 v63, v54, v55\n"
  "v_exp_f32 v50, v130\n"
  "v_exp_f32 v51, v131\n"
  "v_mfma_f32_32x32x16_bf16 v[112:127], v[252:255], v[60:63], v[112:127]\n"
  "v_cvt_pk_bf16_f32 v56, v48, v49\n"
  "v_exp_f32 v52, v132\n"
  "v_exp_f32 v53, v133\n"
  "v_cvt_pk_bf16_f32 v57, v50, v51\n"
  "v_exp_f32 v54, v134\n"
  "v_exp_f32 v55, v135\n"
  "v_mfma_f32_32x32x16_bf16 v[144:159], v[196:199], v[172:175], v[144:159]\n"
  "v_cvt_pk_bf16_f32 v58, v52, v53\n"
  "v_exp_f32 v48, v136\n"
  "v_exp_f32 v49, v137\n"
  "v_cvt_pk_bf16_f32 v59, v54, v55\n"
  "v_exp_f32 v50, v138\n"
  "v_exp_f32 v51, v139\n"
  "s_waitcnt vmcnt(5)\n"
  "v_mfma_f32_32x32x16_bf16 v[64:79], v[200:203], v[56:59], v[64:79]\n"
  "v_cvt_pk_bf16_f32 v60, v48, v49\n"
  "v_exp_f32 v52, v140\n"
  "v_exp_f32 v53, v141\n"
  "v_cvt_pk_bf16_f32 v61, v50, v51\n"
  "v_exp_f32 v54, v142\n"
  "v_exp_f32 v55, v143\n"
  "v_mfma_f32_32x32x16_bf16 v[128:143], v[192:195], v[176:179], 0\n"
  "v_cvt_pk_bf16_f32 v62, v52, v53\n"
  "v_exp_f32 v48, v144\n"
  "v_exp_f32 v49, v145\n"
  "v_cvt_pk_bf16_f32 v63, v54, v55\n"
  "v_exp_f32 v50, v146\n"
  "v_exp_f32 v51, v147\n"
  "v_mfma_f32_32x32x16_bf16 v[64:79], v[204:207], v[60:63], v[64:79]\n"
  "v_cvt_pk_bf16_f32 v56, v48, v49\n"
  "v_exp_f32 v52, v148\n"
  "v_exp_f32 v53, v149\n"
  "v_cvt_pk_bf16_f32 v57, v50, v51\n"
  "v_exp_f32 v54, v150\n"
  "v_exp_f32 v55, v151\n"
  "v_mfma_f32_32x32x16_bf16 v[128:143], v[196:199], v[180:183], v[128:143]\n"
  "v_cvt_pk_bf16_f32 v58, v52, v53\n"
  "v_exp_f32 v48, v152\n"
  "v_exp_f32 v49, v153\n"
  "v_cvt_pk_bf16_f32 v59, v54, v55\n"
  "v_exp_f32 v50, v154\n"
  "v_exp_f32 v51, v155\n"
  "v_mfma_f32_32x32x16_bf16 v[80:95], v[200:203], v[56:59], v[80:95]\n"
  "v_cvt_pk_bf16_f32 v60, v48, v49\n"
  "v_exp_f32 v52, v156\n"
  "v_exp_f32 v53, v157\n"
  "v_cvt_pk_bf16_f32 v61, v50, v51\n"
  "v_exp_f32 v54, v158\n"
  "v_exp_f32 v55, v159\n"
  "v_mfma_f32_32x32x16_bf16 v[144:159], v[192:195], v[184:187], 0\n"
  "ds_write_b128 v6, v[64:67] offset:12288\n"
  "v_cvt_pk_bf16_f32 v62, v52, v53\n"
  "v_exp_f32 v48, v128\n"
  "v_exp_f32 v49, v129\n"
  "v_cvt_pk_bf16_f32 v63, v54, v55\n"
  "v_exp_f32 v50, v130\n"
  "v_exp_f32 v51, v131\n"
  "v_mfma_f32_32x32x16_bf16 v[80:95], v[204:207], v[60:63], v[80:95]\n"
  "ds_write_b128 v6, v[68:71] offset:13312\n"
  "v_cvt_pk_bf16_f32 v56, v48, v49\n"
  "v_exp_f32 v52, v132\n"
  "v_exp_f32 v53, v133\n"
  "v_cvt_pk_bf16_f32 v57, v50, v51\n"
  "v_exp_f32 v54, v134\n"
  "v_exp_f32 v55, v135\n"
  "v_mfma_f32_32x32x16_bf16 v[144:159], v[196:199], v[188:191], v[144:159]\n"
  "ds_write_b128 v6, v[72:75] offset:14336\n"
  "v_cvt_pk_bf16_f32 v58, v52, v53\n"
  "v_exp_f32 v48, v136\n"
  "v_exp_f32 v49, v137\n"
  "v_cvt_pk_bf16_f32 v59, v54, v55\n"
  "v_exp_f32 v50, v138\n"
  "v_exp_f32 v51, v139\n"
  "v_mfma_f32_32x32x16_bf16 v[96:111], v[200:203], v[56:59], v[96:111]\n"
  "ds_write_b128 v6, v[76:79] offset:15360\n"
  "v_cvt_pk_bf16_f32 v60, v48, v49\n"
  "v_exp_f32 v52, v140\n"
  "v_exp_f32 v53, v141\n"
  "v_cvt_pk_bf16_f32 v61, v50, v51\n"
  "v_exp_f32 v54, v142\n"
  "v_exp_f32 v55, v143\n"
  "ds_write_b128 v6, v[80:83] offset:16384\n"
  "v_cvt_pk_bf16_f32 v62, v52, v53\n"
  "v_exp_f32 v48, v144\n"
  "v_exp_f32 v49, v145\n"
  "v_cvt_pk_bf16_f32 v63, v54, v55\n"
  "v_exp_f32 v50, v146\n"
  "v_exp_f32 v51, v147\n"
  "v_mfma_f32_32x32x16_bf16 v[96:111], v[204:207], v[60:63], v[96:111]\n"
  "ds_write_b128 v6, v[84:87] offset:17408\n"
  "v_cvt_pk_bf16_f32 v56, v48, v49\n"
  "v_exp_f32 v52, v148\n"
  "v_exp_f32 v53, v149\n"
  "v_cvt_pk_bf16_f32 v57, v50, v51\n"
  "v_exp_f32 v54, v150\n"
  "v_exp_f32 v55, v151\n"
  "ds_write_b128 v6, v[88:91] offset:18432\n"
  "v_cvt_pk_bf16_f32 v58, v52, v53\n"
  "v_exp_f32 v48, v152\n"
  "v_exp_f32 v49, v153\n"
  "v_cvt_pk_bf16_f32 v59, v54, v55\n"
  "v_exp_f32 v50, v154\n"
  "v_exp_f32 v51, v155\n"
  "v_mfma_f32_32x32x16_bf16 v[112:127], v[200:203], v[56:59], v[112:127]\n"
  "ds_write_b128 v6, v[92:95] offset:19456\n"
  "v_cvt_pk_bf16_f32 v60, v48, v49\n"
  "v_exp_f32 v52, v156\n"
  "v_exp_f32 v53, v157\n"
  "v_cvt_pk_bf16_f32 v61, v50, v51\n"
  "v_exp_f32 v54, v158\n"
  "v_exp_f32 v55, v159\n"
  "ds_write_b128 v6, v[96:99] offset:20480\n"
  "v_cvt_pk_bf16_f32 v62, v52, v53\n"
  "v_cvt_pk_bf16_f32 v63, v54, v55\n"
  "ds_write_b128 v6, v[100:103] offset:21504\n"
  "ds_write_b128 v6, v[104:107] offset:22528\n"
  "ds_write_b128 v6, v[108:111] offset:23552\n"
  "v_mfma_f32_32x32x16_bf16 v[112:127], v[204:207], v[60:63], v[112:127]\n"
  "s_waitcnt vmcnt(0) lgkmcnt(0)\n"
  "s_barrier\n"
  "ds_read_b128 v[128:131], v3 offset:12288\n"
  "ds_read_b128 v[132:135], v3 offset:13312\n"
  "ds_read_b128 v[136:139], v3 offset:14336\n"
  "ds_read_b128 v[140:143], v3 offset:15360\n"
  "ds_read_b128 v[144:147], v4 offset:16384\n"
  "ds_read_b128 v[148:151], v4 offset:17408\n"
  "ds_read_b128 v[152:155], v4 offset:18432\n"
  "ds_read_b128 v[156:159], v4 offset:19456\n"
  "ds_read_b128 v[160:163], v5 offset:20480\n"
  "ds_read_b128 v[164:167], v5 offset:21504\n"
  "ds_read_b128 v[168:171], v5 offset:22528\n"
  "ds_read_b128 v[172:175], v5 offset:23552\n"
  "s_waitcnt lgkmcnt(11)\n"
  "v_pk_add_f32 v[112:113], v[112:113], v[128:129]\n"
  "v_pk_add_f32 v[114:115], v[114:115], v[130:131]\n"
  "s_waitcnt lgkmcnt(10)\n"
  "v_pk_add_f32 v[116:117], v[116:117], v[132:133]\n"
  "v_pk_add_f32 v[118:119], v[118:119], v[134:135]\n"
  "s_waitcnt lgkmcnt(9)\n"
  "v_pk_add_f32 v[120:121], v[120:121], v[136:137]\n"
  "v_pk_add_f32 v[122:123], v[122:123], v[138:139]\n"
  "s_waitcnt lgkmcnt(8)\n"
  "v_pk_add_f32 v[124:125], v[124:125], v[140:141]\n"
  "v_pk_add_f32 v[126:127], v[126:127], v[142:143]\n"
  "s_waitcnt lgkmcnt(7)\n"
  "v_pk_add_f32 v[112:113], v[112:113], v[144:145]\n"
  "v_pk_add_f32 v[114:115], v[114:115], v[146:147]\n"
  "s_waitcnt lgkmcnt(6)\n"
  "v_pk_add_f32 v[116:117], v[116:117], v[148:149]\n"
  "v_pk_add_f32 v[118:119], v[118:119], v[150:151]\n"
  "s_waitcnt lgkmcnt(5)\n"
  "v_pk_add_f32 v[120:121], v[120:121], v[152:153]\n"
  "v_pk_add_f32 v[122:123], v[122:123], v[154:155]\n"
  "s_waitcnt lgkmcnt(4)\n"
  "v_pk_add_f32 v[124:125], v[124:125], v[156:157]\n"
  "v_pk_add_f32 v[126:127], v[126:127], v[158:159]\n"
  "s_waitcnt lgkmcnt(3)\n"
  "v_pk_add_f32 v[112:113], v[112:113], v[160:161]\n"
  "v_pk_add_f32 v[114:115], v[114:115], v[162:163]\n"
  "s_waitcnt lgkmcnt(2)\n"
  "v_pk_add_f32 v[116:117], v[116:117], v[164:165]\n"
  "v_pk_add_f32 v[118:119], v[118:119], v[166:167]\n"
  "s_waitcnt lgkmcnt(1)\n"
  "v_pk_add_f32 v[120:121], v[120:121], v[168:169]\n"
  "v_pk_add_f32 v[122:123], v[122:123], v[170:171]\n"
  "s_waitcnt lgkmcnt(0)\n"
  "s_barrier\n"
  "v_pk_add_f32 v[124:125], v[124:125], v[172:173]\n"
  "v_pk_add_f32 v[126:127], v[126:127], v[174:175]\n"
  "v_mov_b32 v13, v124\n"
  "v_mov_b32 v14, v124\n"
  "v_pk_add_f32 v[128:129], v[112:113], v[114:115]\n"
  "v_pk_add_f32 v[130:131], v[116:117], v[118:119]\n"
  "v_permlane32_swap_b32 v13, v14\n"
  "v_pk_add_f32 v[132:133], v[120:121], v[122:123]\n"
  "v_pk_add_f32 v[134:135], v[124:125], v[126:127]\n"
  "v_pk_add_f32 v[128:129], v[128:129], v[130:131]\n"
  "v_pk_add_f32 v[132:133], v[132:133], v[134:135]\n"
  "v_pk_add_f32 v[128:129], v[128:129], v[132:133]\n"
  "v_add_f32 v128, v128, v129\n"
  "v_mov_b32 v130, 0x21800000\n"
  "v_mov_b32 v131, 0x5d800000\n"
  "v_med3_f32 v130, v14, v130, v131\n"
  "v_fma_f32 v128, v128, 0, v14\n"
  "v_cmp_neq_f32 vcc, v128, v130\n"
  "s_cbranch_vccnz .Lb_safe\n"
  ".Lb_norm:\n"
  "v_rcp_f32 v128, v14\n"
  "s_nop 0\n"
  "v_cndmask_b32 v130, 0, v128, s[26:27]\n"
  "v_pk_mul_f32 v[112:113], v[112:113], v[128:129] op_sel_hi:[1,0]\n"
  "v_pk_mul_f32 v[114:115], v[114:115], v[128:129] op_sel_hi:[1,0]\n"
  "v_pk_mul_f32 v[116:117], v[116:117], v[128:129] op_sel_hi:[1,0]\n"
  "v_pk_mul_f32 v[118:119], v[118:119], v[128:129] op_sel_hi:[1,0]\n"
  "v_pk_mul_f32 v[120:121], v[120:121], v[128:129] op_sel_hi:[1,0]\n"
  "v_pk_mul_f32 v[122:123], v[122:123], v[128:129] op_sel_hi:[1,0]\n"
  "v_pk_mul_f32 v[124:125], v[124:125], v[130:131] op_sel_hi:[1,0]\n"
  "v_pk_mul_f32 v[126:127], v[126:127], v[130:131] op_sel_hi:[1,0]\n"
  "v_cvt_pk_bf16_f32 v176, v112, v113\n"
  "v_cvt_pk_bf16_f32 v177, v114, v115\n"
  "v_cvt_pk_bf16_f32 v178, v116, v117\n"
  "v_cvt_pk_bf16_f32 v179, v118, v119\n"
  "v_cvt_pk_bf16_f32 v180, v120, v121\n"
  "v_cvt_pk_bf16_f32 v181, v122, v123\n"
  "v_cvt_pk_bf16_f32 v182, v124, v125\n"
  "v_cvt_pk_bf16_f32 v183, v126, v127\n"
  "buffer_store_dwordx4 v[176:179], v2, s[36:39], s56 offen sc1\n"
  "buffer_store_dwordx4 v[180:183], v2, s[36:39], s56 offen offset:1024 sc1\n"
  "s_xor_b32 s52, s48, 1\n"
  "s_mul_i32 s52, s52, 40960\n"
  "v_add_u32 v3, s52, v2\n"
  "s_xor_b32 s52, s48, 2\n"
  "s_mul_i32 s52, s52, 40960\n"
  "v_add_u32 v4, s52, v2\n"
  "s_xor_b32 s52, s48, 3\n"
  "s_mul_i32 s52, s52, 40960\n"
  "v_add_u32 v5, s52, v2\n"
  "s_cmp_ge_u32 s50, 28\n"
  "s_cbranch_scc1 .Lb_p5\n"
  "s_add_u32 s76, s95, 917504\n"
  "s_add_u32 s77, s95, 1146880\n"
  "s_add_u32 s78, s95, 1376256\n"
  "s_add_u32 s79, s95, 1605632\n"
  "ds_read_b64_tr_b16 v[192:193], v7 offset:0\n"
  "ds_read_b64_tr_b16 v[194:195], v7 offset:512\n"
  "ds_read_b64_tr_b16 v[196:197], v8 offset:0\n"
  "ds_read_b64_tr_b16 v[198:199], v8 offset:512\n"
  "ds_read_b64_tr_b16 v[200:201], v7 offset:2048\n"
  "ds_read_b64_tr_b16 v[202:203], v7 offset:2560\n"
  "ds_read_b64_tr_b16 v[204:205], v8 offset:2048\n"
  "ds_read_b64_tr_b16 v[206:207], v8 offset:2560\n"
  "ds_read_b64_tr_b16 v[208:209], v7 offset:4096\n"
  "ds_read_b64_tr_b16 v[210:211], v7 offset:4608\n"
  "ds_read_b64_tr_b16 v[212:213], v8 offset:4096\n"
  "ds_read_b64_tr_b16 v[214:215], v8 offset:4608\n"
  "ds_read_b64_tr_b16 v[216:217], v7 offset:6144\n"
  "ds_read_b64_tr_b16 v[218:219], v7 offset:6656\n"
  "ds_read_b64_tr_b16 v[220:221], v8 offset:6144\n"
  "ds_read_b64_tr_b16 v[222:223], v8 offset:6656\n"
  "ds_read_b64_tr_b16 v[224:225], v7 offset:8192\n"
  "ds_read_b64_tr_b16 v[226:227], v7 offset:8704\n"
  "ds_read_b64_tr_b16 v[228:229], v8 offset:8192\n"
  "ds_read_b64_tr_b16 v[230:231], v8 offset:8704\n"
  "ds_read_b64_tr_b16 v[232:233], v7 offset:10240\n"
  "ds_read_b64_tr_b16 v[234:235], v7 offset:10752\n"
  "ds_read_b64_tr_b16 v[236:237], v8 offset:10240\n"
  "ds_read_b64_tr_b16 v[238:239], v8 offset:10752\n"
  "s_lshl_b32 s53, s94, 15\n"
  "v_add_u32 v11, s53, v2\n"
  "v_add_u32 v12, 1024, v11\n"
  "s_lshl_b32 s53, s91, 14\n"
  "s_add_u32 s64, s53, 6291456\n"
  "s_add_u32 s65, s53, 6293504\n"
  "s_add_u32 s66, s53, 6295552\n"
  "s_add_u32 s67, s53, 6297600\n"
  "s_add_u32 s68, s53, 6299648\n"
  "s_add_u32 s69, s53, 6301696\n"
  "s_add_u32 s70, s53, 6303744\n"
  "s_add_u32 s71, s53, 6305792\n"
  "v_lshlrev_b32 v64, 16, v16\n"
  "v_and_b32 v65, 0xffff0000, v16\n"
  "v_lshlrev_b32 v66, 16, v17\n"
  "v_and_b32 v67, 0xffff0000, v17\n"
  "v_lshlrev_b32 v68, 16, v18\n"
  "v_and_b32 v69, 0xffff0000, v18\n"
  "v_lshlrev_b32 v70, 16, v19\n"
  "v_and_b32 v71, 0xffff0000, v19\n"
  "v_lshlrev_b32 v72, 16, v20\n"
  "v_and_b32 v73, 0xffff0000, v20\n"
  "v_lshlrev_b32 v74, 16, v21\n"
  "v_and_b32 v75, 0xffff0000, v21\n"
  "v_lshlrev_b32 v76, 16, v22\n"
  "v_and_b32 v77, 0xffff0000, v22\n"
  "v_lshlrev_b32 v78, 16, v23\n"
  "v_and_b32 v79, 0xffff0000, v23\n"
  "s_mul_i32 s53, s92, 40960\n"
  "s_sub_u32 s53, 24576, s53\n"
  "v_add_u32 v40, s53, v7\n"
  "v_xor_b32 v41, 64, v40\n"
  "s_waitcnt lgkmcnt(0)\n"
  "s_add_u32 m0, s51, 0\n"
  "s_add_u32 s52, s77, 0\n"
  "buffer_load_dwordx4 v9, s[44:47], s52 offen lds\n"
  "buffer_load_dwordx4 v10, s[44:47], s52 offen offset:1024 lds\n"
  "s_add_u32 m0, s51, 2048\n"
  "s_add_u32 s52, s77, 57344\n"
  "buffer_load_dwordx4 v9, s[44:47], s52 offen lds\n"
  "buffer_load_dwordx4 v10, s[44:47], s52 offen offset:1024 lds\n"
  "s_add_u32 m0, s51, 4096\n"
  "s_add_u32 s52, s77, 114688\n"
  "buffer_load_dwordx4 v9, s[44:47], s52 offen lds\n"
  "buffer_load_dwordx4 v10, s[44:47], s52 offen offset:1024 lds\n"
  "ds_read_b64_tr_b16 v[96:97], v40 offset:0\n"
  "ds_read_b64_tr_b16 v[98:99], v40 offset:512\n"
  "ds_read_b64_tr_b16 v[100:101], v41 offset:0\n"
  "ds_read_b64_tr_b16 v[102:103], v41 offset:512\n"
  "ds_read_b64_tr_b16 v[104:105], v40 offset:2048\n"
  "ds_read_b64_tr_b16 v[106:107], v40 offset:2560\n"
  "ds_read_b64_tr_b16 v[108:109], v41 offset:2048\n"
  "ds_read_b64_tr_b16 v[110:111], v41 offset:2560\n"
  "ds_read_b64_tr_b16 v[112:113], v40 offset:4096\n"
  "ds_read_b64_tr_b16 v[114:115], v40 offset:4608\n"
  "ds_read_b64_tr_b16 v[116:117], v41 offset:4096\n"
  "ds_read_b64_tr_b16 v[118:119], v41 offset:4608\n"
  "ds_read_b64_tr_b16 v[120:121], v40 offset:6144\n"
  "ds_read_b64_tr_b16 v[122:123], v40 offset:6656\n"
  "ds_read_b64_tr_b16 v[124:125], v41 offset:6144\n"
  "ds_read_b64_tr_b16 v[126:127], v41 offset:6656\n"
  "ds_read_b64_tr_b16 v[240:241], v40 offset:8192\n"
  "ds_read_b64_tr_b16 v[242:243], v40 offset:8704\n"
  "ds_read_b64_tr_b16 v[244:245], v41 offset:8192\n"
  "ds_read_b64_tr_b16 v[246:247], v41 offset:8704\n"
  "ds_read_b64_tr_b16 v[248:249], v40 offset:10240\n"
  "ds_read_b64_tr_b16 v[250:251], v40 offset:10752\n"
  "ds_read_b64_tr_b16 v[252:253], v41 offset:10240\n"
  "ds_read_b64_tr_b16 v[254:255], v41 offset:10752\n"
  "ds_read_b64_tr_b16 v[16:17], v40 offset:12288\n"
  "ds_read_b64_tr_b16 v[18:19], v40 offset:12800\n"
  "ds_read_b64_tr_b16 v[20:21], v41 offset:12288\n"
  "ds_read_b64_tr_b16 v[22:23], v41 offset:12800\n"
  "ds_read_b64_tr_b16 v[48:49], v40 offset:14336\n"
  "ds_read_b64_tr_b16 v[50:51], v40 offset:14848\n"
  "ds_read_b64_tr_b16 v[52:53], v41 offset:14336\n"
  "ds_read_b64_tr_b16 v[54:55], v41 offset:14848\n"
  "ds_read_b64_tr_b16 v[56:57], v40 offset:40960\n"
  "ds_read_b64_tr_b16 v[58:59], v40 offset:41472\n"
  "ds_read_b64_tr_b16 v[60:61], v41 offset:40960\n"
  "ds_read_b64_tr_b16 v[62:63], v41 offset:41472\n"
  "s_sleep 8\n"
  "buffer_load_dwordx4 v[128:131], v11, s[36:39], s64 offen sc1\n"
  "buffer_load_dwordx4 v[132:135], v12, s[36:39], s64 offen sc1\n"
  "buffer_load_dwordx4 v[136:139], v11, s[36:39], s65 offen sc1\n"
  "buffer_load_dwordx4 v[140:143], v12, s[36:39], s65 offen sc1\n"
  "buffer_load_dwordx4 v[144:147], v11, s[36:39], s66 offen sc1\n"
  "buffer_load_dwordx4 v[148:151], v12, s[36:39], s66 offen sc1\n"
  "buffer_load_dwordx4 v[152:155], v11, s[36:39], s67 offen sc1\n"
  "buffer_load_dwordx4 v[156:159], v12, s[36:39], s67 offen sc1\n"
  "buffer_load_dwordx4 v[160:163], v11, s[36:39], s68 offen sc1\n"
  "buffer_load_dwordx4 v[164:167], v12, s[36:39], s68 offen sc1\n"
  "buffer_load_dwordx4 v[168:171], v11, s[36:39], s69 offen sc1\n"
  "buffer_load_dwordx4 v[172:175], v12, s[36:39], s69 offen sc1\n"
  "buffer_load_dwordx4 v[176:179], v11, s[36:39], s70 offen sc1\n"
  "buffer_load_dwordx4 v[180:183], v12, s[36:39], s70 offen sc1\n"
  "buffer_load_dwordx4 v[184:187], v11, s[36:39], s71 offen sc1\n"
  "buffer_load_dwordx4 v[188:191], v12, s[36:39], s71 offen sc1\n"
  "s_getpc_b64 s[80:81]\n"
  ".Lpc_p3:\n"
  "s_add_u32 s80, s80, .Lh_ring-.Lpc_p3\n"
  "s_addc_u32 s81, s81, 0\n"
  "s_waitcnt vmcnt(14)\n"
  "v_max3_u32 v13, v128, v130, v132\n"
  "v_max_u32 v13, v13, v134\n"
  "v_cmp_eq_u32 vcc, -1, v13\n"
  "s_cbranch_vccnz .Ls_p3_0\n"
  ".Lo_p3_0:\n"
  "s_waitcnt lgkmcnt(15)\n"
  "v_mfma_f32_32x32x16_bf16 v[80:95], v[100:103], v[128:131], 0\n"
  "s_waitcnt vmcnt(12)\n"
  "v_max3_u32 v13, v136, v138, v140\n"
  "v_mfma_f32_32x32x16_bf16 v[64:79], v[96:99], v[128:131], v[64:79]\n"
  "v_max_u32 v13, v13, v142\n"
  "v_cmp_eq_u32 vcc, -1, v13\n"
  "ds_read_b64_tr_b16 v[96:97], v40 offset:43008\n"
  "ds_read_b64_tr_b16 v[98:99], v40 offset:43520\n"
  "ds_read_b64_tr_b16 v[100:101], v41 offset:43008\n"
  "ds_read_b64_tr_b16 v[102:103], v41 offset:43520\n"
  "v_mfma_f32_32x32x16_bf16 v[80:95], v[108:111], v[132:135], v[80:95]\n"
  "s_cbranch_vccnz .Ls_p3_1\n"
  ".Lo_p3_1:\n"
  "v_mfma_f32_32x32x16_bf16 v[64:79], v[104:107], v[132:135], v[64:79]\n"
  "ds_read_b64_tr_b16 v[104:105], v40 offset:45056\n"
  "ds_read_b64_tr_b16 v[106:107], v40 offset:45568\n"
  "ds_read_b64_tr_b16 v[108:109], v41 offset:45056\n"
  "ds_read_b64_tr_b16 v[110:111], v41 offset:45568\n"
  "v_mfma_f32_32x32x16_bf16 v[80:95], v[116:119], v[136:139], v[80:95]\n"
  "s_waitcnt vmcnt(10)\n"
  "v_max3_u32 v13, v144, v146, v148\n"
  "v_mfma_f32_32x32x16_bf16 v[64:79], v[112:115], v[136:139], v[64:79]\n"
  "v_max_u32 v13, v13, v150\n"
  "v_cmp_eq_u32 vcc, -1, v13\n"
  "ds_read_b64_tr_b16 v[112:113], v40 offset:47104\n"
  "ds_read_b64_tr_b16 v[114:115], v40 offset:47616\n"
  "ds_read_b64_tr_b16 v[116:117], v41 offset:47104\n"
  "ds_read_b64_tr_b16 v[118:119], v41 offset:47616\n"
  "v_mfma_f32_32x32x16_bf16 v[80:95], v[124:127], v[140:143], v[80:95]\n"
  "s_cbranch_vccnz .Ls_p3_2\n"
  ".Lo_p3_2:\n"
  "v_mfma_f32_32x32x16_bf16 v[64:79], v[120:123], v[140:143], v[64:79]\n"
  "ds_read_b64_tr_b16 v[120:121], v40 offset:49152\n"
  "ds_read_b64_tr_b16 v[122:123], v40 offset:49664\n"
  "ds_read_b64_tr_b16 v[124:125], v41 offset:49152\n"
  "ds_read_b64_tr_b16 v[126:127], v41 offset:49664\n"
  "v_mfma_f32_32x32x16_bf16 v[80:95], v[244:247], v[144:147], v[80:95]\n"
  "s_waitcnt vmcnt(8)\n"
  "v_max3_u32 v13, v152, v154, v156\n"
  "v_mfma_f32_32x32x16_bf16 v[64:79], v[240:243], v[144:147], v[64:79]\n"
  "v_max_u32 v13, v13, v158\n"
  "v_cmp_eq_u32 vcc, -1, v13\n"
  "ds_read_b64_tr_b16 v[240:241], v40 offset:51200\n"
  "ds_read_b64_tr_b16 v[242:243], v40 offset:51712\n"
  "ds_read_b64_tr_b16 v[244:245], v41 offset:51200\n"
  "ds_read_b64_tr_b16 v[246:247], v41 offset:51712\n"
  "s_waitcnt lgkmcnt(15)\n"
  "v_mfma_f32_32x32x16_bf16 v[80:95], v[252:255], v[148:151], v[80:95]\n"
  "s_cbranch_vccnz .Ls_p3_3\n"
  ".Lo_p3_3:\n"
  "v_mfma_f32_32x32x16_bf16 v[64:79], v[248:251], v[148:151], v[64:79]\n"
  "ds_read_b64_tr_b16 v[248:249], v40 offset:53248\n"
  "ds_read_b64_tr_b16 v[250:251], v40 offset:53760\n"
  "ds_read_b64_tr_b16 v[252:253], v41 offset:53248\n"
  "ds_read_b64_tr_b16 v[254:255], v41 offset:53760\n"
  "v_mfma_f32_32x32x16_bf16 v[80:95], v[20:23], v[152:155], v[80:95]\n"
  "s_waitcnt vmcnt(6)\n"
  "v_max3_u32 v13, v160, v162, v164\n"
  "v_mfma_f32_32x32x16_bf16 v[64:79], v[16:19], v[152:155], v[64:79]\n"
  "v_max_u32 v13, v13, v166\n"
  "v_cmp_eq_u32 vcc, -1, v13\n"
  "ds_read_b64_tr_b16 v[16:17], v40 offset:55296\n"
  "ds_read_b64_tr_b16 v[18:19], v40 offset:55808\n"
  "ds_read_b64_tr_b16 v[20:21], v41 offset:55296\n"
  "ds_read_b64_tr_b16 v[22:23], v41 offset:55808\n"
  "v_mfma_f32_32x32x16_bf16 v[80:95], v[52:55], v[156:159], v[80:95]\n"
  "s_cbranch_vccnz .Ls_p3_4\n"
  ".Lo_p3_4:\n"
  "v_mfma_f32_32x32x16_bf16 v[64:79], v[48:51], v[156:159], v[64:79]\n"
  "v_mfma_f32_32x32x16_bf16 v[80:95], v[60:63], v[160:163], v[80:95]\n"
  "s_waitcnt vmcnt(4)\n"
  "v_max3_u32 v13, v168, v170, v172\n"
  "v_mfma_f32_32x32x16_bf16 v[64:79], v[56:59], v[160:163], v[64:79]\n"
  "v_max_u32 v13, v13, v174\n"
  "v_cmp_eq_u32 vcc, -1, v13\n"
  "s_add_u32 m0, s51, 6144\n"
  "s_add_u32 s52, s77, 172032\n"
  "buffer_load_dwordx4 v9, s[44:47], s52 offen lds\n"
  "buffer_load_dwordx4 v10, s[44:47], s52 offen offset:1024 lds\n"
  "v_mfma_f32_32x32x16_bf16 v[80:95], v[100:103], v[164:167], v[80:95]\n"
  "s_cbranch_vccnz .Ls_p3_5\n"
  ".Lo_p3_5:\n"
  "v_mfma_f32_32x32x16_bf16 v[64:79], v[96:99], v[164:167], v[64:79]\n"
  "s_waitcnt lgkmcnt(15)\n"
  "v_mfma_f32_32x32x16_bf16 v[80:95], v[108:111], v[168:171], v[80:95]\n"
  "s_waitcnt vmcnt(4)\n"
  "v_max3_u32 v13, v176, v178, v180\n"
  "v_mfma_f32_32x32x16_bf16 v[64:79], v[104:107], v[168:171], v[64:79]\n"
  "v_max_u32 v13, v13, v182\n"
  "v_cmp_eq_u32 vcc, -1, v13\n"
  "v_mfma_f32_32x32x16_bf16 v[80:95], v[116:119], v[172:175], v[80:95]\n"
  "s_cbranch_vccnz .Ls_p3_6\n"
  ".Lo_p3_6:\n"
  "v_mfma_f32_32x32x16_bf16 v[64:79], v[112:115], v[172:175], v[64:79]\n"
  "s_waitcnt lgkmcnt(12)\n"
  "v_mfma_f32_32x32x16_bf16 v[80:95], v[124:127], v[176:179], v[80:95]\n"
  "s_waitcnt vmcnt(2)\n"
  "v_max3_u32 v13, v184, v186, v188\n"
  "v_mfma_f32_32x32x16_bf16 v[64:79], v[120:123], v[176:179], v[64:79]\n"
  "v_max_u32 v13, v13, v190\n"
  "v_cmp_eq_u32 vcc, -1, v13\n"
  "s_waitcnt lgkmcnt(8)\n"
  "v_mfma_f32_32x32x16_bf16 v[80:95], v[244:247], v[180:183], v[80:95]\n"
  "s_cbranch_vccnz .Ls_p3_7\n"
  ".Lo_p3_7:\n"
  "s_waitcnt lgkmcnt(4)\n"
  "v_mfma_f32_32x32x16_bf16 v[80:95], v[252:255], v[184:187], v[80:95]\n"
  "s_waitcnt lgkmcnt(0)\n"
  "v_mfma_f32_32x32x16_bf16 v[80:95], v[20:23], v[188:191], v[80:95]\n"
  "v_mfma_f32_32x32x16_bf16 v[64:79], v[240:243], v[180:183], v[64:79]\n"
  "v_mfma_f32_32x32x16_bf16 v[64:79], v[248:251], v[184:187], v[64:79]\n"
  "s_nop 9\n"
  "ds_write_b128 v6, v[80:83] offset:8192\n"
  "ds_write_b128 v6, v[84:87] offset:9216\n"
  "v_mfma_f32_32x32x16_bf16 v[64:79], v[16:19], v[188:191], v[64:79]\n"
  "ds_write_b128 v6, v[88:91] offset:10240\n"
  "ds_write_b128 v6, v[92:95] offset:11264\n"
  "s_lshl_b32 s54, s93, 8\n"
  "s_add_u32 s54, s54, s22\n"
  "s_waitcnt lgkmcnt(0)\n"
  "s_barrier\n"
  "ds_read_b128 v[128:131], v4 offset:8192\n"
  "ds_read_b128 v[132:135], v4 offset:9216\n"
  "ds_read_b128 v[136:139], v4 offset:10240\n"
  "ds_read_b128 v[140:143], v4 offset:11264\n"
  "s_waitcnt lgkmcnt(3)\n"
  "s_nop 0\n"
  "v_pk_add_f32 v[64:65], v[64:65], v[128:129]\n"
  "v_pk_add_f32 v[66:67], v[66:67], v[130:131]\n"
  "s_waitcnt lgkmcnt(2)\n"
  "v_pk_add_f32 v[68:69], v[68:69], v[132:133]\n"
  "v_pk_add_f32 v[70:71], v[70:71], v[134:135]\n"
  "s_waitcnt lgkmcnt(1)\n"
  "v_pk_add_f32 v[72:73], v[72:73], v[136:137]\n"
  "v_pk_add_f32 v[74:75], v[74:75], v[138:139]\n"
  "s_waitcnt lgkmcnt(0)\n"
  "s_barrier\n"
  "v_pk_add_f32 v[76:77], v[76:77], v[140:141]\n"
  "v_pk_add_f32 v[78:79], v[78:79], v[142:143]\n"
  "v_pk_add_f32 v[128:129], v[64:65], v[66:67]\n"
  "v_pk_mul_f32 v[130:131], v[64:65], v[64:65]\n"
  "v_pk_add_f32 v[132:133], v[68:69], v[70:71]\n"
  "v_pk_mul_f32 v[134:135], v[66:67], v[66:67]\n"
  "v_pk_fma_f32 v[130:131], v[68:69], v[68:69], v[130:131]\n"
  "v_pk_fma_f32 v[134:135], v[70:71], v[70:71], v[134:135]\n"
  "v_pk_fma_f32 v[130:131], v[72:73], v[72:73], v[130:131]\n"
  "v_pk_add_f32 v[128:129], v[128:129], v[72:73]\n"
  "v_pk_fma_f32 v[134:135], v[74:75], v[74:75], v[134:135]\n"
  "v_pk_add_f32 v[132:133], v[132:133], v[74:75]\n"
  "v_pk_fma_f32 v[130:131], v[76:77], v[76:77], v[130:131]\n"
  "v_pk_add_f32 v[128:129], v[128:129], v[76:77]\n"
  "v_pk_fma_f32 v[134:135], v[78:79], v[78:79], v[134:135]\n"
  "v_pk_add_f32 v[132:133], v[132:133], v[78:79]\n"
  "v_pk_add_f32 v[128:129], v[128:129], v[132:133]\n"
  "v_pk_add_f32 v[130:131], v[130:131], v[134:135]\n"
  "v_add_f32 v128, v128, v129\n"
  "v_add_f32 v130, v130, v131\n"
  "s_nop 1\n"
  "v_permlane32_swap_b32 v128, v130\n"
  "v_add_f32 v128, v128, v130\n"
  "buffer_store_dword v128, v34, s[36:39], s54 offen sc1\n"
  "v_mov_b32 v80, v24\n"
  "v_mov_b32 v81, v25\n"
  "v_mov_b32 v82, v26\n"
  "v_mov_b32 v83, v27\n"
  "v_mov_b32 v84, v28\n"
  "v_mov_b32 v85, v29\n"
  "v_mov_b32 v86, v30\n"
  "v_mov_b32 v87, v31\n"
  "ds_read_b64_tr_b16 v[240:241], v7 offset:12288\n"
  "ds_read_b64_tr_b16 v[242:243], v7 offset:12800\n"
  "ds_read_b64_tr_b16 v[244:245], v8 offset:12288\n"
  "ds_read_b64_tr_b16 v[246:247], v8 offset:12800\n"
  "ds_read_b64_tr_b16 v[248:249], v7 offset:14336\n"
  "ds_read_b64_tr_b16 v[250:251], v7 offset:14848\n"
  "ds_read_b64_tr_b16 v[252:253], v8 offset:14336\n"
  "ds_read_b64_tr_b16 v[254:255], v8 offset:14848\n"
  "ds_read_b64_tr_b16 v[16:17], v7 offset:16384\n"
  "ds_read_b64_tr_b16 v[18:19], v7 offset:16896\n"
  "ds_read_b64_tr_b16 v[20:21], v8 offset:16384\n"
  "ds_read_b64_tr_b16 v[22:23], v8 offset:16896\n"
  "ds_read_b64_tr_b16 v[24:25], v7 offset:18432\n"
  "ds_read_b64_tr_b16 v[26:27], v7 offset:18944\n"
  "ds_read_b64_tr_b16 v[28:29], v8 offset:18432\n"
  "ds_read_b64_tr_b16 v[30:31], v8 offset:18944\n"
  "ds_read_b64_tr_b16 v[48:49], v7 offset:20480\n"
  "ds_read_b64_tr_b16 v[50:51], v7 offset:20992\n"
  "ds_read_b64_tr_b16 v[52:53], v8 offset:20480\n"
  "ds_read_b64_tr_b16 v[54:55], v8 offset:20992\n"
  "ds_read_b64_tr_b16 v[56:57], v7 offset:22528\n"
  "ds_read_b64_tr_b16 v[58:59], v7 offset:23040\n"
  "ds_read_b64_tr_b16 v[60:61], v8 offset:22528\n"
  "ds_read_b64_tr_b16 v[62:63], v8 offset:23040\n"
  "v_cvt_pk_bf16_f32 v176, v64, v65\n"
  "v_cvt_pk_bf16_f32 v177, v66, v67\n"
  "v_cvt_pk_bf16_f32 v178, v68, v69\n"
  "v_cvt_pk_bf16_f32 v179, v70, v71\n"
  "v_cvt_pk_bf16_f32 v180, v72, v73\n"
  "v_cvt_pk_bf16_f32 v181, v74, v75\n"
  "v_cvt_pk_bf16_f32 v182, v76, v77\n"
  "v_cvt_pk_bf16_f32 v183, v78, v79\n"
  "s_add_u32 s53, s57, 12582912\n"
  "s_sleep 8\n"
  "buffer_load_dwordx2 v[136:137], v35, s[36:39], s22 offen offset:0 sc1\n"
  "buffer_load_dwordx2 v[138:139], v35, s[36:39], s22 offen offset:256 sc1\n"
  "buffer_load_dwordx2 v[140:141], v35, s[36:39], s22 offen offset:512 sc1\n"
  "buffer_load_dwordx2 v[142:143], v35, s[36:39], s22 offen offset:768 sc1\n"
  "buffer_load_dwordx2 v[144:145], v35, s[36:39], s22 offen offset:1024 sc1\n"
  "buffer_load_dwordx2 v[146:147], v35, s[36:39], s22 offen offset:1280 sc1\n"
  "buffer_load_dwordx2 v[148:149], v35, s[36:39], s22 offen offset:1536 sc1\n"
  "buffer_store_dwordx4 v[176:179], v2, s[36:39], s53 offen sc1\n"
  "buffer_store_dwordx4 v[180:183], v2, s[36:39], s53 offen offset:1024 sc1\n"
  "s_waitcnt lgkmcnt(0)\n"
  "s_add_u32 m0, s51, 12288\n"
  "s_add_u32 s52, s78, 114688\n"
  "buffer_load_dwordx4 v9, s[44:47], s52 offen lds\n"
  "buffer_load_dwordx4 v10, s[44:47], s52 offen offset:1024 lds\n"
  "s_add_u32 m0, s51, 14336\n"
  "s_add_u32 s52, s78, 172032\n"
  "buffer_load_dwordx4 v9, s[44:47], s52 offen lds\n"
  "buffer_load_dwordx4 v10, s[44:47], s52 offen offset:1024 lds\n"
  "s_add_u32 m0, s51, 16384\n"
  "s_add_u32 s52, s79, 0\n"
  "buffer_load_dwordx4 v9, s[44:47], s52 offen lds\n"
  "buffer_load_dwordx4 v10, s[44:47], s52 offen offset:1024 lds\n"
  "s_add_u32 m0, s51, 18432\n"
  "s_add_u32 s52, s79, 57344\n"
  "buffer_load_dwordx4 v9, s[44:47], s52 offen lds\n"
  "buffer_load_dwordx4 v10, s[44:47], s52 offen offset:1024 lds\n"
  "s_add_u32 m0, s51, 20480\n"
  "s_add_u32 s52, s79, 114688\n"
  "buffer_load_dwordx4 v9, s[44:47], s52 offen lds\n"
  "buffer_load_dwordx4 v10, s[44:47], s52 offen offset:1024 lds\n"
  "s_add_u32 m0, s51, 22528\n"
  "s_add_u32 s52, s79, 172032\n"
  "buffer_load_dwordx4 v9, s[44:47], s52 offen lds\n"
  "buffer_load_dwordx4 v10, s[44:47], s52 offen offset:1024 lds\n"
  "s_waitcnt vmcnt(14)\n"
  "v_max3_u32 v13, v136, v137, v138\n"
  "v_max3_u32 v14, v139, v140, v141\n"
  "v_max3_u32 v13, v13, v142, v143\n"
  "v_max3_u32 v14, v14, v144, v145\n"
  "v_max3_u32 v13, v13, v146, v147\n"
  "v_max3_u32 v14, v14, v148, v149\n"
  "v_max_u32 v13, v13, v14\n"
  "v_cmp_eq_u32 vcc, -1, v13\n"
  "s_cbranch_vccnz .Lb_stat_retry\n"
  ".Lb_stat_ok:\n"
  "v_pk_add_f32 v[136:137], v[136:137], v[138:139]\n"
  "v_pk_add_f32 v[140:141], v[140:141], v[142:143]\n"
  "v_pk_add_f32 v[144:145], v[144:145], v[146:147]\n"
  "v_pk_add_f32 v[136:137], v[136:137], v[148:149]\n"
  "v_pk_add_f32 v[140:141], v[140:141], v[144:145]\n"
  "v_pk_add_f32 v[136:137], v[136:137], v[140:141]\n"
  "s_nop 1\n"
  "v_permlane32_swap_b32 v136, v137\n"
  "v_add_f32 v136, v136, v137\n"
  "v_mul_f32 v136, 0x3b124925, v136\n"
  "v_mov_b32 v137, v136\n"
  "s_nop 1\n"
  "v_permlane32_swap_b32 v136, v137\n"
  "v_fma_f32 v137, -v136, v136, v137\n"
  "v_max_f32 v137, 0, v137\n"
  "v_add_f32 v138, 0x3727c5ac, v137\n"
  "v_rsq_f32 v138, v138\n"
  "s_nop 0\n"
  "v_mul_f32 v137, v137, v138\n"
  "v_mov_b32 v139, 0x3727c5ac\n"
  "v_fma_f32 v137, v137, v138, v139\n"
  "v_rsq_f32 v137, v137\n"
  "s_nop 0\n"
  "v_mul_f32 v138, v138, v137\n"
  "v_mul_f32 v140, v136, v138\n"
  "v_sub_f32 v140, 0, v140\n"
  "v_pk_fma_f32 v[64:65], v[64:65], v[138:139], v[140:141] op_sel_hi:[1,0,0]\n"
  "v_pk_fma_f32 v[66:67], v[66:67], v[138:139], v[140:141] op_sel_hi:[1,0,0]\n"
  "v_pk_fma_f32 v[68:69], v[68:69], v[138:139], v[140:141] op_sel_hi:[1,0,0]\n"
  "v_pk_fma_f32 v[70:71], v[70:71], v[138:139], v[140:141] op_sel_hi:[1,0,0]\n"
  "v_pk_fma_f32 v[72:73], v[72:73], v[138:139], v[140:141] op_sel_hi:[1,0,0]\n"
  "v_pk_fma_f32 v[74:75], v[74:75], v[138:139], v[140:141] op_sel_hi:[1,0,0]\n"
  "v_pk_fma_f32 v[76:77], v[76:77], v[138:139], v[140:141] op_sel_hi:[1,0,0]\n"
  "v_pk_fma_f32 v[78:79], v[78:79], v[138:139], v[140:141] op_sel_hi:[1,0,0]\n"
  "v_cvt_pk_bf16_f32 v144, v64, v65\n"
  "v_cvt_pk_bf16_f32 v145, v66, v67\n"
  "v_cvt_pk_bf16_f32 v146, v68, v69\n"
  "v_cvt_pk_bf16_f32 v147, v70, v71\n"
  "v_cvt_pk_bf16_f32 v148, v72, v73\n"
  "v_cvt_pk_bf16_f32 v149, v74, v75\n"
  "v_cvt_pk_bf16_f32 v150, v76, v77\n"
  "v_cvt_pk_bf16_f32 v151, v78, v79\n"
  "ds_write_b128 v42, v[144:147]\n"
  "ds_write_b128 v43, v[148:151]\n"
  "s_waitcnt lgkmcnt(0)\n"
  "ds_read_b64_tr_b16 v[152:153], v44 offset:0\n"
  "ds_read_b64_tr_b16 v[154:155], v45 offset:256\n"
  "ds_read_b64_tr_b16 v[156:157], v44 offset:1024\n"
  "ds_read_b64_tr_b16 v[158:159], v45 offset:1280\n"
  "s_add_u32 s53, s57, 8388608\n"
  "s_waitcnt vmcnt(12)\n"
  "s_waitcnt lgkmcnt(0)\n"
  "buffer_store_dwordx4 v[152:155], v2, s[36:39], s53 offen sc1\n"
  "buffer_store_dwordx4 v[156:159], v2, s[36:39], s53 offen offset:1024 sc1\n"
  "s_cmp_eq_u32 s89, 0\n"
  "s_cbranch_scc1 .Lb_p5\n"
  "s_add_u32 m0, s51, 38912\n"
  "s_add_u32 s52, s76, 172032\n"
  "buffer_load_dwordx4 v9, s[44:47], s52 offen lds\n"
  "buffer_load_dwordx4 v10, s[44:47], s52 offen offset:1024 lds\n"
  "s_add_u32 m0, s51, 8192\n"
  "s_add_u32 s52, s78, 0\n"
  "buffer_load_dwordx4 v9, s[44:47], s52 offen lds\n"
  "buffer_load_dwordx4 v10, s[44:47], s52 offen offset:1024 lds\n"
  "s_add_u32 m0, s51, 10240\n"
  "s_add_u32 s52, s78, 57344\n"
  "buffer_load_dwordx4 v9, s[44:47], s52 offen lds\n"
  "buffer_load_dwordx4 v10, s[44:47], s52 offen offset:1024 lds\n"
  "s_barrier\n"
  "v_lshlrev_b32 v64, 16, v80\n"
  "v_and_b32 v65, 0xffff0000, v80\n"
  "v_lshlrev_b32 v66, 16, v81\n"
  "v_and_b32 v67, 0xffff0000, v81\n"
  "v_lshlrev_b32 v68, 16, v82\n"
  "v_and_b32 v69, 0xffff0000, v82\n"
  "v_lshlrev_b32 v70, 16, v83\n"
  "v_and_b32 v71, 0xffff0000, v83\n"
  "v_lshlrev_b32 v72, 16, v84\n"
  "v_and_b32 v73, 0xffff0000, v84\n"
  "v_lshlrev_b32 v74, 16, v85\n"
  "v_and_b32 v75, 0xffff0000, v85\n"
  "v_lshlrev_b32 v76, 16, v86\n"
  "v_and_b32 v77, 0xffff0000, v86\n"
  "v_lshlrev_b32 v78, 16, v87\n"
  "v_and_b32 v79, 0xffff0000, v87\n"
  "s_lshl_b32 s53, s85, 1\n"
  "s_add_u32 s93, s53, s92\n"
  "s_lshl_b32 s53, s93, 17\n"
  "v_add_u32 v11, s53, v2\n"
  "v_xor_b32 v12, 0x20000, v11\n"
  "s_lshl_b32 s53, s48, 15\n"
  "s_add_u32 s53, s53, 8388608\n"
  "s_add_u32 s72, s53, 0\n"
  "s_add_u32 s73, s53, 4096\n"
  "s_add_u32 s74, s53, 8192\n"
  "s_add_u32 s75, s53, 12288\n"
  "s_add_u32 s76, s53, 16384\n"
  "s_add_u32 s77, s53, 20480\n"
  "s_add_u32 s78, s53, 24576\n"
  "s_add_u32 s79, s53, 28672\n"
  "s_add_u32 s64, s72, 0\n"
  "s_add_u32 s65, s72, 1024\n"
  "s_add_u32 s66, s72, 2048\n"
  "s_add_u32 s67, s72, 3072\n"
  "s_add_u32 s68, s73, 0\n"
  "s_add_u32 s69, s73, 1024\n"
  "s_add_u32 s70, s73, 2048\n"
  "s_add_u32 s71, s73, 3072\n"
  "s_mul_i32 s53, s93, 56\n"
  "s_lshl_b32 s54, s86, 1\n"
  "s_add_u32 s53, s53, s54\n"
  "s_add_u32 s53, s53, s91\n"
  "s_lshl_b32 s53, s53, 11\n"
  "s_add_u32 s24, s53, 10452992\n"
  "s_sleep 13\n"
  "buffer_load_dwordx4 v[128:131], v11, s[36:39], s64 offen sc1\n"
  "buffer_load_dwordx4 v[132:135], v12, s[36:39], s64 offen sc1\n"
  "buffer_load_dwordx4 v[136:139], v11, s[36:39], s65 offen sc1\n"
  "buffer_load_dwordx4 v[140:143], v12, s[36:39], s65 offen sc1\n"
  "buffer_load_dwordx4 v[144:147], v11, s[36:39], s66 offen sc1\n"
  "buffer_load_dwordx4 v[148:151], v12, s[36:39], s66 offen sc1\n"
  "buffer_load_dwordx4 v[152:155], v11, s[36:39], s67 offen sc1\n"
  "buffer_load_dwordx4 v[156:159], v12, s[36:39], s67 offen sc1\n"
  "buffer_load_dwordx4 v[160:163], v11, s[36:39], s68 offen sc1\n"
  "buffer_load_dwordx4 v[164:167], v12, s[36:39], s68 offen sc1\n"
  "buffer_load_dwordx4 v[168:171], v11, s[36:39], s69 offen sc1\n"
  "buffer_load_dwordx4 v[172:175], v12, s[36:39], s69 offen sc1\n"
  "buffer_load_dwordx4 v[176:179], v11, s[36:39], s70 offen sc1\n"
  "buffer_load_dwordx4 v[180:183], v12, s[36:39], s70 offen sc1\n"
  "buffer_load_dwordx4 v[184:187], v11, s[36:39], s71 offen sc1\n"
  "buffer_load_dwordx4 v[188:191], v12, s[36:39], s71 offen sc1\n"
  "s_getpc_b64 s[80:81]\n"
  ".Lpc_p4:\n"
  "s_add_u32 s80, s80, .Lh_ring-.Lpc_p4\n"
  "s_addc_u32 s81, s81, 0\n"
  "s_waitcnt vmcnt(14)\n"
  "v_max3_u32 v13, v128, v130, v132\n"
  "v_max_u32 v13, v13, v134\n"
  "v_cmp_eq_u32 vcc, -1, v13\n"
  "s_cbranch_vccnz .Ls_p4_0\n"
  ".Lo_p4_0:\n"
  "v_mfma_f32_32x32x16_bf16 v[64:79], v[192:195], v[128:131], v[64:79]\n"
  "s_waitcnt vmcnt(12)\n"
  "v_max3_u32 v13, v136, v138, v140\n"
  "v_mfma_f32_32x32x16_bf16 v[80:95], v[192:195], v[132:135], 0\n"
  "v_max_u32 v13, v13, v142\n"
  "v_cmp_eq_u32 vcc, -1, v13\n"
  "v_mfma_f32_32x32x16_bf16 v[96:111], v[196:199], v[128:131], 0\n"
  "s_cbranch_vccnz .Ls_p4_1\n"
  ".Lo_p4_1:\n"
  "s_add_u32 s64, s74, 0\n"
  "v_mfma_f32_32x32x16_bf16 v[112:127], v[196:199], v[132:135], 0\n"
  "buffer_load_dwordx4 v[128:131], v11, s[36:39], s64 offen sc1\n"
  "buffer_load_dwordx4 v[132:135], v12, s[36:39], s64 offen sc1\n"
  "v_mfma_f32_32x32x16_bf16 v[64:79], v[200:203], v[136:139], v[64:79]\n"
  "s_waitcnt vmcnt(12)\n"
  "v_max3_u32 v13, v144, v146, v148\n"
  "v_mfma_f32_32x32x16_bf16 v[80:95], v[200:203], v[140:143], v[80:95]\n"
  "v_max_u32 v13, v13, v150\n"
  "v_cmp_eq_u32 vcc, -1, v13\n"
  "v_mfma_f32_32x32x16_bf16 v[96:111], v[204:207], v[136:139], v[96:111]\n"
  "s_cbranch_vccnz .Ls_p4_2\n"
  ".Lo_p4_2:\n"
  "s_add_u32 s65, s74, 1024\n"
  "v_mfma_f32_32x32x16_bf16 v[112:127], v[204:207], v[140:143], v[112:127]\n"
  "buffer_load_dwordx4 v[136:139], v11, s[36:39], s65 offen sc1\n"
  "buffer_load_dwordx4 v[140:143], v12, s[36:39], s65 offen sc1\n"
  "v_mfma_f32_32x32x16_bf16 v[64:79], v[208:211], v[144:147], v[64:79]\n"
  "s_waitcnt vmcnt(12)\n"
  "v_max3_u32 v13, v152, v154, v156\n"
  "v_mfma_f32_32x32x16_bf16 v[80:95], v[208:211], v[148:151], v[80:95]\n"
  "v_max_u32 v13, v13, v158\n"
  "v_cmp_eq_u32 vcc, -1, v13\n"
  "v_mfma_f32_32x32x16_bf16 v[96:111], v[212:215], v[144:147], v[96:111]\n"
  "s_cbranch_vccnz .Ls_p4_3\n"
  ".Lo_p4_3:\n"
  "s_add_u32 s66, s74, 2048\n"
  "v_mfma_f32_32x32x16_bf16 v[112:127], v[212:215], v[148:151], v[112:127]\n"
  "buffer_load_dwordx4 v[144:147], v11, s[36:39], s66 offen sc1\n"
  "buffer_load_dwordx4 v[148:151], v12, s[36:39], s66 offen sc1\n"
  "v_mfma_f32_32x32x16_bf16 v[64:79], v[216:219], v[152:155], v[64:79]\n"
  "s_waitcnt vmcnt(12)\n"
  "v_max3_u32 v13, v160, v162, v164\n"
  "v_mfma_f32_32x32x16_bf16 v[80:95], v[216:219], v[156:159], v[80:95]\n"
  "v_max_u32 v13, v13, v166\n"
  "v_cmp_eq_u32 vcc, -1, v13\n"
  "v_mfma_f32_32x32x16_bf16 v[96:111], v[220:223], v[152:155], v[96:111]\n"
  "s_cbranch_vccnz .Ls_p4_4\n"
  ".Lo_p4_4:\n"
  "s_add_u32 s67, s74, 3072\n"
  "v_mfma_f32_32x32x16_bf16 v[112:127], v[220:223], v[156:159], v[112:127]\n"
  "buffer_load_dwordx4 v[152:155], v11, s[36:39], s67 offen sc1\n"
  "buffer_load_dwordx4 v[156:159], v12, s[36:39], s67 offen sc1\n"
  "v_mfma_f32_32x32x16_bf16 v[64:79], v[224:227], v[160:163], v[64:79]\n"
  "s_waitcnt vmcnt(12)\n"
  "v_max3_u32 v13, v168, v170, v172\n"
  "v_mfma_f32_32x32x16_bf16 v[80:95], v[224:227], v[164:167], v[80:95]\n"
  "v_max_u32 v13, v13, v174\n"
  "v_cmp_eq_u32 vcc, -1, v13\n"
  "v_mfma_f32_32x32x16_bf16 v[96:111], v[228:231], v[160:163], v[96:111]\n"
  "s_cbranch_vccnz .Ls_p4_5\n"
  ".Lo_p4_5:\n"
  "s_add_u32 s68, s75, 0\n"
  "v_mfma_f32_32x32x16_bf16 v[112:127], v[228:231], v[164:167], v[112:127]\n"
  "buffer_load_dwordx4 v[160:163], v11, s[36:39], s68 offen sc1\n"
  "buffer_load_dwordx4 v[164:167], v12, s[36:39], s68 offen sc1\n"
  "v_mfma_f32_32x32x16_bf16 v[64:79], v[232:235], v[168:171], v[64:79]\n"
  "s_waitcnt vmcnt(12)\n"
  "v_max3_u32 v13, v176, v178, v180\n"
  "v_mfma_f32_32x32x16_bf16 v[80:95], v[232:235], v[172:175], v[80:95]\n"
  "v_max_u32 v13, v13, v182\n"
  "v_cmp_eq_u32 vcc, -1, v13\n"
  "v_mfma_f32_32x32x16_bf16 v[96:111], v[236:239], v[168:171], v[96:111]\n"
  "s_cbranch_vccnz .Ls_p4_6\n"
  ".Lo_p4_6:\n"
  "s_add_u32 s69, s75, 1024\n"
  "v_mfma_f32_32x32x16_bf16 v[112:127], v[236:239], v[172:175], v[112:127]\n"
  "buffer_load_dwordx4 v[168:171], v11, s[36:39], s69 offen sc1\n"
  "buffer_load_dwordx4 v[172:175], v12, s[36:39], s69 offen sc1\n"
  "v_mfma_f32_32x32x16_bf16 v[64:79], v[240:243], v[176:179], v[64:79]\n"
  "s_waitcnt vmcnt(12)\n"
  "v_max3_u32 v13, v184, v186, v188\n"
  "v_mfma_f32_32x32x16_bf16 v[80:95], v[240:243], v[180:183], v[80:95]\n"
  "v_max_u32 v13, v13, v190\n"
  "v_cmp_eq_u32 vcc, -1, v13\n"
  "v_mfma_f32_32x32x16_bf16 v[96:111], v[244:247], v[176:179], v[96:111]\n"
  "s_cbranch_vccnz .Ls_p4_7\n"
  ".Lo_p4_7:\n"
  "s_add_u32 s70, s75, 2048\n"
  "v_mfma_f32_32x32x16_bf16 v[112:127], v[244:247], v[180:183], v[112:127]\n"
  "buffer_load_dwordx4 v[176:179], v11, s[36:39], s70 offen sc1\n"
  "buffer_load_dwordx4 v[180:183], v12, s[36:39], s70 offen sc1\n"
  "v_mfma_f32_32x32x16_bf16 v[64:79], v[248:251], v[184:187], v[64:79]\n"
  "s_waitcnt vmcnt(12)\n"
  "v_max3_u32 v13, v128, v130, v132\n"
  "v_mfma_f32_32x32x16_bf16 v[80:95], v[248:251], v[188:191], v[80:95]\n"
  "v_max_u32 v13, v13, v134\n"
  "v_cmp_eq_u32 vcc, -1, v13\n"
  "v_mfma_f32_32x32x16_bf16 v[96:111], v[252:255], v[184:187], v[96:111]\n"
  "s_cbranch_vccnz .Ls_p4_8\n"
  ".Lo_p4_8:\n"
  "s_add_u32 s71, s75, 3072\n"
  "v_mfma_f32_32x32x16_bf16 v[112:127], v[252:255], v[188:191], v[112:127]\n"
  "buffer_load_dwordx4 v[184:187], v11, s[36:39], s71 offen sc1\n"
  "buffer_load_dwordx4 v[188:191], v12, s[36:39], s71 offen sc1\n"
  "v_mfma_f32_32x32x16_bf16 v[64:79], v[16:19], v[128:131], v[64:79]\n"
  "s_waitcnt vmcnt(12)\n"
  "v_max3_u32 v13, v136, v138, v140\n"
  "v_mfma_f32_32x32x16_bf16 v[80:95], v[16:19], v[132:135], v[80:95]\n"
  "v_max_u32 v13, v13, v142\n"
  "v_cmp_eq_u32 vcc, -1, v13\n"
  "v_mfma_f32_32x32x16_bf16 v[96:111], v[20:23], v[128:131], v[96:111]\n"
  "s_cbranch_vccnz .Ls_p4_9\n"
  ".Lo_p4_9:\n"
  "s_add_u32 s64, s76, 0\n"
  "v_mfma_f32_32x32x16_bf16 v[112:127], v[20:23], v[132:135], v[112:127]\n"
  "buffer_load_dwordx4 v[128:131], v11, s[36:39], s64 offen sc1\n"
  "buffer_load_dwordx4 v[132:135], v12, s[36:39], s64 offen sc1\n"
  "v_mfma_f32_32x32x16_bf16 v[64:79], v[24:27], v[136:139], v[64:79]\n"
  "s_waitcnt vmcnt(12)\n"
  "v_max3_u32 v13, v144, v146, v148\n"
  "v_mfma_f32_32x32x16_bf16 v[80:95], v[24:27], v[140:143], v[80:95]\n"
  "v_max_u32 v13, v13, v150\n"
  "v_cmp_eq_u32 vcc, -1, v13\n"
  "v_mfma_f32_32x32x16_bf16 v[96:111], v[28:31], v[136:139], v[96:111]\n"
  "s_cbranch_vccnz .Ls_p4_10\n"
  ".Lo_p4_10:\n"
  "s_add_u32 s65, s76, 1024\n"
  "v_mfma_f32_32x32x16_bf16 v[112:127], v[28:31], v[140:143], v[112:127]\n"
  "buffer_load_dwordx4 v[136:139], v11, s[36:39], s65 offen sc1\n"
  "buffer_load_dwordx4 v[140:143], v12, s[36:39], s65 offen sc1\n"
  "v_mfma_f32_32x32x16_bf16 v[64:79], v[48:51], v[144:147], v[64:79]\n"
  "s_waitcnt vmcnt(12)\n"
  "v_max3_u32 v13, v152, v154, v156\n"
  "v_mfma_f32_32x32x16_bf16 v[80:95], v[48:51], v[148:151], v[80:95]\n"
  "v_max_u32 v13, v13, v158\n"
  "v_cmp_eq_u32 vcc, -1, v13\n"
  "v_mfma_f32_32x32x16_bf16 v[96:111], v[52:55], v[144:147], v[96:111]\n"
  "s_cbranch_vccnz .Ls_p4_11\n"
  ".Lo_p4_11:\n"
  "s_add_u32 s66, s76, 2048\n"
  "v_mfma_f32_32x32x16_bf16 v[112:127], v[52:55], v[148:151], v[112:127]\n"
  "buffer_load_dwordx4 v[144:147], v11, s[36:39], s66 offen sc1\n"
  "buffer_load_dwordx4 v[148:151], v12, s[36:39], s66 offen sc1\n"
  "v_mfma_f32_32x32x16_bf16 v[64:79], v[56:59], v[152:155], v[64:79]\n"
  "s_waitcnt vmcnt(12)\n"
  "ds_read_b64_tr_b16 v[48:49], v7 offset:24576\n"
  "ds_read_b64_tr_b16 v[50:51], v7 offset:25088\n"
  "v_max3_u32 v13, v160, v162, v164\n"
  "v_mfma_f32_32x32x16_bf16 v[80:95], v[56:59], v[156:159], v[80:95]\n"
  "ds_read_b64_tr_b16 v[52:53], v8 offset:24576\n"
  "ds_read_b64_tr_b16 v[54:55], v8 offset:25088\n"
  "v_max_u32 v13, v13, v166\n"
  "v_cmp_eq_u32 vcc, -1, v13\n"
  "v_mfma_f32_32x32x16_bf16 v[96:111], v[60:63], v[152:155], v[96:111]\n"
  "s_cbranch_vccnz .Ls_p4_12\n"
  ".Lo_p4_12:\n"
  "s_add_u32 s67, s76, 3072\n"
  "v_mfma_f32_32x32x16_bf16 v[112:127], v[60:63], v[156:159], v[112:127]\n"
  "buffer_load_dwordx4 v[152:155], v11, s[36:39], s67 offen sc1\n"
  "buffer_load_dwordx4 v[156:159], v12, s[36:39], s67 offen sc1\n"
  "s_waitcnt lgkmcnt(0)\n"
  "v_mfma_f32_32x32x16_bf16 v[64:79], v[48:51], v[160:163], v[64:79]\n"
  "s_waitcnt vmcnt(12)\n"
  "ds_read_b64_tr_b16 v[56:57], v7 offset:26624\n"
  "ds_read_b64_tr_b16 v[58:59], v7 offset:27136\n"
  "v_max3_u32 v13, v168, v170, v172\n"
  "v_mfma_f32_32x32x16_bf16 v[80:95], v[48:51], v[164:167], v[80:95]\n"
  "ds_read_b64_tr_b16 v[60:61], v8 offset:26624\n"
  "ds_read_b64_tr_b16 v[62:63], v8 offset:27136\n"
  "v_max_u32 v13, v13, v174\n"
  "v_cmp_eq_u32 vcc, -1, v13\n"
  "v_mfma_f32_32x32x16_bf16 v[96:111], v[52:55], v[160:163], v[96:111]\n"
  "s_cbranch_vccnz .Ls_p4_13\n"
  ".Lo_p4_13:\n"
  "s_add_u32 s68, s77, 0\n"
  "v_mfma_f32_32x32x16_bf16 v[112:127], v[52:55], v[164:167], v[112:127]\n"
  "buffer_load_dwordx4 v[160:163], v11, s[36:39], s68 offen sc1\n"
  "buffer_load_dwordx4 v[164:167], v12, s[36:39], s68 offen sc1\n"
  "s_waitcnt lgkmcnt(0)\n"
  "v_mfma_f32_32x32x16_bf16 v[64:79], v[56:59], v[168:171], v[64:79]\n"
  "s_waitcnt vmcnt(12)\n"
  "ds_read_b64_tr_b16 v[48:49], v7 offset:28672\n"
  "ds_read_b64_tr_b16 v[50:51], v7 offset:29184\n"
  "v_max3_u32 v13, v176, v178, v180\n"
  "v_mfma_f32_32x32x16_bf16 v[80:95], v[56:59], v[172:175], v[80:95]\n"
  "ds_read_b64_tr_b16 v[52:53], v8 offset:28672\n"
  "ds_read_b64_tr_b16 v[54:55], v8 offset:29184\n"
  "v_max_u32 v13, v13, v182\n"
  "v_cmp_eq_u32 vcc, -1, v13\n"
  "v_mfma_f32_32x32x16_bf16 v[96:111], v[60:63], v[168:171], v[96:111]\n"
  "s_cbranch_vccnz .Ls_p4_14\n"
  ".Lo_p4_14:\n"
  "s_add_u32 s69, s77, 1024\n"
  "v_mfma_f32_32x32x16_bf16 v[112:127], v[60:63], v[172:175], v[112:127]\n"
  "buffer_load_dwordx4 v[168:171], v11, s[36:39], s69 offen sc1\n"
  "buffer_load_dwordx4 v[172:175], v12, s[36:39], s69 offen sc1\n"
  "s_waitcnt lgkmcnt(0)\n"
  "v_mfma_f32_32x32x16_bf16 v[64:79], v[48:51], v[176:179], v[64:79]\n"
  "s_waitcnt vmcnt(12)\n"
  "ds_read_b64_tr_b16 v[56:57], v7 offset:30720\n"
  "ds_read_b64_tr_b16 v[58:59], v7 offset:31232\n"
  "v_max3_u32 v13, v184, v186, v188\n"
  "v_mfma_f32_32x32x16_bf16 v[80:95], v[48:51], v[180:183], v[80:95]\n"
  "ds_read_b64_tr_b16 v[60:61], v8 offset:30720\n"
  "ds_read_b64_tr_b16 v[62:63], v8 offset:31232\n"
  "v_max_u32 v13, v13, v190\n"
  "v_cmp_eq_u32 vcc, -1, v13\n"
  "v_mfma_f32_32x32x16_bf16 v[96:111], v[52:55], v[176:179], v[96:111]\n"
  "s_cbranch_vccnz .Ls_p4_15\n"
  ".Lo_p4_15:\n"
  "s_add_u32 s70, s77, 2048\n"
  "v_mfma_f32_32x32x16_bf16 v[112:127], v[52:55], v[180:183], v[112:127]\n"
  "buffer_load_dwordx4 v[176:179], v11, s[36:39], s70 offen sc1\n"
  "buffer_load_dwordx4 v[180:183], v12, s[36:39], s70 offen sc1\n"
  "s_waitcnt lgkmcnt(0)\n"
  "v_mfma_f32_32x32x16_bf16 v[64:79], v[56:59], v[184:187], v[64:79]\n"
  "s_waitcnt vmcnt(12)\n"
  "ds_read_b64_tr_b16 v[48:49], v7 offset:32768\n"
  "ds_read_b64_tr_b16 v[50:51], v7 offset:33280\n"
  "v_max3_u32 v13, v128, v130, v132\n"
  "v_mfma_f32_32x32x16_bf16 v[80:95], v[56:59], v[188:191], v[80:95]\n"
  "ds_read_b64_tr_b16 v[52:53], v8 offset:32768\n"
  "ds_read_b64_tr_b16 v[54:55], v8 offset:33280\n"
  "v_max_u32 v13, v13, v134\n"
  "v_cmp_eq_u32 vcc, -1, v13\n"
  "v_mfma_f32_32x32x16_bf16 v[96:111], v[60:63], v[184:187], v[96:111]\n"
  "s_cbranch_vccnz .Ls_p4_16\n"
  ".Lo_p4_16:\n"
  "s_add_u32 s71, s77, 3072\n"
  "v_mfma_f32_32x32x16_bf16 v[112:127], v[60:63], v[188:191], v[112:127]\n"
  "buffer_load_dwordx4 v[184:187], v11, s[36:39], s71 offen sc1\n"
  "buffer_load_dwordx4 v[188:191], v12, s[36:39], s71 offen sc1\n"
  "s_waitcnt lgkmcnt(0)\n"
  "v_mfma_f32_32x32x16_bf16 v[64:79], v[48:51], v[128:131], v[64:79]\n"
  "s_waitcnt vmcnt(12)\n"
  "ds_read_b64_tr_b16 v[56:57], v7 offset:34816\n"
  "ds_read_b64_tr_b16 v[58:59], v7 offset:35328\n"
  "v_max3_u32 v13, v136, v138, v140\n"
  "v_mfma_f32_32x32x16_bf16 v[80:95], v[48:51], v[132:135], v[80:95]\n"
  "ds_read_b64_tr_b16 v[60:61], v8 offset:34816\n"
  "ds_read_b64_tr_b16 v[62:63], v8 offset:35328\n"
  "v_max_u32 v13, v13, v142\n"
  "v_cmp_eq_u32 vcc, -1, v13\n"
  "v_mfma_f32_32x32x16_bf16 v[96:111], v[52:55], v[128:131], v[96:111]\n"
  "s_cbranch_vccnz .Ls_p4_17\n"
  ".Lo_p4_17:\n"
  "s_add_u32 s64, s78, 0\n"
  "v_mfma_f32_32x32x16_bf16 v[112:127], v[52:55], v[132:135], v[112:127]\n"
  "buffer_load_dwordx4 v[128:131], v11, s[36:39], s64 offen sc1\n"
  "buffer_load_dwordx4 v[132:135], v12, s[36:39], s64 offen sc1\n"
  "s_waitcnt lgkmcnt(0)\n"
  "v_mfma_f32_32x32x16_bf16 v[64:79], v[56:59], v[136:139], v[64:79]\n"
  "s_waitcnt vmcnt(12)\n"
  "ds_read_b64_tr_b16 v[48:49], v7 offset:36864\n"
  "ds_read_b64_tr_b16 v[50:51], v7 offset:37376\n"
  "v_max3_u32 v13, v144, v146, v148\n"
  "v_mfma_f32_32x32x16_bf16 v[80:95], v[56:59], v[140:143], v[80:95]\n"
  "ds_read_b64_tr_b16 v[52:53], v8 offset:36864\n"
  "ds_read_b64_tr_b16 v[54:55], v8 offset:37376\n"
  "v_max_u32 v13, v13, v150\n"
  "v_cmp_eq_u32 vcc, -1, v13\n"
  "v_mfma_f32_32x32x16_bf16 v[96:111], v[60:63], v[136:139], v[96:111]\n"
  "s_cbranch_vccnz .Ls_p4_18\n"
  ".Lo_p4_18:\n"
  "s_add_u32 s65, s78, 1024\n"
  "v_mfma_f32_32x32x16_bf16 v[112:127], v[60:63], v[140:143], v[112:127]\n"
  "buffer_load_dwordx4 v[136:139], v11, s[36:39], s65 offen sc1\n"
  "buffer_load_dwordx4 v[140:143], v12, s[36:39], s65 offen sc1\n"
  "s_waitcnt lgkmcnt(0)\n"
  "v_mfma_f32_32x32x16_bf16 v[64:79], v[48:51], v[144:147], v[64:79]\n"
  "s_waitcnt vmcnt(12)\n"
  "ds_read_b64_tr_b16 v[56:57], v7 offset:38912\n"
  "ds_read_b64_tr_b16 v[58:59], v7 offset:39424\n"
  "v_max3_u32 v13, v152, v154, v156\n"
  "v_mfma_f32_32x32x16_bf16 v[80:95], v[48:51], v[148:151], v[80:95]\n"
  "ds_read_b64_tr_b16 v[60:61], v8 offset:38912\n"
  "ds_read_b64_tr_b16 v[62:63], v8 offset:39424\n"
  "v_max_u32 v13, v13, v158\n"
  "v_cmp_eq_u32 vcc, -1, v13\n"
  "v_mfma_f32_32x32x16_bf16 v[96:111], v[52:55], v[144:147], v[96:111]\n"
  "s_cbranch_vccnz .Ls_p4_19\n"
  ".Lo_p4_19:\n"
  "s_add_u32 s66, s78, 2048\n"
  "v_mfma_f32_32x32x16_bf16 v[112:127], v[52:55], v[148:151], v[112:127]\n"
  "buffer_load_dwordx4 v[144:147], v11, s[36:39], s66 offen sc1\n"
  "buffer_load_dwordx4 v[148:151], v12, s[36:39], s66 offen sc1\n"
  "s_waitcnt lgkmcnt(0)\n"
  "v_mfma_f32_32x32x16_bf16 v[64:79], v[56:59], v[152:155], v[64:79]\n"
  "s_waitcnt vmcnt(12)\n"
  "ds_read_b64_tr_b16 v[48:49], v7 offset:0\n"
  "ds_read_b64_tr_b16 v[50:51], v7 offset:512\n"
  "v_max3_u32 v13, v160, v162, v164\n"
  "v_mfma_f32_32x32x16_bf16 v[80:95], v[56:59], v[156:159], v[80:95]\n"
  "ds_read_b64_tr_b16 v[52:53], v8 offset:0\n"
  "ds_read_b64_tr_b16 v[54:55], v8 offset:512\n"
  "v_max_u32 v13, v13, v166\n"
  "v_cmp_eq_u32 vcc, -1, v13\n"
  "v_mfma_f32_32x32x16_bf16 v[96:111], v[60:63], v[152:155], v[96:111]\n"
  "s_cbranch_vccnz .Ls_p4_20\n"
  ".Lo_p4_20:\n"
  "s_add_u32 s67, s78, 3072\n"
  "v_mfma_f32_32x32x16_bf16 v[112:127], v[60:63], v[156:159], v[112:127]\n"
  "buffer_load_dwordx4 v[152:155], v11, s[36:39], s67 offen sc1\n"
  "buffer_load_dwordx4 v[156:159], v12, s[36:39], s67 offen sc1\n"
  "s_waitcnt lgkmcnt(0)\n"
  "v_mfma_f32_32x32x16_bf16 v[64:79], v[48:51], v[160:163], v[64:79]\n"
  "s_waitcnt vmcnt(12)\n"
  "ds_read_b64_tr_b16 v[56:57], v7 offset:2048\n"
  "ds_read_b64_tr_b16 v[58:59], v7 offset:2560\n"
  "v_max3_u32 v13, v168, v170, v172\n"
  "v_mfma_f32_32x32x16_bf16 v[80:95], v[48:51], v[164:167], v[80:95]\n"
  "ds_read_b64_tr_b16 v[60:61], v8 offset:2048\n"
  "ds_read_b64_tr_b16 v[62:63], v8 offset:2560\n"
  "v_max_u32 v13, v13, v174\n"
  "v_cmp_eq_u32 vcc, -1, v13\n"
  "v_mfma_f32_32x32x16_bf16 v[96:111], v[52:55], v[160:163], v[96:111]\n"
  "s_cbranch_vccnz .Ls_p4_21\n"
  ".Lo_p4_21:\n"
  "s_add_u32 s68, s79, 0\n"
  "v_mfma_f32_32x32x16_bf16 v[112:127], v[52:55], v[164:167], v[112:127]\n"
  "buffer_load_dwordx4 v[160:163], v11, s[36:39], s68 offen sc1\n"
  "buffer_load_dwordx4 v[164:167], v12, s[36:39], s68 offen sc1\n"
  "s_waitcnt lgkmcnt(0)\n"
  "v_mfma_f32_32x32x16_bf16 v[64:79], v[56:59], v[168:171], v[64:79]\n"
  "s_waitcnt vmcnt(12)\n"
  "ds_read_b64_tr_b16 v[48:49], v7 offset:4096\n"
  "ds_read_b64_tr_b16 v[50:51], v7 offset:4608\n"
  "v_max3_u32 v13, v176, v178, v180\n"
  "v_mfma_f32_32x32x16_bf16 v[80:95], v[56:59], v[172:175], v[80:95]\n"
  "ds_read_b64_tr_b16 v[52:53], v8 offset:4096\n"
  "ds_read_b64_tr_b16 v[54:55], v8 offset:4608\n"
  "v_max_u32 v13, v13, v182\n"
  "v_cmp_eq_u32 vcc, -1, v13\n"
  "v_mfma_f32_32x32x16_bf16 v[96:111], v[60:63], v[168:171], v[96:111]\n"
  "s_cbranch_vccnz .Ls_p4_22\n"
  ".Lo_p4_22:\n"
  "s_add_u32 s69, s79, 1024\n"
  "v_mfma_f32_32x32x16_bf16 v[112:127], v[60:63], v[172:175], v[112:127]\n"
  "buffer_load_dwordx4 v[168:171], v11, s[36:39], s69 offen sc1\n"
  "buffer_load_dwordx4 v[172:175], v12, s[36:39], s69 offen sc1\n"
  "s_waitcnt lgkmcnt(0)\n"
  "v_mfma_f32_32x32x16_bf16 v[64:79], v[48:51], v[176:179], v[64:79]\n"
  "s_waitcnt vmcnt(12)\n"
  "ds_read_b64_tr_b16 v[56:57], v7 offset:6144\n"
  "ds_read_b64_tr_b16 v[58:59], v7 offset:6656\n"
  "v_max3_u32 v13, v184, v186, v188\n"
  "v_mfma_f32_32x32x16_bf16 v[80:95], v[48:51], v[180:183], v[80:95]\n"
  "ds_read_b64_tr_b16 v[60:61], v8 offset:6144\n"
  "ds_read_b64_tr_b16 v[62:63], v8 offset:6656\n"
  "v_max_u32 v13, v13, v190\n"
  "v_cmp_eq_u32 vcc, -1, v13\n"
  "v_mfma_f32_32x32x16_bf16 v[96:111], v[52:55], v[176:179], v[96:111]\n"
  "s_cbranch_vccnz .Ls_p4_23\n"
  ".Lo_p4_23:\n"
  "s_add_u32 s70, s79, 2048\n"
  "s_barrier\n"
  "v_mfma_f32_32x32x16_bf16 v[112:127], v[52:55], v[180:183], v[112:127]\n"
  "buffer_load_dwordx4 v[176:179], v11, s[36:39], s70 offen sc1\n"
  "buffer_load_dwordx4 v[180:183], v12, s[36:39], s70 offen sc1\n"
  "s_waitcnt lgkmcnt(0)\n"
  "v_mfma_f32_32x32x16_bf16 v[64:79], v[56:59], v[184:187], v[64:79]\n"
  "s_waitcnt vmcnt(12)\n"
  "ds_read_b64_tr_b16 v[48:49], v7 offset:8192\n"
  "ds_read_b64_tr_b16 v[50:51], v7 offset:8704\n"
  "v_max3_u32 v13, v128, v130, v132\n"
  "v_mfma_f32_32x32x16_bf16 v[80:95], v[56:59], v[188:191], v[80:95]\n"
  "ds_read_b64_tr_b16 v[52:53], v8 offset:8192\n"
  "ds_read_b64_tr_b16 v[54:55], v8 offset:8704\n"
  "v_max_u32 v13, v13, v134\n"
  "v_cmp_eq_u32 vcc, -1, v13\n"
  "v_mfma_f32_32x32x16_bf16 v[96:111], v[60:63], v[184:187], v[96:111]\n"
  "s_cbranch_vccnz .Ls_p4_24\n"
  ".Lo_p4_24:\n"
  "s_add_u32 s71, s79, 3072\n"
  "v_mfma_f32_32x32x16_bf16 v[112:127], v[60:63], v[188:191], v[112:127]\n"
  "buffer_load_dwordx4 v[184:187], v11, s[36:39], s71 offen sc1\n"
  "buffer_load_dwordx4 v[188:191], v12, s[36:39], s71 offen sc1\n"
  "s_waitcnt lgkmcnt(0)\n"
  "v_mfma_f32_32x32x16_bf16 v[64:79], v[48:51], v[128:131], v[64:79]\n"
  "s_waitcnt vmcnt(12)\n"
  "ds_read_b64_tr_b16 v[56:57], v7 offset:10240\n"
  "ds_read_b64_tr_b16 v[58:59], v7 offset:10752\n"
  "v_max3_u32 v13, v136, v138, v140\n"
  "v_mfma_f32_32x32x16_bf16 v[80:95], v[48:51], v[132:135], v[80:95]\n"
  "ds_read_b64_tr_b16 v[60:61], v8 offset:10240\n"
  "ds_read_b64_tr_b16 v[62:63], v8 offset:10752\n"
  "v_max_u32 v13, v13, v142\n"
  "v_cmp_eq_u32 vcc, -1, v13\n"
  "v_mfma_f32_32x32x16_bf16 v[96:111], v[52:55], v[128:131], v[96:111]\n"
  "s_cbranch_vccnz .Ls_p4_25\n"
  ".Lo_p4_25:\n"
  "v_mfma_f32_32x32x16_bf16 v[112:127], v[52:55], v[132:135], v[112:127]\n"
  "s_waitcnt lgkmcnt(0)\n"
  "v_mfma_f32_32x32x16_bf16 v[64:79], v[56:59], v[136:139], v[64:79]\n"
  "s_waitcnt vmcnt(10)\n"
  "ds_read_b64_tr_b16 v[48:49], v7 offset:12288\n"
  "ds_read_b64_tr_b16 v[50:51], v7 offset:12800\n"
  "v_max3_u32 v13, v144, v146, v148\n"
  "v_mfma_f32_32x32x16_bf16 v[80:95], v[56:59], v[140:143], v[80:95]\n"
  "ds_read_b64_tr_b16 v[52:53], v8 offset:12288\n"
  "ds_read_b64_tr_b16 v[54:55], v8 offset:12800\n"
  "v_max_u32 v13, v13, v150\n"
  "v_cmp_eq_u32 vcc, -1, v13\n"
  "v_mfma_f32_32x32x16_bf16 v[96:111], v[60:63], v[136:139], v[96:111]\n"
  "s_cbranch_vccnz .Ls_p4_26\n"
  ".Lo_p4_26:\n"
  "v_mfma_f32_32x32x16_bf16 v[112:127], v[60:63], v[140:143], v[112:127]\n"
  "s_waitcnt lgkmcnt(0)\n"
  "v_mfma_f32_32x32x16_bf16 v[64:79], v[48:51], v[144:147], v[64:79]\n"
  "s_waitcnt vmcnt(8)\n"
  "ds_read_b64_tr_b16 v[56:57], v7 offset:14336\n"
  "ds_read_b64_tr_b16 v[58:59], v7 offset:14848\n"
  "v_max3_u32 v13, v152, v154, v156\n"
  "v_mfma_f32_32x32x16_bf16 v[80:95], v[48:51], v[148:151], v[80:95]\n"
  "ds_read_b64_tr_b16 v[60:61], v8 offset:14336\n"
  "ds_read_b64_tr_b16 v[62:63], v8 offset:14848\n"
  "v_max_u32 v13, v13, v158\n"
  "v_cmp_eq_u32 vcc, -1, v13\n"
  "v_mfma_f32_32x32x16_bf16 v[96:111], v[52:55], v[144:147], v[96:111]\n"
  "s_cbranch_vccnz .Ls_p4_27\n"
  ".Lo_p4_27:\n"
  "v_mfma_f32_32x32x16_bf16 v[112:127], v[52:55], v[148:151], v[112:127]\n"
  "s_waitcnt lgkmcnt(0)\n"
  "v_mfma_f32_32x32x16_bf16 v[64:79], v[56:59], v[152:155], v[64:79]\n"
  "s_waitcnt vmcnt(6)\n"
  "ds_read_b64_tr_b16 v[48:49], v7 offset:16384\n"
  "ds_read_b64_tr_b16 v[50:51], v7 offset:16896\n"
  "v_max3_u32 v13, v160, v162, v164\n"
  "v_mfma_f32_32x32x16_bf16 v[80:95], v[56:59], v[156:159], v[80:95]\n"
  "ds_read_b64_tr_b16 v[52:53], v8 offset:16384\n"
  "ds_read_b64_tr_b16 v[54:55], v8 offset:16896\n"
  "v_max_u32 v13, v13, v166\n"
  "v_cmp_eq_u32 vcc, -1, v13\n"
  "v_mfma_f32_32x32x16_bf16 v[96:111], v[60:63], v[152:155], v[96:111]\n"
  "s_cbranch_vccnz .Ls_p4_28\n"
  ".Lo_p4_28:\n"
  "v_mfma_f32_32x32x16_bf16 v[112:127], v[60:63], v[156:159], v[112:127]\n"
  "s_waitcnt lgkmcnt(0)\n"
  "v_mfma_f32_32x32x16_bf16 v[64:79], v[48:51], v[160:163], v[64:79]\n"
  "s_waitcnt vmcnt(4)\n"
  "ds_read_b64_tr_b16 v[56:57], v7 offset:18432\n"
  "ds_read_b64_tr_b16 v[58:59], v7 offset:18944\n"
  "v_max3_u32 v13, v168, v170, v172\n"
  "v_mfma_f32_32x32x16_bf16 v[80:95], v[48:51], v[164:167], v[80:95]\n"
  "ds_read_b64_tr_b16 v[60:61], v8 offset:18432\n"
  "ds_read_b64_tr_b16 v[62:63], v8 offset:18944\n"
  "v_max_u32 v13, v13, v174\n"
  "v_cmp_eq_u32 vcc, -1, v13\n"
  "v_mfma_f32_32x32x16_bf16 v[96:111], v[52:55], v[160:163], v[96:111]\n"
  "s_cbranch_vccnz .Ls_p4_29\n"
  ".Lo_p4_29:\n"
  "v_mfma_f32_32x32x16_bf16 v[112:127], v[52:55], v[164:167], v[112:127]\n"
  "s_waitcnt lgkmcnt(0)\n"
  "v_mfma_f32_32x32x16_bf16 v[64:79], v[56:59], v[168:171], v[64:79]\n"
  "s_waitcnt vmcnt(2)\n"
  "ds_read_b64_tr_b16 v[48:49], v7 offset:20480\n"
  "ds_read_b64_tr_b16 v[50:51], v7 offset:20992\n"
  "v_max3_u32 v13, v176, v178, v180\n"
  "v_mfma_f32_32x32x16_bf16 v[80:95], v[56:59], v[172:175], v[80:95]\n"
  "ds_read_b64_tr_b16 v[52:53], v8 offset:20480\n"
  "ds_read_b64_tr_b16 v[54:55], v8 offset:20992\n"
  "v_max_u32 v13, v13, v182\n"
  "v_cmp_eq_u32 vcc, -1, v13\n"
  "v_mfma_f32_32x32x16_bf16 v[96:111], v[60:63], v[168:171], v[96:111]\n"
  "s_cbranch_vccnz .Ls_p4_30\n"
  ".Lo_p4_30:\n"
  "v_mfma_f32_32x32x16_bf16 v[112:127], v[60:63], v[172:175], v[112:127]\n"
  "s_waitcnt lgkmcnt(0)\n"
  "v_mfma_f32_32x32x16_bf16 v[80:95], v[48:51], v[180:183], v[80:95]\n"
  "s_waitcnt vmcnt(0)\n"
  "ds_read_b64_tr_b16 v[56:57], v7 offset:22528\n"
  "ds_read_b64_tr_b16 v[58:59], v7 offset:23040\n"
  "v_max3_u32 v13, v184, v186, v188\n"
  "v_mfma_f32_32x32x16_bf16 v[96:111], v[52:55], v[176:179], v[96:111]\n"
  "ds_read_b64_tr_b16 v[60:61], v8 offset:22528\n"
  "ds_read_b64_tr_b16 v[62:63], v8 offset:23040\n"
  "v_max_u32 v13, v13, v190\n"
  "v_cmp_eq_u32 vcc, -1, v13\n"
  "v_mfma_f32_32x32x16_bf16 v[112:127], v[52:55], v[180:183], v[112:127]\n"
  "s_cbranch_vccnz .Ls_p4_31\n"
  ".Lo_p4_31:\n"
  "s_waitcnt lgkmcnt(0)\n"
  "v_mfma_f32_32x32x16_bf16 v[80:95], v[56:59], v[188:191], v[80:95]\n"
  "v_mfma_f32_32x32x16_bf16 v[96:111], v[60:63], v[184:187], v[96:111]\n"
  "v_mfma_f32_32x32x16_bf16 v[112:127], v[60:63], v[188:191], v[112:127]\n"
  "s_nop 9\n"
  "ds_write_b128 v6, v[80:83] offset:12288\n"
  "v_mfma_f32_32x32x16_bf16 v[64:79], v[48:51], v[176:179], v[64:79]\n"
  "ds_write_b128 v6, v[84:87] offset:13312\n"
  "ds_write_b128 v6, v[88:91] offset:14336\n"
  "ds_write_b128 v6, v[92:95] offset:15360\n"
  "ds_write_b128 v6, v[96:99] offset:16384\n"
  "v_mfma_f32_32x32x16_bf16 v[64:79], v[56:59], v[184:187], v[64:79]\n"
  "ds_write_b128 v6, v[100:103] offset:17408\n"
  "ds_write_b128 v6, v[104:107] offset:18432\n"
  "ds_write_b128 v6, v[108:111] offset:19456\n"
  "ds_write_b128 v6, v[112:115] offset:20480\n"
  "ds_write_b128 v6, v[116:119] offset:21504\n"
  "ds_write_b128 v6, v[120:123] offset:22528\n"
  "ds_write_b128 v6, v[124:127] offset:23552\n"
  "v_mov_b32 v16, 0xbdd2d3e8\n"
  "v_mov_b32 v18, 0xc0135761\n"
  "s_waitcnt lgkmcnt(0)\n"
  "s_barrier\n"
  "ds_read_b128 v[128:131], v3 offset:12288\n"
  "ds_read_b128 v[144:147], v4 offset:16384\n"
  "ds_read_b128 v[160:163], v5 offset:20480\n"
  "ds_read_b128 v[132:135], v3 offset:13312\n"
  "ds_read_b128 v[148:151], v4 offset:17408\n"
  "ds_read_b128 v[164:167], v5 offset:21504\n"
  "ds_read_b128 v[136:139], v3 offset:14336\n"
  "ds_read_b128 v[152:155], v4 offset:18432\n"
  "ds_read_b128 v[168:171], v5 offset:22528\n"
  "ds_read_b128 v[140:143], v3 offset:15360\n"
  "ds_read_b128 v[156:159], v4 offset:19456\n"
  "ds_read_b128 v[172:175], v5 offset:23552\n"
  "s_waitcnt lgkmcnt(10)\n"
  "v_pk_add_f32 v[128:129], v[128:129], v[144:145]\n"
  "v_pk_add_f32 v[130:131], v[130:131], v[146:147]\n"
  "s_waitcnt lgkmcnt(9)\n"
  "v_pk_add_f32 v[64:65], v[64:65], v[160:161]\n"
  "v_pk_add_f32 v[66:67], v[66:67], v[162:163]\n"
  "v_pk_add_f32 v[64:65], v[64:65], v[128:129]\n"
  "v_pk_add_f32 v[66:67], v[66:67], v[130:131]\n"
  "s_waitcnt lgkmcnt(7)\n"
  "v_pk_add_f32 v[132:133], v[132:133], v[148:149]\n"
  "v_pk_add_f32 v[134:135], v[134:135], v[150:151]\n"
  "s_waitcnt lgkmcnt(6)\n"
  "v_pk_add_f32 v[68:69], v[68:69], v[164:165]\n"
  "v_pk_add_f32 v[70:71], v[70:71], v[166:167]\n"
  "v_pk_add_f32 v[68:69], v[68:69], v[132:133]\n"
  "v_pk_add_f32 v[70:71], v[70:71], v[134:135]\n"
  "v_pk_mul_f32 v[128:129], v[64:65], v[64:65]\n"
  "v_pk_mul_f32 v[130:131], v[66:67], v[66:67]\n"
  "v_pk_mul_f32 v[132:133], v[68:69], v[68:69]\n"
  "v_pk_mul_f32 v[134:135], v[70:71], v[70:71]\n"
  "v_pk_fma_f32 v[128:129], v[128:129], v[16:17], v[18:19] op_sel_hi:[1,0,0]\n"
  "v_pk_fma_f32 v[130:131], v[130:131], v[16:17], v[18:19] op_sel_hi:[1,0,0]\n"
  "v_pk_fma_f32 v[132:133], v[132:133], v[16:17], v[18:19] op_sel_hi:[1,0,0]\n"
  "v_pk_fma_f32 v[134:135], v[134:135], v[16:17], v[18:19] op_sel_hi:[1,0,0]\n"
  "v_pk_mul_f32 v[128:129], v[128:129], v[64:65]\n"
  "v_pk_mul_f32 v[130:131], v[130:131], v[66:67]\n"
  "v_pk_mul_f32 v[132:133], v[132:133], v[68:69]\n"
  "v_pk_mul_f32 v[134:135], v[134:135], v[70:71]\n"
  "v_exp_f32 v128, v128\n"
  "v_exp_f32 v129, v129\n"
  "v_exp_f32 v130, v130\n"
  "v_exp_f32 v131, v131\n"
  "v_exp_f32 v132, v132\n"
  "v_exp_f32 v133, v133\n"
  "v_exp_f32 v134, v134\n"
  "v_exp_f32 v135, v135\n"
  "v_pk_add_f32 v[128:129], v[128:129], 1.0 op_sel_hi:[1,0]\n"
  "v_pk_add_f32 v[130:131], v[130:131], 1.0 op_sel_hi:[1,0]\n"
  "v_pk_add_f32 v[132:133], v[132:133], 1.0 op_sel_hi:[1,0]\n"
  "v_pk_add_f32 v[134:135], v[134:135], 1.0 op_sel_hi:[1,0]\n"
  "v_rcp_f32 v128, v128\n"
  "v_rcp_f32 v129, v129\n"
  "v_rcp_f32 v130, v130\n"
  "v_rcp_f32 v131, v131\n"
  "v_rcp_f32 v132, v132\n"
  "v_rcp_f32 v133, v133\n"
  "v_rcp_f32 v134, v134\n"
  "v_rcp_f32 v135, v135\n"
  "v_pk_mul_f32 v[128:129], v[128:129], v[64:65]\n"
  "v_pk_mul_f32 v[130:131], v[130:131], v[66:67]\n"
  "v_pk_mul_f32 v[132:133], v[132:133], v[68:69]\n"
  "v_pk_mul_f32 v[134:135], v[134:135], v[70:71]\n"
  "v_cvt_pk_bf16_f32 v176, v128, v129\n"
  "v_cvt_pk_bf16_f32 v177, v130, v131\n"
  "v_cvt_pk_bf16_f32 v178, v132, v133\n"
  "v_cvt_pk_bf16_f32 v179, v134, v135\n"
  "buffer_store_dwordx4 v[176:179], v2, s[36:39], s24 offen offset:0 sc1\n"
  "s_waitcnt lgkmcnt(4)\n"
  "v_pk_add_f32 v[136:137], v[136:137], v[152:153]\n"
  "v_pk_add_f32 v[138:139], v[138:139], v[154:155]\n"
  "s_waitcnt lgkmcnt(3)\n"
  "v_pk_add_f32 v[72:73], v[72:73], v[168:169]\n"
  "v_pk_add_f32 v[74:75], v[74:75], v[170:171]\n"
  "v_pk_add_f32 v[72:73], v[72:73], v[136:137]\n"
  "v_pk_add_f32 v[74:75], v[74:75], v[138:139]\n"
  "s_waitcnt lgkmcnt(1)\n"
  "v_pk_add_f32 v[140:141], v[140:141], v[156:157]\n"
  "v_pk_add_f32 v[142:143], v[142:143], v[158:159]\n"
  "s_waitcnt lgkmcnt(0)\n"
  "s_barrier\n"
  "v_pk_add_f32 v[76:77], v[76:77], v[172:173]\n"
  "v_pk_add_f32 v[78:79], v[78:79], v[174:175]\n"
  "v_pk_add_f32 v[76:77], v[76:77], v[140:141]\n"
  "v_pk_add_f32 v[78:79], v[78:79], v[142:143]\n"
  "v_pk_mul_f32 v[136:137], v[72:73], v[72:73]\n"
  "v_pk_mul_f32 v[138:139], v[74:75], v[74:75]\n"
  "v_pk_mul_f32 v[140:141], v[76:77], v[76:77]\n"
  "v_pk_mul_f32 v[142:143], v[78:79], v[78:79]\n"
  "v_pk_fma_f32 v[136:137], v[136:137], v[16:17], v[18:19] op_sel_hi:[1,0,0]\n"
  "v_pk_fma_f32 v[138:139], v[138:139], v[16:17], v[18:19] op_sel_hi:[1,0,0]\n"
  "v_pk_fma_f32 v[140:141], v[140:141], v[16:17], v[18:19] op_sel_hi:[1,0,0]\n"
  "v_pk_fma_f32 v[142:143], v[142:143], v[16:17], v[18:19] op_sel_hi:[1,0,0]\n"
  "v_pk_mul_f32 v[136:137], v[136:137], v[72:73]\n"
  "v_pk_mul_f32 v[138:139], v[138:139], v[74:75]\n"
  "v_pk_mul_f32 v[140:141], v[140:141], v[76:77]\n"
  "v_pk_mul_f32 v[142:143], v[142:143], v[78:79]\n"
  "v_exp_f32 v136, v136\n"
  "v_exp_f32 v137, v137\n"
  "v_exp_f32 v138, v138\n"
  "v_exp_f32 v139, v139\n"
  "v_exp_f32 v140, v140\n"
  "v_exp_f32 v141, v141\n"
  "v_exp_f32 v142, v142\n"
  "v_exp_f32 v143, v143\n"
  "v_pk_add_f32 v[136:137], v[136:137], 1.0 op_sel_hi:[1,0]\n"
  "v_pk_add_f32 v[138:139], v[138:139], 1.0 op_sel_hi:[1,0]\n"
  "v_pk_add_f32 v[140:141], v[140:141], 1.0 op_sel_hi:[1,0]\n"
  "v_pk_add_f32 v[142:143], v[142:143], 1.0 op_sel_hi:[1,0]\n"
  "v_rcp_f32 v136, v136\n"
  "v_rcp_f32 v137, v137\n"
  "v_rcp_f32 v138, v138\n"
  "v_rcp_f32 v139, v139\n"
  "v_rcp_f32 v140, v140\n"
  "v_rcp_f32 v141, v141\n"
  "v_rcp_f32 v142, v142\n"
  "v_rcp_f32 v143, v143\n"
  "v_pk_mul_f32 v[136:137], v[136:137], v[72:73]\n"
  "v_pk_mul_f32 v[138:139], v[138:139], v[74:75]\n"
  "v_pk_mul_f32 v[140:141], v[140:141], v[76:77]\n"
  "v_pk_mul_f32 v[142:143], v[142:143], v[78:79]\n"
  "v_cvt_pk_bf16_f32 v180, v136, v137\n"
  "v_cvt_pk_bf16_f32 v181, v138, v139\n"
  "v_cvt_pk_bf16_f32 v182, v140, v141\n"
  "v_cvt_pk_bf16_f32 v183, v142, v143\n"
  "buffer_store_dwordx4 v[180:183], v2, s[36:39], s24 offen offset:1024 sc1\n"
  "ds_read_b64_tr_b16 v[192:193], v38 offset:24576\n"
  "ds_read_b64_tr_b16 v[194:195], v38 offset:25088\n"
  "ds_read_b64_tr_b16 v[196:197], v39 offset:24576\n"
  "ds_read_b64_tr_b16 v[198:199], v39 offset:25088\n"
  "ds_read_b64_tr_b16 v[200:201], v38 offset:26624\n"
  "ds_read_b64_tr_b16 v[202:203], v38 offset:27136\n"
  "ds_read_b64_tr_b16 v[204:205], v39 offset:26624\n"
  "ds_read_b64_tr_b16 v[206:207], v39 offset:27136\n"
  "ds_read_b64_tr_b16 v[208:209], v38 offset:28672\n"
  "ds_read_b64_tr_b16 v[210:211], v38 offset:29184\n"
  "ds_read_b64_tr_b16 v[212:213], v39 offset:28672\n"
  "ds_read_b64_tr_b16 v[214:215], v39 offset:29184\n"
  "ds_read_b64_tr_b16 v[216:217], v38 offset:30720\n"
  "ds_read_b64_tr_b16 v[218:219], v38 offset:31232\n"
  "ds_read_b64_tr_b16 v[220:221], v39 offset:30720\n"
  "ds_read_b64_tr_b16 v[222:223], v39 offset:31232\n"
  "ds_read_b64_tr_b16 v[224:225], v38 offset:32768\n"
  "ds_read_b64_tr_b16 v[226:227], v38 offset:33280\n"
  "ds_read_b64_tr_b16 v[228:229], v39 offset:32768\n"
  "ds_read_b64_tr_b16 v[230:231], v39 offset:33280\n"
  "ds_read_b64_tr_b16 v[232:233], v38 offset:34816\n"
  "ds_read_b64_tr_b16 v[234:235], v38 offset:35328\n"
  "ds_read_b64_tr_b16 v[236:237], v39 offset:34816\n"
  "ds_read_b64_tr_b16 v[238:239], v39 offset:35328\n"
  "ds_read_b64_tr_b16 v[240:241], v38 offset:36864\n"
  "ds_read_b64_tr_b16 v[242:243], v38 offset:37376\n"
  "ds_read_b64_tr_b16 v[244:245], v39 offset:36864\n"
  "ds_read_b64_tr_b16 v[246:247], v39 offset:37376\n"
  "ds_read_b64_tr_b16 v[248:249], v38 offset:38912\n"
  "ds_read_b64_tr_b16 v[250:251], v38 offset:39424\n"
  "ds_read_b64_tr_b16 v[252:253], v39 offset:38912\n"
  "ds_read_b64_tr_b16 v[254:255], v39 offset:39424\n"
  "s_waitcnt lgkmcnt(0)\n"
  "s_barrier\n"
  ".Lb_p5b:\n"
  "s_lshl_b32 s53, s87, 1\n"
  "s_add_u32 s93, s53, s92\n"
  "s_lshl_b32 s53, s88, 1\n"
  "s_add_u32 s94, s53, s91\n"
  "s_mul_i32 s53, s93, 114688\n"
  "v_add_u32 v11, s53, v2\n"
  "s_xor_b32 s54, s93, 1\n"
  "s_mul_i32 s53, s54, 114688\n"
  "v_add_u32 v12, s53, v2\n"
  "s_mul_i32 s53, s48, 28672\n"
  "s_add_u32 s53, s53, 10452992\n"
  "s_add_u32 s72, s53, 0\n"
  "s_add_u32 s73, s53, 4096\n"
  "s_add_u32 s74, s53, 8192\n"
  "s_add_u32 s75, s53, 12288\n"
  "s_add_u32 s76, s53, 16384\n"
  "s_add_u32 s77, s53, 20480\n"
  "s_add_u32 s78, s53, 24576\n"
  "s_add_u32 s64, s72, 0\n"
  "s_add_u32 s65, s72, 1024\n"
  "s_add_u32 s66, s72, 2048\n"
  "s_add_u32 s67, s72, 3072\n"
  "s_add_u32 s68, s73, 0\n"
  "s_add_u32 s69, s73, 1024\n"
  "s_add_u32 s70, s73, 2048\n"
  "s_add_u32 s71, s73, 3072\n"
  "s_sleep 8\n"
  "buffer_load_dwordx4 v[128:131], v11, s[36:39], s64 offen sc1\n"
  "buffer_load_dwordx4 v[132:135], v12, s[36:39], s64 offen sc1\n"
  "buffer_load_dwordx4 v[136:139], v11, s[36:39], s65 offen sc1\n"
  "buffer_load_dwordx4 v[140:143], v12, s[36:39], s65 offen sc1\n"
  "buffer_load_dwordx4 v[144:147], v11, s[36:39], s66 offen sc1\n"
  "buffer_load_dwordx4 v[148:151], v12, s[36:39], s66 offen sc1\n"
  "buffer_load_dwordx4 v[152:155], v11, s[36:39], s67 offen sc1\n"
  "buffer_load_dwordx4 v[156:159], v12, s[36:39], s67 offen sc1\n"
  "buffer_load_dwordx4 v[160:163], v11, s[36:39], s68 offen sc1\n"
  "buffer_load_dwordx4 v[164:167], v12, s[36:39], s68 offen sc1\n"
  "buffer_load_dwordx4 v[168:171], v11, s[36:39], s69 offen sc1\n"
  "buffer_load_dwordx4 v[172:175], v12, s[36:39], s69 offen sc1\n"
  "buffer_load_dwordx4 v[176:179], v11, s[36:39], s70 offen sc1\n"
  "buffer_load_dwordx4 v[180:183], v12, s[36:39], s70 offen sc1\n"
  "buffer_load_dwordx4 v[184:187], v11, s[36:39], s71 offen sc1\n"
  "buffer_load_dwordx4 v[188:191], v12, s[36:39], s71 offen sc1\n"
  "s_lshl_b32 s53, s93, 6\n"
  "s_add_u32 s53, s53, s94\n"
  "s_lshl_b32 s53, s53, 11\n"
  "s_add_u32 s23, s53, 12582912\n"
  "s_mul_i32 s53, s94, 28672\n"
  "s_lshl_b32 s54, s93, 6\n"
  "s_add_u32 s21, s53, s54\n"
  "v_lshlrev_b32 v64, 16, v33\n"
  "v_mov_b32 v65, v64\n"
  "v_mov_b32 v66, v64\n"
  "v_mov_b32 v67, v64\n"
  "v_mov_b32 v68, v64\n"
  "v_mov_b32 v69, v64\n"
  "v_mov_b32 v70, v64\n"
  "v_mov_b32 v71, v64\n"
  "v_mov_b32 v72, v64\n"
  "v_mov_b32 v73, v64\n"
  "v_mov_b32 v74, v64\n"
  "v_mov_b32 v75, v64\n"
  "v_mov_b32 v76, v64\n"
  "v_mov_b32 v77, v64\n"
  "v_mov_b32 v78, v64\n"
  "v_mov_b32 v79, v64\n"
  "s_getpc_b64 s[80:81]\n"
  ".Lpc_p5:\n"
  "s_add_u32 s80, s80, .Lh_ring-.Lpc_p5\n"
  "s_addc_u32 s81, s81, 0\n"
  "s_waitcnt vmcnt(14)\n"
  "v_max3_u32 v13, v128, v130, v132\n"
  "v_max_u32 v13, v13, v134\n"
  "v_cmp_eq_u32 vcc, -1, v13\n"
  "s_cbranch_vccnz .Ls_p5_0\n"
  ".Lo_p5_0:\n"
  "v_mfma_f32_32x32x16_bf16 v[64:79], v[128:131], v[192:195], v[64:79]\n"
  "s_waitcnt vmcnt(12)\n"
  "v_max3_u32 v13, v136, v138, v140\n"
  "v_mfma_f32_32x32x16_bf16 v[80:95], v[132:135], v[192:195], 0\n"
  "v_max_u32 v13, v13, v142\n"
  "v_cmp_eq_u32 vcc, -1, v13\n"
  "v_mfma_f32_32x32x16_bf16 v[96:111], v[128:131], v[196:199], 0\n"
  "s_cbranch_vccnz .Ls_p5_1\n"
  ".Lo_p5_1:\n"
  "s_add_u32 s64, s74, 0\n"
  "v_mfma_f32_32x32x16_bf16 v[112:127], v[132:135], v[196:199], 0\n"
  "buffer_load_dwordx4 v[128:131], v11, s[36:39], s64 offen sc1\n"
  "buffer_load_dwordx4 v[132:135], v12, s[36:39], s64 offen sc1\n"
  "v_mfma_f32_32x32x16_bf16 v[64:79], v[136:139], v[200:203], v[64:79]\n"
  "s_waitcnt vmcnt(12)\n"
  "v_max3_u32 v13, v144, v146, v148\n"
  "v_mfma_f32_32x32x16_bf16 v[80:95], v[140:143], v[200:203], v[80:95]\n"
  "v_max_u32 v13, v13, v150\n"
  "v_cmp_eq_u32 vcc, -1, v13\n"
  "v_mfma_f32_32x32x16_bf16 v[96:111], v[136:139], v[204:207], v[96:111]\n"
  "s_cbranch_vccnz .Ls_p5_2\n"
  ".Lo_p5_2:\n"
  "s_add_u32 s65, s74, 1024\n"
  "v_mfma_f32_32x32x16_bf16 v[112:127], v[140:143], v[204:207], v[112:127]\n"
  "buffer_load_dwordx4 v[136:139], v11, s[36:39], s65 offen sc1\n"
  "buffer_load_dwordx4 v[140:143], v12, s[36:39], s65 offen sc1\n"
  "v_mfma_f32_32x32x16_bf16 v[64:79], v[144:147], v[208:211], v[64:79]\n"
  "s_waitcnt vmcnt(12)\n"
  "v_max3_u32 v13, v152, v154, v156\n"
  "v_mfma_f32_32x32x16_bf16 v[80:95], v[148:151], v[208:211], v[80:95]\n"
  "v_max_u32 v13, v13, v158\n"
  "v_cmp_eq_u32 vcc, -1, v13\n"
  "v_mfma_f32_32x32x16_bf16 v[96:111], v[144:147], v[212:215], v[96:111]\n"
  "s_cbranch_vccnz .Ls_p5_3\n"
  ".Lo_p5_3:\n"
  "s_add_u32 s66, s74, 2048\n"
  "v_mfma_f32_32x32x16_bf16 v[112:127], v[148:151], v[212:215], v[112:127]\n"
  "buffer_load_dwordx4 v[144:147], v11, s[36:39], s66 offen sc1\n"
  "buffer_load_dwordx4 v[148:151], v12, s[36:39], s66 offen sc1\n"
  "v_mfma_f32_32x32x16_bf16 v[64:79], v[152:155], v[216:219], v[64:79]\n"
  "s_waitcnt vmcnt(12)\n"
  "v_max3_u32 v13, v160, v162, v164\n"
  "v_mfma_f32_32x32x16_bf16 v[80:95], v[156:159], v[216:219], v[80:95]\n"
  "v_max_u32 v13, v13, v166\n"
  "v_cmp_eq_u32 vcc, -1, v13\n"
  "v_mfma_f32_32x32x16_bf16 v[96:111], v[152:155], v[220:223], v[96:111]\n"
  "s_cbranch_vccnz .Ls_p5_4\n"
  ".Lo_p5_4:\n"
  "s_add_u32 s67, s74, 3072\n"
  "v_mfma_f32_32x32x16_bf16 v[112:127], v[156:159], v[220:223], v[112:127]\n"
  "buffer_load_dwordx4 v[152:155], v11, s[36:39], s67 offen sc1\n"
  "buffer_load_dwordx4 v[156:159], v12, s[36:39], s67 offen sc1\n"
  "v_mfma_f32_32x32x16_bf16 v[64:79], v[160:163], v[224:227], v[64:79]\n"
  "s_waitcnt vmcnt(12)\n"
  "v_max3_u32 v13, v168, v170, v172\n"
  "v_mfma_f32_32x32x16_bf16 v[80:95], v[164:167], v[224:227], v[80:95]\n"
  "v_max_u32 v13, v13, v174\n"
  "v_cmp_eq_u32 vcc, -1, v13\n"
  "v_mfma_f32_32x32x16_bf16 v[96:111], v[160:163], v[228:231], v[96:111]\n"
  "s_cbranch_vccnz .Ls_p5_5\n"
  ".Lo_p5_5:\n"
  "s_add_u32 s68, s75, 0\n"
  "v_mfma_f32_32x32x16_bf16 v[112:127], v[164:167], v[228:231], v[112:127]\n"
  "buffer_load_dwordx4 v[160:163], v11, s[36:39], s68 offen sc1\n"
  "buffer_load_dwordx4 v[164:167], v12, s[36:39], s68 offen sc1\n"
  "v_mfma_f32_32x32x16_bf16 v[64:79], v[168:171], v[232:235], v[64:79]\n"
  "s_waitcnt vmcnt(12)\n"
  "v_max3_u32 v13, v176, v178, v180\n"
  "v_mfma_f32_32x32x16_bf16 v[80:95], v[172:175], v[232:235], v[80:95]\n"
  "v_max_u32 v13, v13, v182\n"
  "v_cmp_eq_u32 vcc, -1, v13\n"
  "v_mfma_f32_32x32x16_bf16 v[96:111], v[168:171], v[236:239], v[96:111]\n"
  "s_cbranch_vccnz .Ls_p5_6\n"
  ".Lo_p5_6:\n"
  "s_add_u32 s69, s75, 1024\n"
  "s_barrier\n"
  "v_mfma_f32_32x32x16_bf16 v[112:127], v[172:175], v[236:239], v[112:127]\n"
  "buffer_load_dwordx4 v[168:171], v11, s[36:39], s69 offen sc1\n"
  "buffer_load_dwordx4 v[172:175], v12, s[36:39], s69 offen sc1\n"
  "v_mfma_f32_32x32x16_bf16 v[64:79], v[176:179], v[240:243], v[64:79]\n"
  "s_waitcnt vmcnt(12)\n"
  "v_max3_u32 v13, v184, v186, v188\n"
  "v_mfma_f32_32x32x16_bf16 v[80:95], v[180:183], v[240:243], v[80:95]\n"
  "v_max_u32 v13, v13, v190\n"
  "v_cmp_eq_u32 vcc, -1, v13\n"
  "v_mfma_f32_32x32x16_bf16 v[96:111], v[176:179], v[244:247], v[96:111]\n"
  "s_cbranch_vccnz .Ls_p5_7\n"
  ".Lo_p5_7:\n"
  "s_add_u32 s70, s75, 2048\n"
  "v_mfma_f32_32x32x16_bf16 v[112:127], v[180:183], v[244:247], v[112:127]\n"
  "buffer_load_dwordx4 v[176:179], v11, s[36:39], s70 offen sc1\n"
  "buffer_load_dwordx4 v[180:183], v12, s[36:39], s70 offen sc1\n"
  "v_mfma_f32_32x32x16_bf16 v[64:79], v[184:187], v[248:251], v[64:79]\n"
  "s_waitcnt vmcnt(12)\n"
  "ds_read_b64_tr_b16 v[48:49], v38 offset:0\n"
  "ds_read_b64_tr_b16 v[50:51], v38 offset:512\n"
  "v_max3_u32 v13, v128, v130, v132\n"
  "v_mfma_f32_32x32x16_bf16 v[80:95], v[188:191], v[248:251], v[80:95]\n"
  "ds_read_b64_tr_b16 v[52:53], v39 offset:0\n"
  "ds_read_b64_tr_b16 v[54:55], v39 offset:512\n"
  "v_max_u32 v13, v13, v134\n"
  "v_cmp_eq_u32 vcc, -1, v13\n"
  "v_mfma_f32_32x32x16_bf16 v[96:111], v[184:187], v[252:255], v[96:111]\n"
  "s_cbranch_vccnz .Ls_p5_8\n"
  ".Lo_p5_8:\n"
  "s_add_u32 s71, s75, 3072\n"
  "v_mfma_f32_32x32x16_bf16 v[112:127], v[188:191], v[252:255], v[112:127]\n"
  "buffer_load_dwordx4 v[184:187], v11, s[36:39], s71 offen sc1\n"
  "buffer_load_dwordx4 v[188:191], v12, s[36:39], s71 offen sc1\n"
  "s_waitcnt lgkmcnt(0)\n"
  "v_mfma_f32_32x32x16_bf16 v[64:79], v[128:131], v[48:51], v[64:79]\n"
  "s_waitcnt vmcnt(12)\n"
  "ds_read_b64_tr_b16 v[56:57], v38 offset:2048\n"
  "ds_read_b64_tr_b16 v[58:59], v38 offset:2560\n"
  "v_max3_u32 v13, v136, v138, v140\n"
  "v_mfma_f32_32x32x16_bf16 v[80:95], v[132:135], v[48:51], v[80:95]\n"
  "ds_read_b64_tr_b16 v[60:61], v39 offset:2048\n"
  "ds_read_b64_tr_b16 v[62:63], v39 offset:2560\n"
  "v_max_u32 v13, v13, v142\n"
  "v_cmp_eq_u32 vcc, -1, v13\n"
  "v_mfma_f32_32x32x16_bf16 v[96:111], v[128:131], v[52:55], v[96:111]\n"
  "s_cbranch_vccnz .Ls_p5_9\n"
  ".Lo_p5_9:\n"
  "s_add_u32 s64, s76, 0\n"
  "v_mfma_f32_32x32x16_bf16 v[112:127], v[132:135], v[52:55], v[112:127]\n"
  "buffer_load_dwordx4 v[128:131], v11, s[36:39], s64 offen sc1\n"
  "buffer_load_dwordx4 v[132:135], v12, s[36:39], s64 offen sc1\n"
  "s_waitcnt lgkmcnt(0)\n"
  "v_mfma_f32_32x32x16_bf16 v[64:79], v[136:139], v[56:59], v[64:79]\n"
  "s_waitcnt vmcnt(12)\n"
  "ds_read_b64_tr_b16 v[48:49], v38 offset:4096\n"
  "ds_read_b64_tr_b16 v[50:51], v38 offset:4608\n"
  "v_max3_u32 v13, v144, v146, v148\n"
  "v_mfma_f32_32x32x16_bf16 v[80:95], v[140:143], v[56:59], v[80:95]\n"
  "ds_read_b64_tr_b16 v[52:53], v39 offset:4096\n"
  "ds_read_b64_tr_b16 v[54:55], v39 offset:4608\n"
  "v_max_u32 v13, v13, v150\n"
  "v_cmp_eq_u32 vcc, -1, v13\n"
  "v_mfma_f32_32x32x16_bf16 v[96:111], v[136:139], v[60:63], v[96:111]\n"
  "s_cbranch_vccnz .Ls_p5_10\n"
  ".Lo_p5_10:\n"
  "s_add_u32 s65, s76, 1024\n"
  "v_mfma_f32_32x32x16_bf16 v[112:127], v[140:143], v[60:63], v[112:127]\n"
  "buffer_load_dwordx4 v[136:139], v11, s[36:39], s65 offen sc1\n"
  "buffer_load_dwordx4 v[140:143], v12, s[36:39], s65 offen sc1\n"
  "s_waitcnt lgkmcnt(0)\n"
  "v_mfma_f32_32x32x16_bf16 v[64:79], v[144:147], v[48:51], v[64:79]\n"
  "s_waitcnt vmcnt(12)\n"
  "ds_read_b64_tr_b16 v[56:57], v38 offset:6144\n"
  "ds_read_b64_tr_b16 v[58:59], v38 offset:6656\n"
  "v_max3_u32 v13, v152, v154, v156\n"
  "v_mfma_f32_32x32x16_bf16 v[80:95], v[148:151], v[48:51], v[80:95]\n"
  "ds_read_b64_tr_b16 v[60:61], v39 offset:6144\n"
  "ds_read_b64_tr_b16 v[62:63], v39 offset:6656\n"
  "v_max_u32 v13, v13, v158\n"
  "v_cmp_eq_u32 vcc, -1, v13\n"
  "v_mfma_f32_32x32x16_bf16 v[96:111], v[144:147], v[52:55], v[96:111]\n"
  "s_cbranch_vccnz .Ls_p5_11\n"
  ".Lo_p5_11:\n"
  "s_add_u32 s66, s76, 2048\n"
  "v_mfma_f32_32x32x16_bf16 v[112:127], v[148:151], v[52:55], v[112:127]\n"
  "buffer_load_dwordx4 v[144:147], v11, s[36:39], s66 offen sc1\n"
  "buffer_load_dwordx4 v[148:151], v12, s[36:39], s66 offen sc1\n"
  "s_waitcnt lgkmcnt(0)\n"
  "v_mfma_f32_32x32x16_bf16 v[64:79], v[152:155], v[56:59], v[64:79]\n"
  "s_waitcnt vmcnt(12)\n"
  "ds_read_b64_tr_b16 v[48:49], v38 offset:8192\n"
  "ds_read_b64_tr_b16 v[50:51], v38 offset:8704\n"
  "v_max3_u32 v13, v160, v162, v164\n"
  "v_mfma_f32_32x32x16_bf16 v[80:95], v[156:159], v[56:59], v[80:95]\n"
  "ds_read_b64_tr_b16 v[52:53], v39 offset:8192\n"
  "ds_read_b64_tr_b16 v[54:55], v39 offset:8704\n"
  "v_max_u32 v13, v13, v166\n"
  "v_cmp_eq_u32 vcc, -1, v13\n"
  "v_mfma_f32_32x32x16_bf16 v[96:111], v[152:155], v[60:63], v[96:111]\n"
  "s_cbranch_vccnz .Ls_p5_12\n"
  ".Lo_p5_12:\n"
  "s_add_u32 s67, s76, 3072\n"
  "v_mfma_f32_32x32x16_bf16 v[112:127], v[156:159], v[60:63], v[112:127]\n"
  "buffer_load_dwordx4 v[152:155], v11, s[36:39], s67 offen sc1\n"
  "buffer_load_dwordx4 v[156:159], v12, s[36:39], s67 offen sc1\n"
  "s_waitcnt lgkmcnt(0)\n"
  "v_mfma_f32_32x32x16_bf16 v[64:79], v[160:163], v[48:51], v[64:79]\n"
  "s_waitcnt vmcnt(12)\n"
  "ds_read_b64_tr_b16 v[56:57], v38 offset:10240\n"
  "ds_read_b64_tr_b16 v[58:59], v38 offset:10752\n"
  "v_max3_u32 v13, v168, v170, v172\n"
  "v_mfma_f32_32x32x16_bf16 v[80:95], v[164:167], v[48:51], v[80:95]\n"
  "ds_read_b64_tr_b16 v[60:61], v39 offset:10240\n"
  "ds_read_b64_tr_b16 v[62:63], v39 offset:10752\n"
  "v_max_u32 v13, v13, v174\n"
  "v_cmp_eq_u32 vcc, -1, v13\n"
  "v_mfma_f32_32x32x16_bf16 v[96:111], v[160:163], v[52:55], v[96:111]\n"
  "s_cbranch_vccnz .Ls_p5_13\n"
  ".Lo_p5_13:\n"
  "s_add_u32 s68, s77, 0\n"
  "v_mfma_f32_32x32x16_bf16 v[112:127], v[164:167], v[52:55], v[112:127]\n"
  "buffer_load_dwordx4 v[160:163], v11, s[36:39], s68 offen sc1\n"
  "buffer_load_dwordx4 v[164:167], v12, s[36:39], s68 offen sc1\n"
  "s_waitcnt lgkmcnt(0)\n"
  "v_mfma_f32_32x32x16_bf16 v[64:79], v[168:171], v[56:59], v[64:79]\n"
  "s_waitcnt vmcnt(12)\n"
  "ds_read_b64_tr_b16 v[48:49], v38 offset:12288\n"
  "ds_read_b64_tr_b16 v[50:51], v38 offset:12800\n"
  "v_max3_u32 v13, v176, v178, v180\n"
  "v_mfma_f32_32x32x16_bf16 v[80:95], v[172:175], v[56:59], v[80:95]\n"
  "ds_read_b64_tr_b16 v[52:53], v39 offset:12288\n"
  "ds_read_b64_tr_b16 v[54:55], v39 offset:12800\n"
  "v_max_u32 v13, v13, v182\n"
  "v_cmp_eq_u32 vcc, -1, v13\n"
  "v_mfma_f32_32x32x16_bf16 v[96:111], v[168:171], v[60:63], v[96:111]\n"
  "s_cbranch_vccnz .Ls_p5_14\n"
  ".Lo_p5_14:\n"
  "s_add_u32 s69, s77, 1024\n"
  "v_mfma_f32_32x32x16_bf16 v[112:127], v[172:175], v[60:63], v[112:127]\n"
  "buffer_load_dwordx4 v[168:171], v11, s[36:39], s69 offen sc1\n"
  "buffer_load_dwordx4 v[172:175], v12, s[36:39], s69 offen sc1\n"
  "s_waitcnt lgkmcnt(0)\n"
  "v_mfma_f32_32x32x16_bf16 v[64:79], v[176:179], v[48:51], v[64:79]\n"
  "s_waitcnt vmcnt(12)\n"
  "ds_read_b64_tr_b16 v[56:57], v38 offset:14336\n"
  "ds_read_b64_tr_b16 v[58:59], v38 offset:14848\n"
  "v_max3_u32 v13, v184, v186, v188\n"
  "v_mfma_f32_32x32x16_bf16 v[80:95], v[180:183], v[48:51], v[80:95]\n"
  "ds_read_b64_tr_b16 v[60:61], v39 offset:14336\n"
  "ds_read_b64_tr_b16 v[62:63], v39 offset:14848\n"
  "v_max_u32 v13, v13, v190\n"
  "v_cmp_eq_u32 vcc, -1, v13\n"
  "v_mfma_f32_32x32x16_bf16 v[96:111], v[176:179], v[52:55], v[96:111]\n"
  "s_cbranch_vccnz .Ls_p5_15\n"
  ".Lo_p5_15:\n"
  "s_add_u32 s70, s77, 2048\n"
  "v_mfma_f32_32x32x16_bf16 v[112:127], v[180:183], v[52:55], v[112:127]\n"
  "buffer_load_dwordx4 v[176:179], v11, s[36:39], s70 offen sc1\n"
  "buffer_load_dwordx4 v[180:183], v12, s[36:39], s70 offen sc1\n"
  "s_waitcnt lgkmcnt(0)\n"
  "v_mfma_f32_32x32x16_bf16 v[64:79], v[184:187], v[56:59], v[64:79]\n"
  "s_waitcnt vmcnt(12)\n"
  "ds_read_b64_tr_b16 v[48:49], v38 offset:16384\n"
  "ds_read_b64_tr_b16 v[50:51], v38 offset:16896\n"
  "v_max3_u32 v13, v128, v130, v132\n"
  "v_mfma_f32_32x32x16_bf16 v[80:95], v[188:191], v[56:59], v[80:95]\n"
  "ds_read_b64_tr_b16 v[52:53], v39 offset:16384\n"
  "ds_read_b64_tr_b16 v[54:55], v39 offset:16896\n"
  "v_max_u32 v13, v13, v134\n"
  "v_cmp_eq_u32 vcc, -1, v13\n"
  "v_mfma_f32_32x32x16_bf16 v[96:111], v[184:187], v[60:63], v[96:111]\n"
  "s_cbranch_vccnz .Ls_p5_16\n"
  ".Lo_p5_16:\n"
  "s_add_u32 s71, s77, 3072\n"
  "v_mfma_f32_32x32x16_bf16 v[112:127], v[188:191], v[60:63], v[112:127]\n"
  "buffer_load_dwordx4 v[184:187], v11, s[36:39], s71 offen sc1\n"
  "buffer_load_dwordx4 v[188:191], v12, s[36:39], s71 offen sc1\n"
  "s_waitcnt lgkmcnt(0)\n"
  "v_mfma_f32_32x32x16_bf16 v[64:79], v[128:131], v[48:51], v[64:79]\n"
  "s_waitcnt vmcnt(12)\n"
  "ds_read_b64_tr_b16 v[56:57], v38 offset:18432\n"
  "ds_read_b64_tr_b16 v[58:59], v38 offset:18944\n"
  "v_max3_u32 v13, v136, v138, v140\n"
  "v_mfma_f32_32x32x16_bf16 v[80:95], v[132:135], v[48:51], v[80:95]\n"
  "ds_read_b64_tr_b16 v[60:61], v39 offset:18432\n"
  "ds_read_b64_tr_b16 v[62:63], v39 offset:18944\n"
  "v_max_u32 v13, v13, v142\n"
  "v_cmp_eq_u32 vcc, -1, v13\n"
  "v_mfma_f32_32x32x16_bf16 v[96:111], v[128:131], v[52:55], v[96:111]\n"
  "s_cbranch_vccnz .Ls_p5_17\n"
  ".Lo_p5_17:\n"
  "s_add_u32 s64, s78, 0\n"
  "v_mfma_f32_32x32x16_bf16 v[112:127], v[132:135], v[52:55], v[112:127]\n"
  "buffer_load_dwordx4 v[128:131], v11, s[36:39], s64 offen sc1\n"
  "buffer_load_dwordx4 v[132:135], v12, s[36:39], s64 offen sc1\n"
  "s_waitcnt lgkmcnt(0)\n"
  "v_mfma_f32_32x32x16_bf16 v[64:79], v[136:139], v[56:59], v[64:79]\n"
  "s_waitcnt vmcnt(12)\n"
  "ds_read_b64_tr_b16 v[48:49], v38 offset:20480\n"
  "ds_read_b64_tr_b16 v[50:51], v38 offset:20992\n"
  "v_max3_u32 v13, v144, v146, v148\n"
  "v_mfma_f32_32x32x16_bf16 v[80:95], v[140:143], v[56:59], v[80:95]\n"
  "ds_read_b64_tr_b16 v[52:53], v39 offset:20480\n"
  "ds_read_b64_tr_b16 v[54:55], v39 offset:20992\n"
  "v_max_u32 v13, v13, v150\n"
  "v_cmp_eq_u32 vcc, -1, v13\n"
  "v_mfma_f32_32x32x16_bf16 v[96:111], v[136:139], v[60:63], v[96:111]\n"
  "s_cbranch_vccnz .Ls_p5_18\n"
  ".Lo_p5_18:\n"
  "s_add_u32 s65, s78, 1024\n"
  "s_barrier\n"
  "v_mfma_f32_32x32x16_bf16 v[112:127], v[140:143], v[60:63], v[112:127]\n"
  "buffer_load_dwordx4 v[136:139], v11, s[36:39], s65 offen sc1\n"
  "buffer_load_dwordx4 v[140:143], v12, s[36:39], s65 offen sc1\n"
  "s_waitcnt lgkmcnt(0)\n"
  "v_mfma_f32_32x32x16_bf16 v[64:79], v[144:147], v[48:51], v[64:79]\n"
  "s_waitcnt vmcnt(12)\n"
  "ds_read_b64_tr_b16 v[56:57], v38 offset:22528\n"
  "ds_read_b64_tr_b16 v[58:59], v38 offset:23040\n"
  "v_max3_u32 v13, v152, v154, v156\n"
  "v_mfma_f32_32x32x16_bf16 v[80:95], v[148:151], v[48:51], v[80:95]\n"
  "ds_read_b64_tr_b16 v[60:61], v39 offset:22528\n"
  "ds_read_b64_tr_b16 v[62:63], v39 offset:23040\n"
  "v_max_u32 v13, v13, v158\n"
  "v_cmp_eq_u32 vcc, -1, v13\n"
  "v_mfma_f32_32x32x16_bf16 v[96:111], v[144:147], v[52:55], v[96:111]\n"
  "s_cbranch_vccnz .Ls_p5_19\n"
  ".Lo_p5_19:\n"
  "s_add_u32 s66, s78, 2048\n"
  "v_mfma_f32_32x32x16_bf16 v[112:127], v[148:151], v[52:55], v[112:127]\n"
  "buffer_load_dwordx4 v[144:147], v11, s[36:39], s66 offen sc1\n"
  "buffer_load_dwordx4 v[148:151], v12, s[36:39], s66 offen sc1\n"
  "s_waitcnt lgkmcnt(0)\n"
  "v_mfma_f32_32x32x16_bf16 v[64:79], v[152:155], v[56:59], v[64:79]\n"
  "s_waitcnt vmcnt(12)\n"
  "ds_read_b64_tr_b16 v[48:49], v38 offset:24576\n"
  "ds_read_b64_tr_b16 v[50:51], v38 offset:25088\n"
  "v_max3_u32 v13, v160, v162, v164\n"
  "v_mfma_f32_32x32x16_bf16 v[80:95], v[156:159], v[56:59], v[80:95]\n"
  "ds_read_b64_tr_b16 v[52:53], v39 offset:24576\n"
  "ds_read_b64_tr_b16 v[54:55], v39 offset:25088\n"
  "v_max_u32 v13, v13, v166\n"
  "v_cmp_eq_u32 vcc, -1, v13\n"
  "v_mfma_f32_32x32x16_bf16 v[96:111], v[152:155], v[60:63], v[96:111]\n"
  "s_cbranch_vccnz .Ls_p5_20\n"
  ".Lo_p5_20:\n"
  "s_add_u32 s67, s78, 3072\n"
  "v_mfma_f32_32x32x16_bf16 v[112:127], v[156:159], v[60:63], v[112:127]\n"
  "buffer_load_dwordx4 v[152:155], v11, s[36:39], s67 offen sc1\n"
  "buffer_load_dwordx4 v[156:159], v12, s[36:39], s67 offen sc1\n"
  "s_waitcnt lgkmcnt(0)\n"
  "v_mfma_f32_32x32x16_bf16 v[64:79], v[160:163], v[48:51], v[64:79]\n"
  "s_waitcnt vmcnt(12)\n"
  "ds_read_b64_tr_b16 v[56:57], v38 offset:26624\n"
  "ds_read_b64_tr_b16 v[58:59], v38 offset:27136\n"
  "v_max3_u32 v13, v168, v170, v172\n"
  "v_mfma_f32_32x32x16_bf16 v[80:95], v[164:167], v[48:51], v[80:95]\n"
  "ds_read_b64_tr_b16 v[60:61], v39 offset:26624\n"
  "ds_read_b64_tr_b16 v[62:63], v39 offset:27136\n"
  "v_max_u32 v13, v13, v174\n"
  "v_cmp_eq_u32 vcc, -1, v13\n"
  "v_mfma_f32_32x32x16_bf16 v[96:111], v[160:163], v[52:55], v[96:111]\n"
  "s_cbranch_vccnz .Ls_p5_21\n"
  ".Lo_p5_21:\n"
  "v_mfma_f32_32x32x16_bf16 v[112:127], v[164:167], v[52:55], v[112:127]\n"
  "buffer_load_dwordx4 v[16:19], v2, s[36:39], s23 offen sc1\n"
  "buffer_load_dwordx4 v[20:23], v2, s[36:39], s23 offen offset:1024 sc1\n"
  "s_waitcnt lgkmcnt(0)\n"
  "v_mfma_f32_32x32x16_bf16 v[64:79], v[168:171], v[56:59], v[64:79]\n"
  "s_waitcnt vmcnt(12)\n"
  "ds_read_b64_tr_b16 v[48:49], v38 offset:28672\n"
  "ds_read_b64_tr_b16 v[50:51], v38 offset:29184\n"
  "v_max3_u32 v13, v176, v178, v180\n"
  "v_mfma_f32_32x32x16_bf16 v[80:95], v[172:175], v[56:59], v[80:95]\n"
  "ds_read_b64_tr_b16 v[52:53], v39 offset:28672\n"
  "ds_read_b64_tr_b16 v[54:55], v39 offset:29184\n"
  "v_max_u32 v13, v13, v182\n"
  "v_cmp_eq_u32 vcc, -1, v13\n"
  "v_mfma_f32_32x32x16_bf16 v[96:111], v[168:171], v[60:63], v[96:111]\n"
  "s_cbranch_vccnz .Ls_p5_22\n"
  ".Lo_p5_22:\n"
  "v_mfma_f32_32x32x16_bf16 v[112:127], v[172:175], v[60:63], v[112:127]\n"
  "s_mov_b64 s[96:97], s[18:19]\n"
  "s_mov_b32 s98, 1835008\n"
  "s_mov_b32 s99, 0x20000\n"
  "s_waitcnt lgkmcnt(0)\n"
  "v_mfma_f32_32x32x16_bf16 v[64:79], v[176:179], v[48:51], v[64:79]\n"
  "s_waitcnt vmcnt(10)\n"
  "ds_read_b64_tr_b16 v[56:57], v38 offset:30720\n"
  "ds_read_b64_tr_b16 v[58:59], v38 offset:31232\n"
  "v_max3_u32 v13, v184, v186, v188\n"
  "v_mfma_f32_32x32x16_bf16 v[80:95], v[180:183], v[48:51], v[80:95]\n"
  "ds_read_b64_tr_b16 v[60:61], v39 offset:30720\n"
  "ds_read_b64_tr_b16 v[62:63], v39 offset:31232\n"
  "v_max_u32 v13, v13, v190\n"
  "v_cmp_eq_u32 vcc, -1, v13\n"
  "v_mfma_f32_32x32x16_bf16 v[96:111], v[176:179], v[52:55], v[96:111]\n"
  "s_cbranch_vccnz .Ls_p5_23\n"
  ".Lo_p5_23:\n"
  "v_mfma_f32_32x32x16_bf16 v[112:127], v[180:183], v[52:55], v[112:127]\n"
  "s_waitcnt lgkmcnt(0)\n"
  "v_mfma_f32_32x32x16_bf16 v[64:79], v[184:187], v[56:59], v[64:79]\n"
  "s_waitcnt vmcnt(8)\n"
  "ds_read_b64_tr_b16 v[48:49], v38 offset:32768\n"
  "ds_read_b64_tr_b16 v[50:51], v38 offset:33280\n"
  "v_max3_u32 v13, v128, v130, v132\n"
  "v_mfma_f32_32x32x16_bf16 v[80:95], v[188:191], v[56:59], v[80:95]\n"
  "ds_read_b64_tr_b16 v[52:53], v39 offset:32768\n"
  "ds_read_b64_tr_b16 v[54:55], v39 offset:33280\n"
  "v_max_u32 v13, v13, v134\n"
  "v_cmp_eq_u32 vcc, -1, v13\n"
  "v_mfma_f32_32x32x16_bf16 v[96:111], v[184:187], v[60:63], v[96:111]\n"
  "s_cbranch_vccnz .Ls_p5_24\n"
  ".Lo_p5_24:\n"
  "v_mfma_f32_32x32x16_bf16 v[112:127], v[188:191], v[60:63], v[112:127]\n"
  "s_waitcnt lgkmcnt(0)\n"
  "v_mfma_f32_32x32x16_bf16 v[64:79], v[128:131], v[48:51], v[64:79]\n"
  "s_waitcnt vmcnt(6)\n"
  "ds_read_b64_tr_b16 v[56:57], v38 offset:34816\n"
  "ds_read_b64_tr_b16 v[58:59], v38 offset:35328\n"
  "v_max3_u32 v13, v136, v138, v140\n"
  "v_mfma_f32_32x32x16_bf16 v[80:95], v[132:135], v[48:51], v[80:95]\n"
  "ds_read_b64_tr_b16 v[60:61], v39 offset:34816\n"
  "ds_read_b64_tr_b16 v[62:63], v39 offset:35328\n"
  "v_max_u32 v13, v13, v142\n"
  "v_cmp_eq_u32 vcc, -1, v13\n"
  "v_mfma_f32_32x32x16_bf16 v[96:111], v[128:131], v[52:55], v[96:111]\n"
  "s_cbranch_vccnz .Ls_p5_25\n"
  ".Lo_p5_25:\n"
  "v_mfma_f32_32x32x16_bf16 v[112:127], v[132:135], v[52:55], v[112:127]\n"
  "s_waitcnt lgkmcnt(0)\n"
  "v_mfma_f32_32x32x16_bf16 v[64:79], v[136:139], v[56:59], v[64:79]\n"
  "s_waitcnt vmcnt(4)\n"
  "ds_read_b64_tr_b16 v[48:49], v38 offset:36864\n"
  "ds_read_b64_tr_b16 v[50:51], v38 offset:37376\n"
  "v_max3_u32 v13, v144, v146, v148\n"
  "v_mfma_f32_32x32x16_bf16 v[80:95], v[140:143], v[56:59], v[80:95]\n"
  "ds_read_b64_tr_b16 v[52:53], v39 offset:36864\n"
  "ds_read_b64_tr_b16 v[54:55], v39 offset:37376\n"
  "v_max_u32 v13, v13, v150\n"
  "v_cmp_eq_u32 vcc, -1, v13\n"
  "v_mfma_f32_32x32x16_bf16 v[96:111], v[136:139], v[60:63], v[96:111]\n"
  "s_cbranch_vccnz .Ls_p5_26\n"
  ".Lo_p5_26:\n"
  "v_mfma_f32_32x32x16_bf16 v[112:127], v[140:143], v[60:63], v[112:127]\n"
  "s_waitcnt lgkmcnt(0)\n"
  "v_mfma_f32_32x32x16_bf16 v[80:95], v[148:151], v[48:51], v[80:95]\n"
  "s_waitcnt vmcnt(2)\n"
  "ds_read_b64_tr_b16 v[56:57], v38 offset:38912\n"
  "ds_read_b64_tr_b16 v[58:59], v38 offset:39424\n"
  "v_max3_u32 v13, v152, v154, v156\n"
  "v_mfma_f32_32x32x16_bf16 v[96:111], v[144:147], v[52:55], v[96:111]\n"
  "ds_read_b64_tr_b16 v[60:61], v39 offset:38912\n"
  "ds_read_b64_tr_b16 v[62:63], v39 offset:39424\n"
  "v_max_u32 v13, v13, v158\n"
  "v_cmp_eq_u32 vcc, -1, v13\n"
  "v_mfma_f32_32x32x16_bf16 v[112:127], v[148:151], v[52:55], v[112:127]\n"
  "s_cbranch_vccnz .Ls_p5_27\n"
  ".Lo_p5_27:\n"
  "s_waitcnt lgkmcnt(0)\n"
  "v_mfma_f32_32x32x16_bf16 v[80:95], v[156:159], v[56:59], v[80:95]\n"
  "v_mfma_f32_32x32x16_bf16 v[96:111], v[152:155], v[60:63], v[96:111]\n"
  "v_mfma_f32_32x32x16_bf16 v[112:127], v[156:159], v[60:63], v[112:127]\n"
  "s_nop 9\n"
  "ds_write_b128 v6, v[80:83] offset:0\n"
  "v_mfma_f32_32x32x16_bf16 v[64:79], v[144:147], v[48:51], v[64:79]\n"
  "ds_write_b128 v6, v[84:87] offset:1024\n"
  "ds_write_b128 v6, v[88:91] offset:2048\n"
  "ds_write_b128 v6, v[92:95] offset:3072\n"
  "ds_write_b128 v6, v[96:99] offset:4096\n"
  "v_mfma_f32_32x32x16_bf16 v[64:79], v[152:155], v[56:59], v[64:79]\n"
  "ds_write_b128 v6, v[100:103] offset:5120\n"
  "ds_write_b128 v6, v[104:107] offset:6144\n"
  "ds_write_b128 v6, v[108:111] offset:7168\n"
  "ds_write_b128 v6, v[112:115] offset:8192\n"
  "ds_write_b128 v6, v[116:119] offset:9216\n"
  "ds_write_b128 v6, v[120:123] offset:10240\n"
  "ds_write_b128 v6, v[124:127] offset:11264\n"
  "s_waitcnt vmcnt(0)\n"
  "v_lshlrev_b32 v176, 16, v16\n"
  "v_and_b32 v177, 0xffff0000, v16\n"
  "v_lshlrev_b32 v178, 16, v17\n"
  "v_and_b32 v179, 0xffff0000, v17\n"
  "v_lshlrev_b32 v180, 16, v18\n"
  "v_and_b32 v181, 0xffff0000, v18\n"
  "v_lshlrev_b32 v182, 16, v19\n"
  "v_and_b32 v183, 0xffff0000, v19\n"
  "v_lshlrev_b32 v184, 16, v20\n"
  "v_and_b32 v185, 0xffff0000, v20\n"
  "v_lshlrev_b32 v186, 16, v21\n"
  "v_and_b32 v187, 0xffff0000, v21\n"
  "v_lshlrev_b32 v188, 16, v22\n"
  "v_and_b32 v189, 0xffff0000, v22\n"
  "v_lshlrev_b32 v190, 16, v23\n"
  "v_and_b32 v191, 0xffff0000, v23\n"
  "v_pk_add_f32 v[64:65], v[64:65], v[176:177]\n"
  "v_pk_add_f32 v[66:67], v[66:67], v[178:179]\n"
  "v_pk_add_f32 v[68:69], v[68:69], v[180:181]\n"
  "v_pk_add_f32 v[70:71], v[70:71], v[182:183]\n"
  "v_pk_add_f32 v[72:73], v[72:73], v[184:185]\n"
  "v_pk_add_f32 v[74:75], v[74:75], v[186:187]\n"
  "v_pk_add_f32 v[76:77], v[76:77], v[188:189]\n"
  "v_pk_add_f32 v[78:79], v[78:79], v[190:191]\n"
  "s_waitcnt lgkmcnt(0)\n"
  "s_barrier\n"
  "ds_read_b128 v[128:131], v3 offset:0\n"
  "ds_read_b128 v[144:147], v4 offset:4096\n"
  "ds_read_b128 v[160:163], v5 offset:8192\n"
  "ds_read_b128 v[132:135], v3 offset:1024\n"
  "ds_read_b128 v[148:151], v4 offset:5120\n"
  "ds_read_b128 v[164:167], v5 offset:9216\n"
  "ds_read_b128 v[136:139], v3 offset:2048\n"
  "ds_read_b128 v[152:155], v4 offset:6144\n"
  "ds_read_b128 v[168:171], v5 offset:10240\n"
  "ds_read_b128 v[140:143], v3 offset:3072\n"
  "ds_read_b128 v[156:159], v4 offset:7168\n"
  "ds_read_b128 v[172:175], v5 offset:11264\n"
  "s_waitcnt lgkmcnt(10)\n"
  "v_pk_add_f32 v[128:129], v[128:129], v[144:145]\n"
  "v_pk_add_f32 v[130:131], v[130:131], v[146:147]\n"
  "s_waitcnt lgkmcnt(9)\n"
  "v_pk_add_f32 v[64:65], v[64:65], v[160:161]\n"
  "v_pk_add_f32 v[66:67], v[66:67], v[162:163]\n"
  "v_pk_add_f32 v[64:65], v[64:65], v[128:129]\n"
  "v_pk_add_f32 v[66:67], v[66:67], v[130:131]\n"
  "v_cvt_pk_bf16_f32 v176, v64, v65\n"
  "v_cvt_pk_bf16_f32 v177, v66, v67\n"
  "s_waitcnt lgkmcnt(7)\n"
  "v_pk_add_f32 v[132:133], v[132:133], v[148:149]\n"
  "v_pk_add_f32 v[134:135], v[134:135], v[150:151]\n"
  "s_waitcnt lgkmcnt(6)\n"
  "v_pk_add_f32 v[68:69], v[68:69], v[164:165]\n"
  "v_pk_add_f32 v[70:71], v[70:71], v[166:167]\n"
  "v_pk_add_f32 v[68:69], v[68:69], v[132:133]\n"
  "v_pk_add_f32 v[70:71], v[70:71], v[134:135]\n"
  "v_cvt_pk_bf16_f32 v178, v68, v69\n"
  "v_cvt_pk_bf16_f32 v179, v70, v71\n"
  "buffer_store_dwordx4 v[176:179], v32, s[96:99], s21 offen offset:0\n"
  "s_waitcnt lgkmcnt(4)\n"
  "v_pk_add_f32 v[136:137], v[136:137], v[152:153]\n"
  "v_pk_add_f32 v[138:139], v[138:139], v[154:155]\n"
  "s_waitcnt lgkmcnt(3)\n"
  "v_pk_add_f32 v[72:73], v[72:73], v[168:169]\n"
  "v_pk_add_f32 v[74:75], v[74:75], v[170:171]\n"
  "v_pk_add_f32 v[72:73], v[72:73], v[136:137]\n"
  "v_pk_add_f32 v[74:75], v[74:75], v[138:139]\n"
  "v_cvt_pk_bf16_f32 v180, v72, v73\n"
  "v_cvt_pk_bf16_f32 v181, v74, v75\n"
  "s_waitcnt lgkmcnt(1)\n"
  "v_pk_add_f32 v[140:141], v[140:141], v[156:157]\n"
  "v_pk_add_f32 v[142:143], v[142:143], v[158:159]\n"
  "s_waitcnt lgkmcnt(0)\n"
  "v_pk_add_f32 v[76:77], v[76:77], v[172:173]\n"
  "v_pk_add_f32 v[78:79], v[78:79], v[174:175]\n"
  "v_pk_add_f32 v[76:77], v[76:77], v[140:141]\n"
  "v_pk_add_f32 v[78:79], v[78:79], v[142:143]\n"
  "v_cvt_pk_bf16_f32 v182, v76, v77\n"
  "v_cvt_pk_bf16_f32 v183, v78, v79\n"
  "buffer_store_dwordx4 v[180:183], v32, s[96:99], s21 offen offset:32\n"
  ".Lb_end:\n"
  "s_endpgm\n"
  ".Lb_p5:\n"
  "s_waitcnt vmcnt(0) lgkmcnt(0)\n"
  "s_cmp_eq_u32 s90, 0\n"
  "s_cbranch_scc1 .Lb_end\n"
  "s_add_u32 s72, s20, 0\n"
  "s_add_u32 s73, s20, 262144\n"
  "s_add_u32 s74, s20, 524288\n"
  "s_add_u32 s75, s20, 786432\n"
  "s_add_u32 s76, s20, 1048576\n"
  "s_add_u32 s77, s20, 1310720\n"
  "s_add_u32 s78, s20, 1572864\n"
  "s_add_u32 m0, s51, 24576\n"
  "s_add_u32 s52, s72, 0\n"
  "buffer_load_dwordx4 v36, s[96:99], s52 offen lds\n"
  "buffer_load_dwordx4 v37, s[96:99], s52 offen offset:1024 lds\n"
  "s_add_u32 m0, s51, 26624\n"
  "s_add_u32 s52, s72, 65536\n"
  "buffer_load_dwordx4 v36, s[96:99], s52 offen lds\n"
  "buffer_load_dwordx4 v37, s[96:99], s52 offen offset:1024 lds\n"
  "s_add_u32 m0, s51, 28672\n"
  "s_add_u32 s52, s72, 131072\n"
  "buffer_load_dwordx4 v36, s[96:99], s52 offen lds\n"
  "buffer_load_dwordx4 v37, s[96:99], s52 offen offset:1024 lds\n"
  "s_add_u32 m0, s51, 30720\n"
  "s_add_u32 s52, s72, 196608\n"
  "buffer_load_dwordx4 v36, s[96:99], s52 offen lds\n"
  "buffer_load_dwordx4 v37, s[96:99], s52 offen offset:1024 lds\n"
  "s_add_u32 m0, s51, 32768\n"
  "s_add_u32 s52, s73, 0\n"
  "buffer_load_dwordx4 v36, s[96:99], s52 offen lds\n"
  "buffer_load_dwordx4 v37, s[96:99], s52 offen offset:1024 lds\n"
  "s_add_u32 m0, s51, 34816\n"
  "s_add_u32 s52, s73, 65536\n"
  "buffer_load_dwordx4 v36, s[96:99], s52 offen lds\n"
  "buffer_load_dwordx4 v37, s[96:99], s52 offen offset:1024 lds\n"
  "s_add_u32 m0, s51, 36864\n"
  "s_add_u32 s52, s73, 131072\n"
  "buffer_load_dwordx4 v36, s[96:99], s52 offen lds\n"
  "buffer_load_dwordx4 v37, s[96:99], s52 offen offset:1024 lds\n"
  "s_add_u32 m0, s51, 38912\n"
  "s_add_u32 s52, s73, 196608\n"
  "buffer_load_dwordx4 v36, s[96:99], s52 offen lds\n"
  "buffer_load_dwordx4 v37, s[96:99], s52 offen offset:1024 lds\n"
  "s_add_u32 m0, s51, 0\n"
  "s_add_u32 s52, s74, 0\n"
  "buffer_load_dwordx4 v36, s[96:99], s52 offen lds\n"
  "buffer_load_dwordx4 v37, s[96:99], s52 offen offset:1024 lds\n"
  "s_add_u32 m0, s51, 2048\n"
  "s_add_u32 s52, s74, 65536\n"
  "buffer_load_dwordx4 v36, s[96:99], s52 offen lds\n"
  "buffer_load_dwordx4 v37, s[96:99], s52 offen offset:1024 lds\n"
  "s_add_u32 m0, s51, 4096\n"
  "s_add_u32 s52, s74, 131072\n"
  "buffer_load_dwordx4 v36, s[96:99], s52 offen lds\n"
  "buffer_load_dwordx4 v37, s[96:99], s52 offen offset:1024 lds\n"
  "s_add_u32 m0, s51, 6144\n"
  "s_add_u32 s52, s74, 196608\n"
  "buffer_load_dwordx4 v36, s[96:99], s52 offen lds\n"
  "buffer_load_dwordx4 v37, s[96:99], s52 offen offset:1024 lds\n"
  "s_add_u32 m0, s51, 8192\n"
  "s_add_u32 s52, s75, 0\n"
  "buffer_load_dwordx4 v36, s[96:99], s52 offen lds\n"
  "buffer_load_dwordx4 v37, s[96:99], s52 offen offset:1024 lds\n"
  "s_add_u32 m0, s51, 10240\n"
  "s_add_u32 s52, s75, 65536\n"
  "buffer_load_dwordx4 v36, s[96:99], s52 offen lds\n"
  "buffer_load_dwordx4 v37, s[96:99], s52 offen offset:1024 lds\n"
  "s_add_u32 m0, s51, 12288\n"
  "s_add_u32 s52, s75, 131072\n"
  "buffer_load_dwordx4 v36, s[96:99], s52 offen lds\n"
  "buffer_load_dwordx4 v37, s[96:99], s52 offen offset:1024 lds\n"
  "s_add_u32 m0, s51, 14336\n"
  "s_add_u32 s52, s75, 196608\n"
  "buffer_load_dwordx4 v36, s[96:99], s52 offen lds\n"
  "buffer_load_dwordx4 v37, s[96:99], s52 offen offset:1024 lds\n"
  "s_add_u32 m0, s51, 16384\n"
  "s_add_u32 s52, s76, 0\n"
  "buffer_load_dwordx4 v36, s[96:99], s52 offen lds\n"
  "buffer_load_dwordx4 v37, s[96:99], s52 offen offset:1024 lds\n"
  "s_add_u32 m0, s51, 18432\n"
  "s_add_u32 s52, s76, 65536\n"
  "buffer_load_dwordx4 v36, s[96:99], s52 offen lds\n"
  "buffer_load_dwordx4 v37, s[96:99], s52 offen offset:1024 lds\n"
  "s_add_u32 m0, s51, 20480\n"
  "s_add_u32 s52, s76, 131072\n"
  "buffer_load_dwordx4 v36, s[96:99], s52 offen lds\n"
  "buffer_load_dwordx4 v37, s[96:99], s52 offen offset:1024 lds\n"
  "s_add_u32 m0, s51, 22528\n"
  "s_add_u32 s52, s76, 196608\n"
  "buffer_load_dwordx4 v36, s[96:99], s52 offen lds\n"
  "buffer_load_dwordx4 v37, s[96:99], s52 offen offset:1024 lds\n"
  "s_waitcnt vmcnt(0) lgkmcnt(0)\n"
  "ds_read_b64_tr_b16 v[192:193], v38 offset:24576\n"
  "ds_read_b64_tr_b16 v[194:195], v38 offset:25088\n"
  "ds_read_b64_tr_b16 v[196:197], v39 offset:24576\n"
  "ds_read_b64_tr_b16 v[198:199], v39 offset:25088\n"
  "ds_read_b64_tr_b16 v[200:201], v38 offset:26624\n"
  "ds_read_b64_tr_b16 v[202:203], v38 offset:27136\n"
  "ds_read_b64_tr_b16 v[204:205], v39 offset:26624\n"
  "ds_read_b64_tr_b16 v[206:207], v39 offset:27136\n"
  "ds_read_b64_tr_b16 v[208:209], v38 offset:28672\n"
  "ds_read_b64_tr_b16 v[210:211], v38 offset:29184\n"
  "ds_read_b64_tr_b16 v[212:213], v39 offset:28672\n"
  "ds_read_b64_tr_b16 v[214:215], v39 offset:29184\n"
  "ds_read_b64_tr_b16 v[216:217], v38 offset:30720\n"
  "ds_read_b64_tr_b16 v[218:219], v38 offset:31232\n"
  "ds_read_b64_tr_b16 v[220:221], v39 offset:30720\n"
  "ds_read_b64_tr_b16 v[222:223], v39 offset:31232\n"
  "ds_read_b64_tr_b16 v[224:225], v38 offset:32768\n"
  "ds_read_b64_tr_b16 v[226:227], v38 offset:33280\n"
  "ds_read_b64_tr_b16 v[228:229], v39 offset:32768\n"
  "ds_read_b64_tr_b16 v[230:231], v39 offset:33280\n"
  "ds_read_b64_tr_b16 v[232:233], v38 offset:34816\n"
  "ds_read_b64_tr_b16 v[234:235], v38 offset:35328\n"
  "ds_read_b64_tr_b16 v[236:237], v39 offset:34816\n"
  "ds_read_b64_tr_b16 v[238:239], v39 offset:35328\n"
  "ds_read_b64_tr_b16 v[240:241], v38 offset:36864\n"
  "ds_read_b64_tr_b16 v[242:243], v38 offset:37376\n"
  "ds_read_b64_tr_b16 v[244:245], v39 offset:36864\n"
  "ds_read_b64_tr_b16 v[246:247], v39 offset:37376\n"
  "ds_read_b64_tr_b16 v[248:249], v38 offset:38912\n"
  "ds_read_b64_tr_b16 v[250:251], v38 offset:39424\n"
  "ds_read_b64_tr_b16 v[252:253], v39 offset:38912\n"
  "ds_read_b64_tr_b16 v[254:255], v39 offset:39424\n"
  "s_waitcnt vmcnt(0) lgkmcnt(0)\n"
  "s_add_u32 m0, s51, 24576\n"
  "s_add_u32 s52, s77, 0\n"
  "buffer_load_dwordx4 v36, s[96:99], s52 offen lds\n"
  "buffer_load_dwordx4 v37, s[96:99], s52 offen offset:1024 lds\n"
  "s_add_u32 m0, s51, 26624\n"
  "s_add_u32 s52, s77, 65536\n"
  "buffer_load_dwordx4 v36, s[96:99], s52 offen lds\n"
  "buffer_load_dwordx4 v37, s[96:99], s52 offen offset:1024 lds\n"
  "s_add_u32 m0, s51, 28672\n"
  "s_add_u32 s52, s77, 131072\n"
  "buffer_load_dwordx4 v36, s[96:99], s52 offen lds\n"
  "buffer_load_dwordx4 v37, s[96:99], s52 offen offset:1024 lds\n"
  "s_add_u32 m0, s51, 30720\n"
  "s_add_u32 s52, s77, 196608\n"
  "buffer_load_dwordx4 v36, s[96:99], s52 offen lds\n"
  "buffer_load_dwordx4 v37, s[96:99], s52 offen offset:1024 lds\n"
  "s_add_u32 m0, s51, 32768\n"
  "s_add_u32 s52, s78, 0\n"
  "buffer_load_dwordx4 v36, s[96:99], s52 offen lds\n"
  "buffer_load_dwordx4 v37, s[96:99], s52 offen offset:1024 lds\n"
  "s_add_u32 m0, s51, 34816\n"
  "s_add_u32 s52, s78, 65536\n"
  "buffer_load_dwordx4 v36, s[96:99], s52 offen lds\n"
  "buffer_load_dwordx4 v37, s[96:99], s52 offen offset:1024 lds\n"
  "s_add_u32 m0, s51, 36864\n"
  "s_add_u32 s52, s78, 131072\n"
  "buffer_load_dwordx4 v36, s[96:99], s52 offen lds\n"
  "buffer_load_dwordx4 v37, s[96:99], s52 offen offset:1024 lds\n"
  "s_add_u32 m0, s51, 38912\n"
  "s_add_u32 s52, s78, 196608\n"
  "buffer_load_dwordx4 v36, s[96:99], s52 offen lds\n"
  "buffer_load_dwordx4 v37, s[96:99], s52 offen offset:1024 lds\n"
  "s_waitcnt vmcnt(0) lgkmcnt(0)\n"
  "s_mul_i32 s53, s87, 112\n"
  "s_lshr_b32 s54, s49, 1\n"
  "s_mul_i32 s54, s54, 14\n"
  "s_add_u32 s53, s53, s54\n"
  "s_lshl_b32 s53, s53, 11\n"
  "s_add_u32 s53, s53, 10452992\n"
  "v_mov_b32 v14, 0\n"
  ".Lb_uh:\n"
  "buffer_load_dword v13, v14, s[36:39], s53 offen sc1\n"
  "s_add_u32 s63, s63, 1\n"
  "s_waitcnt vmcnt(0)\n"
  "v_cmp_eq_u32 vcc, -1, v13\n"
  "s_cbranch_vccz .Lb_uh2\n"
  "s_sleep 4\n"
  "s_cmp_lt_u32 s63, 65536\n"
  "s_cbranch_scc1 .Lb_uh\n"
  ".Lb_uh2:\n"
  "s_sleep 1\n"
  "s_branch .Lb_p5b\n"
  ".Lb_safe:\n"
  "s_lshl_b32 s52, s60, 18\n"
  "s_add_u32 s52, s52, 2097152\n"
  "s_mov_b32 s53, 64\n"
  "s_lshl_b32 s54, s60, 17\n"
  "s_lshl_b32 s55, s61, 13\n"
  "s_add_u32 s54, s54, s55\n"
  "s_lshl_b32 s55, s48, 11\n"
  "s_add_u32 s54, s54, s55\n"
  "buffer_load_dwordx4 v[184:187], v2, s[36:39], s54 offen\n"
  "buffer_load_dwordx4 v[188:191], v2, s[36:39], s54 offen offset:1024\n"
  "v_mov_b32 v112, 0\n"
  "v_mov_b32 v113, 0\n"
  "v_mov_b32 v114, 0\n"
  "v_mov_b32 v115, 0\n"
  "v_mov_b32 v116, 0\n"
  "v_mov_b32 v117, 0\n"
  "v_mov_b32 v118, 0\n"
  "v_mov_b32 v119, 0\n"
  "v_mov_b32 v120, 0\n"
  "v_mov_b32 v121, 0\n"
  "v_mov_b32 v122, 0\n"
  "v_mov_b32 v123, 0\n"
  "v_mov_b32 v124, 0\n"
  "v_mov_b32 v125, 0\n"
  "v_mov_b32 v126, 0\n"
  "v_mov_b32 v127, 0\n"
  "v_mov_b32 v144, 0xff800000\n"
  ".Lb_safe_loop:\n"
  "buffer_load_dwordx4 v[192:195], v2, s[36:39], s52 offen offset:0\n"
  "buffer_load_dwordx4 v[196:199], v2, s[36:39], s52 offen offset:1024\n"
  "buffer_load_dwordx4 v[200:203], v2, s[36:39], s52 offen offset:2048\n"
  "buffer_load_dwordx4 v[204:207], v2, s[36:39], s52 offen offset:3072\n"
  "s_add_u32 s52, s52, 4096\n"
  "s_waitcnt vmcnt(0)\n"
  "v_mfma_f32_32x32x16_bf16 v[128:143], v[192:195], v[184:187], 0\n"
  "v_mfma_f32_32x32x16_bf16 v[128:143], v[196:199], v[188:191], v[128:143]\n"
  "s_nop 11\n"
  "v_max3_f32 v145, v128, v129, v144\n"
  "v_max3_f32 v145, v130, v131, v145\n"
  "v_max3_f32 v145, v132, v133, v145\n"
  "v_max3_f32 v145, v134, v135, v145\n"
  "v_max3_f32 v145, v136, v137, v145\n"
  "v_max3_f32 v145, v138, v139, v145\n"
  "v_max3_f32 v145, v140, v141, v145\n"
  "v_max3_f32 v145, v142, v143, v145\n"
  "v_mov_b32 v146, v145\n"
  "s_nop 1\n"
  "v_permlane32_swap_b32 v145, v146\n"
  "v_max_f32 v145, v145, v146\n"
  "v_sub_f32 v146, v144, v145\n"
  "v_exp_f32 v146, v146\n"
  "v_mov_b32 v144, v145\n"
  "v_sub_f32 v128, v128, v145\n"
  "v_sub_f32 v129, v129, v145\n"
  "v_sub_f32 v130, v130, v145\n"
  "v_sub_f32 v131, v131, v145\n"
  "v_sub_f32 v132, v132, v145\n"
  "v_sub_f32 v133, v133, v145\n"
  "v_sub_f32 v134, v134, v145\n"
  "v_sub_f32 v135, v135, v145\n"
  "v_sub_f32 v136, v136, v145\n"
  "v_sub_f32 v137, v137, v145\n"
  "v_sub_f32 v138, v138, v145\n"
  "v_sub_f32 v139, v139, v145\n"
  "v_sub_f32 v140, v140, v145\n"
  "v_sub_f32 v141, v141, v145\n"
  "v_sub_f32 v142, v142, v145\n"
  "v_sub_f32 v143, v143, v145\n"
  "v_exp_f32 v128, v128\n"
  "v_exp_f32 v129, v129\n"
  "v_exp_f32 v130, v130\n"
  "v_exp_f32 v131, v131\n"
  "v_exp_f32 v132, v132\n"
  "v_exp_f32 v133, v133\n"
  "v_exp_f32 v134, v134\n"
  "v_exp_f32 v135, v135\n"
  "v_exp_f32 v136, v136\n"
  "v_exp_f32 v137, v137\n"
  "v_exp_f32 v138, v138\n"
  "v_exp_f32 v139, v139\n"
  "v_exp_f32 v140, v140\n"
  "v_exp_f32 v141, v141\n"
  "v_exp_f32 v142, v142\n"
  "v_exp_f32 v143, v143\n"
  "v_mul_f32 v112, v112, v146\n"
  "v_mul_f32 v113, v113, v146\n"
  "v_mul_f32 v114, v114, v146\n"
  "v_mul_f32 v115, v115, v146\n"
  "v_mul_f32 v116, v116, v146\n"
  "v_mul_f32 v117, v117, v146\n"
  "v_mul_f32 v118, v118, v146\n"
  "v_mul_f32 v119, v119, v146\n"
  "v_mul_f32 v120, v120, v146\n"
  "v_mul_f32 v121, v121, v146\n"
  "v_mul_f32 v122, v122, v146\n"
  "v_mul_f32 v123, v123, v146\n"
  "v_mul_f32 v124, v124, v146\n"
  "v_mul_f32 v125, v125, v146\n"
  "v_mul_f32 v126, v126, v146\n"
  "v_mul_f32 v127, v127, v146\n"
  "v_cvt_pk_bf16_f32 v56, v128, v129\n"
  "v_cvt_pk_bf16_f32 v57, v130, v131\n"
  "v_cvt_pk_bf16_f32 v58, v132, v133\n"
  "v_cvt_pk_bf16_f32 v59, v134, v135\n"
  "v_cvt_pk_bf16_f32 v60, v136, v137\n"
  "v_cvt_pk_bf16_f32 v61, v138, v139\n"
  "v_cvt_pk_bf16_f32 v62, v140, v141\n"
  "v_cvt_pk_bf16_f32 v63, v142, v143\n"
  "v_mfma_f32_32x32x16_bf16 v[112:127], v[200:203], v[56:59], v[112:127]\n"
  "s_nop 0\n"
  "v_mfma_f32_32x32x16_bf16 v[112:127], v[204:207], v[60:63], v[112:127]\n"
  "s_sub_u32 s53, s53, 1\n"
  "s_cmp_lg_u32 s53, 0\n"
  "s_nop 11\n"
  "s_cbranch_scc1 .Lb_safe_loop\n"
  "v_mov_b32 v13, v124\n"
  "v_mov_b32 v14, v124\n"
  "s_nop 1\n"
  "v_permlane32_swap_b32 v13, v14\n"
  "s_nop 1\n"
  "s_branch .Lb_norm\n"
  ".Lb_help:\n"
  "v_lshlrev_b32 v20, 7, v1\n"
  "s_sleep 36\n"
  "s_waitcnt lgkmcnt(0)\n"
  "s_mov_b64 s[36:37], s[16:17]\n"
  "s_mov_b32 s38, 16777216\n"
  "s_mov_b32 s39, 0x20000\n"
  "v_lshrrev_b32 v13, 3, v1\n"
  "v_and_b32 v14, 7, v1\n"
  "v_lshlrev_b32 v15, 12, v13\n"
  "v_lshl_add_u32 v21, v14, 7, v15\n"
  "v_min_u32 v7, 6, v14\n"
  "v_mul_u32_u24 v13, 3584, v13\n"
  "v_lshl_add_u32 v25, v7, 7, v13\n"
  "v_lshrrev_b32 v13, 3, v1\n"
  "v_mul_u32_u24 v13, 896, v13\n"
  "v_lshrrev_b32 v14, 2, v1\n"
  "v_and_b32 v14, 4, v14\n"
  "v_and_b32 v15, 7, v1\n"
  "v_xor_b32 v14, v14, v15\n"
  "v_lshl_add_u32 v26, v14, 4, v13\n"
  "v_add_u32 v27, 6144, v26\n"
  "v_lshrrev_b32 v13, 3, v1\n"
  "v_mul_u32_u24 v13, 3584, v13\n"
  "v_lshrrev_b32 v14, 2, v1\n"
  "v_and_b32 v14, 4, v14\n"
  "v_and_b32 v15, 7, v1\n"
  "v_xor_b32 v14, v14, v15\n"
  "v_lshl_add_u32 v9, v14, 4, v13\n"
  "v_add_u32 v10, 27648, v9\n"
  "v_lshrrev_b32 v13, 3, v1\n"
  "v_mul_u32_u24 v13, 4096, v13\n"
  "v_lshrrev_b32 v14, 2, v1\n"
  "v_and_b32 v14, 4, v14\n"
  "v_and_b32 v15, 7, v1\n"
  "v_xor_b32 v14, v14, v15\n"
  "v_lshl_add_u32 v28, v14, 4, v13\n"
  "v_add_u32 v29, 31744, v28\n"
  "s_mov_b64 s[92:93], s[6:7]\n"
  "s_mov_b32 s94, 401408\n"
  "s_mov_b32 s95, 0x20000\n"
  "s_cmp_ge_u32 s50, 28\n"
  "s_cselect_b32 s94, 0, s94\n"
  "s_mov_b64 s[44:45], s[8:9]\n"
  "s_mov_b32 s46, 7340032\n"
  "s_mov_b32 s47, 0x20000\n"
  "s_mov_b64 s[40:41], s[8:9]\n"
  "s_mov_b32 s42, 7340032\n"
  "s_mov_b32 s43, 0x20000\n"
  "s_cmp_eq_u32 s89, 0\n"
  "s_cselect_b32 s46, 0, s46\n"
  "s_mov_b64 s[96:97], s[12:13]\n"
  "s_mov_b32 s98, 7340032\n"
  "s_mov_b32 s99, 0x20000\n"
  "s_lshl_b32 s53, s86, 7\n"
  "s_add_u32 s70, s53, 0\n"
  "s_add_u32 s71, s53, 229376\n"
  "s_add_u32 s72, s53, 458752\n"
  "s_add_u32 s73, s53, 688128\n"
  "s_add_u32 s78, s53, 917504\n"
  "s_lshl_b32 s53, s88, 7\n"
  "s_add_u32 s74, s53, 0\n"
  "s_add_u32 s75, s53, 262144\n"
  "s_add_u32 s76, s53, 524288\n"
  "s_add_u32 s77, s53, 786432\n"
  "s_add_u32 s79, s53, 1048576\n"
  "s_add_u32 s80, s53, 1310720\n"
  "s_add_u32 s81, s53, 1572864\n"
  "s_lshl_b32 s54, s59, 7\n"
  "s_mov_b32 s66, 0\n"
  "s_add_u32 s20, s54, 0\n"
  "s_mov_b32 s67, 40960\n"
  "s_add_u32 s21, s54, 100352\n"
  "s_mov_b32 s68, 81920\n"
  "s_add_u32 s22, s54, 200704\n"
  "s_mov_b32 s69, 122880\n"
  "s_add_u32 s23, s54, 301056\n"
  "v_and_b32 v12, 7, v1\n"
  "s_lshr_b32 s53, s50, 3\n"
  "s_lshl_b32 s53, s53, 9\n"
  "s_and_b32 s54, s50, 7\n"
  "s_lshl_b32 s54, s54, 3\n"
  "s_add_u32 s53, s53, s54\n"
  "s_mul_i32 s53, s53, 3584\n"
  "s_lshr_b32 s54, s49, 1\n"
  "s_mul_i32 s54, s54, 896\n"
  "s_add_u32 s64, s53, s54\n"
  "s_cmp_lg_u32 s50, 5\n"
  "s_cbranch_scc1 .Lb_h5\n"
  "s_mov_b64 s[84:85], s[10:11]\n"
  "s_mov_b32 s86, 3584\n"
  "s_mov_b32 s87, 0x20000\n"
  "buffer_load_dword v22, v20, s[84:87], 0 offen\n"
  "s_mov_b64 s[84:85], s[14:15]\n"
  "s_mov_b32 s86, 4096\n"
  "s_mov_b32 s87, 0x20000\n"
  "buffer_load_dword v22, v20, s[84:87], 0 offen\n"
  "s_mov_b64 s[84:85], s[18:19]\n"
  "s_mov_b32 s86, 1835008\n"
  "s_mov_b32 s87, 0x20000\n"
  "v_mul_u32_u24 v11, 114688, v1\n"
  "s_mov_b64 exec, 0xffff\n"
  "buffer_load_dword v22, v11, s[84:87], 0 offen\n"
  "s_mov_b64 exec, -1\n"
  ".Lb_h5:\n"
  "s_mov_b64 s[84:85], s[6:7]\n"
  "s_mov_b32 s86, 401408\n"
  "s_mov_b32 s87, 0x20000\n"
  "s_mul_i32 s54, s50, 12544\n"
  "s_and_b32 s83, s49, 1\n"
  "s_mul_i32 s83, s83, 4352\n"
  "s_add_u32 s83, s54, s83\n"
  "s_bfm_b64 exec, 16, 0\n"
  "buffer_load_dword v22, v20, s[84:87], s83 offen\n"
  "s_mov_b64 exec, -1\n"
  "s_sleep 1\n"
  "s_bfm_b64 exec, 16, 16\n"
  "buffer_load_dword v22, v20, s[84:87], s83 offen\n"
  "s_mov_b64 exec, -1\n"
  "s_sleep 1\n"
  "s_bfm_b64 exec, 16, 32\n"
  "buffer_load_dword v22, v20, s[84:87], s83 offen\n"
  "s_mov_b64 exec, -1\n"
  "s_sleep 1\n"
  "s_bfm_b64 exec, 16, 48\n"
  "buffer_load_dword v22, v20, s[84:87], s83 offen\n"
  "s_mov_b64 exec, -1\n"
  "s_add_u32 s54, s54, 4352\n"
  "s_sleep 1\n"
  "s_sleep 1\n"
  "s_and_b32 s83, s49, 1\n"
  "s_mul_i32 s83, s83, 4352\n"
  "s_sub_u32 s83, s54, s83\n"
  "s_bfm_b64 exec, 16, 0\n"
  "buffer_load_dword v22, v20, s[84:87], s83 offen\n"
  "s_mov_b64 exec, -1\n"
  "s_sleep 1\n"
  "s_bfm_b64 exec, 16, 16\n"
  "buffer_load_dword v22, v20, s[84:87], s83 offen\n"
  "s_mov_b64 exec, -1\n"
  "s_sleep 1\n"
  "s_bfm_b64 exec, 16, 32\n"
  "buffer_load_dword v22, v20, s[84:87], s83 offen\n"
  "s_mov_b64 exec, -1\n"
  "s_sleep 1\n"
  "s_sleep 1\n"
  "s_bfm_b64 exec, 16, 48\n"
  "buffer_load_dword v22, v20, s[84:87], s83 offen\n"
  "s_mov_b64 exec, -1\n"
  "s_add_u32 s54, s54, 4352\n"
  "s_sleep 1\n"
  "s_and_b32 s83, s49, 1\n"
  "s_mul_i32 s83, s83, 229376\n"
  "s_add_u32 s83, s64, s83\n"
  "s_bfm_b64 exec, 16, 0\n"
  "buffer_load_dword v22, v25, s[40:43], s83 offen\n"
  "s_mov_b64 exec, -1\n"
  "s_sleep 1\n"
  "s_bfm_b64 exec, 16, 16\n"
  "buffer_load_dword v22, v25, s[40:43], s83 offen\n"
  "s_mov_b64 exec, -1\n"
  "s_sleep 1\n"
  "s_bfm_b64 exec, 16, 32\n"
  "buffer_load_dword v22, v25, s[40:43], s83 offen\n"
  "s_mov_b64 exec, -1\n"
  "s_add_u32 m0, s66, 24576\n"
  "s_add_u32 s52, s20, 0\n"
  "buffer_load_dwordx4 v26, s[92:95], s52 offen lds\n"
  "buffer_load_dwordx4 v27, s[92:95], s52 offen offset:1024 lds\n"
  "s_sleep 1\n"
  "s_add_u32 m0, s67, 24576\n"
  "s_add_u32 s52, s21, 0\n"
  "buffer_load_dwordx4 v26, s[92:95], s52 offen lds\n"
  "buffer_load_dwordx4 v27, s[92:95], s52 offen offset:1024 lds\n"
  "s_sleep 1\n"
  "s_bfm_b64 exec, 16, 48\n"
  "buffer_load_dword v22, v25, s[40:43], s83 offen\n"
  "s_mov_b64 exec, -1\n"
  "s_add_u32 s64, s64, 229376\n"
  "s_add_u32 m0, s68, 24576\n"
  "s_add_u32 s52, s22, 0\n"
  "buffer_load_dwordx4 v26, s[92:95], s52 offen lds\n"
  "buffer_load_dwordx4 v27, s[92:95], s52 offen offset:1024 lds\n"
  "s_waitcnt vmcnt(15)\n"
  "s_sleep 1\n"
  "s_and_b32 s83, s49, 1\n"
  "s_mul_i32 s83, s83, 229376\n"
  "s_sub_u32 s83, s64, s83\n"
  "s_bfm_b64 exec, 16, 0\n"
  "buffer_load_dword v22, v25, s[40:43], s83 offen\n"
  "s_mov_b64 exec, -1\n"
  "s_add_u32 m0, s69, 24576\n"
  "s_add_u32 s52, s23, 0\n"
  "buffer_load_dwordx4 v26, s[92:95], s52 offen lds\n"
  "buffer_load_dwordx4 v27, s[92:95], s52 offen offset:1024 lds\n"
  "s_add_u32 m0, s66, 26624\n"
  "s_add_u32 s52, s20, 14336\n"
  "buffer_load_dwordx4 v26, s[92:95], s52 offen lds\n"
  "buffer_load_dwordx4 v27, s[92:95], s52 offen offset:1024 lds\n"
  "s_waitcnt vmcnt(15)\n"
  "s_sleep 1\n"
  "s_bfm_b64 exec, 16, 16\n"
  "buffer_load_dword v22, v25, s[40:43], s83 offen\n"
  "s_mov_b64 exec, -1\n"
  "s_add_u32 m0, s67, 26624\n"
  "s_add_u32 s52, s21, 14336\n"
  "buffer_load_dwordx4 v26, s[92:95], s52 offen lds\n"
  "buffer_load_dwordx4 v27, s[92:95], s52 offen offset:1024 lds\n"
  "s_waitcnt vmcnt(15)\n"
  "s_sleep 1\n"
  "s_add_u32 m0, s68, 26624\n"
  "s_add_u32 s52, s22, 14336\n"
  "buffer_load_dwordx4 v26, s[92:95], s52 offen lds\n"
  "buffer_load_dwordx4 v27, s[92:95], s52 offen offset:1024 lds\n"
  "s_waitcnt vmcnt(15)\n"
  "s_sleep 1\n"
  "s_bfm_b64 exec, 16, 32\n"
  "buffer_load_dword v22, v25, s[40:43], s83 offen\n"
  "s_mov_b64 exec, -1\n"
  "s_add_u32 m0, s69, 26624\n"
  "s_add_u32 s52, s23, 14336\n"
  "buffer_load_dwordx4 v26, s[92:95], s52 offen lds\n"
  "buffer_load_dwordx4 v27, s[92:95], s52 offen offset:1024 lds\n"
  "s_waitcnt vmcnt(15)\n"
  "s_sleep 1\n"
  "s_bfm_b64 exec, 16, 48\n"
  "buffer_load_dword v22, v25, s[40:43], s83 offen\n"
  "s_mov_b64 exec, -1\n"
  "s_add_u32 s64, s64, 229376\n"
  "s_add_u32 m0, s66, 28672\n"
  "s_add_u32 s52, s20, 25088\n"
  "buffer_load_dwordx4 v26, s[92:95], s52 offen lds\n"
  "buffer_load_dwordx4 v27, s[92:95], s52 offen offset:1024 lds\n"
  "s_add_u32 m0, s67, 28672\n"
  "s_add_u32 s52, s21, 25088\n"
  "buffer_load_dwordx4 v26, s[92:95], s52 offen lds\n"
  "buffer_load_dwordx4 v27, s[92:95], s52 offen offset:1024 lds\n"
  "s_waitcnt vmcnt(15)\n"
  "s_sleep 1\n"
  "s_and_b32 s83, s49, 1\n"
  "s_mul_i32 s83, s83, 229376\n"
  "s_add_u32 s83, s64, s83\n"
  "s_bfm_b64 exec, 16, 0\n"
  "buffer_load_dword v22, v25, s[40:43], s83 offen\n"
  "s_mov_b64 exec, -1\n"
  "s_add_u32 m0, s68, 28672\n"
  "s_add_u32 s52, s22, 25088\n"
  "buffer_load_dwordx4 v26, s[92:95], s52 offen lds\n"
  "buffer_load_dwordx4 v27, s[92:95], s52 offen offset:1024 lds\n"
  "s_waitcnt vmcnt(15)\n"
  "s_sleep 1\n"
  "s_add_u32 m0, s69, 28672\n"
  "s_add_u32 s52, s23, 25088\n"
  "buffer_load_dwordx4 v26, s[92:95], s52 offen lds\n"
  "buffer_load_dwordx4 v27, s[92:95], s52 offen offset:1024 lds\n"
  "s_waitcnt vmcnt(15)\n"
  "s_sleep 1\n"
  "s_bfm_b64 exec, 16, 16\n"
  "buffer_load_dword v22, v25, s[40:43], s83 offen\n"
  "s_mov_b64 exec, -1\n"
  "s_add_u32 m0, s66, 30720\n"
  "s_add_u32 s52, s20, 39424\n"
  "buffer_load_dwordx4 v26, s[92:95], s52 offen lds\n"
  "buffer_load_dwordx4 v27, s[92:95], s52 offen offset:1024 lds\n"
  "s_add_u32 m0, s67, 30720\n"
  "s_add_u32 s52, s21, 39424\n"
  "buffer_load_dwordx4 v26, s[92:95], s52 offen lds\n"
  "buffer_load_dwordx4 v27, s[92:95], s52 offen offset:1024 lds\n"
  "s_waitcnt vmcnt(15)\n"
  "s_sleep 1\n"
  "s_bfm_b64 exec, 16, 32\n"
  "buffer_load_dword v22, v25, s[40:43], s83 offen\n"
  "s_mov_b64 exec, -1\n"
  "s_add_u32 m0, s68, 30720\n"
  "s_add_u32 s52, s22, 39424\n"
  "buffer_load_dwordx4 v26, s[92:95], s52 offen lds\n"
  "buffer_load_dwordx4 v27, s[92:95], s52 offen offset:1024 lds\n"
  "s_waitcnt vmcnt(15)\n"
  "s_sleep 1\n"
  "s_bfm_b64 exec, 16, 48\n"
  "buffer_load_dword v22, v25, s[40:43], s83 offen\n"
  "s_mov_b64 exec, -1\n"
  "s_add_u32 s64, s64, 229376\n"
  "s_add_u32 m0, s69, 30720\n"
  "s_add_u32 s52, s23, 39424\n"
  "buffer_load_dwordx4 v26, s[92:95], s52 offen lds\n"
  "buffer_load_dwordx4 v27, s[92:95], s52 offen offset:1024 lds\n"
  "s_waitcnt vmcnt(15)\n"
  "s_sleep 1\n"
  "s_and_b32 s83, s49, 1\n"
  "s_mul_i32 s83, s83, 229376\n"
  "s_sub_u32 s83, s64, s83\n"
  "s_bfm_b64 exec, 16, 0\n"
  "buffer_load_dword v22, v25, s[40:43], s83 offen\n"
  "s_mov_b64 exec, -1\n"
  "s_add_u32 m0, s66, 32768\n"
  "s_add_u32 s52, s20, 50176\n"
  "buffer_load_dwordx4 v26, s[92:95], s52 offen lds\n"
  "buffer_load_dwordx4 v27, s[92:95], s52 offen offset:1024 lds\n"
  "s_waitcnt vmcnt(15)\n"
  "s_sleep 1\n"
  "s_add_u32 m0, s67, 32768\n"
  "s_add_u32 s52, s21, 50176\n"
  "buffer_load_dwordx4 v26, s[92:95], s52 offen lds\n"
  "buffer_load_dwordx4 v27, s[92:95], s52 offen offset:1024 lds\n"
  "s_add_u32 m0, s68, 32768\n"
  "s_add_u32 s52, s22, 50176\n"
  "buffer_load_dwordx4 v26, s[92:95], s52 offen lds\n"
  "buffer_load_dwordx4 v27, s[92:95], s52 offen offset:1024 lds\n"
  "s_waitcnt vmcnt(15)\n"
  "s_sleep 1\n"
  "s_bfm_b64 exec, 16, 16\n"
  "buffer_load_dword v22, v25, s[40:43], s83 offen\n"
  "s_mov_b64 exec, -1\n"
  "s_add_u32 m0, s69, 32768\n"
  "s_add_u32 s52, s23, 50176\n"
  "buffer_load_dwordx4 v26, s[92:95], s52 offen lds\n"
  "buffer_load_dwordx4 v27, s[92:95], s52 offen offset:1024 lds\n"
  "s_waitcnt vmcnt(15)\n"
  "s_sleep 1\n"
  "s_bfm_b64 exec, 16, 32\n"
  "buffer_load_dword v22, v25, s[40:43], s83 offen\n"
  "s_mov_b64 exec, -1\n"
  "s_add_u32 m0, s66, 34816\n"
  "s_add_u32 s52, s20, 64512\n"
  "buffer_load_dwordx4 v26, s[92:95], s52 offen lds\n"
  "buffer_load_dwordx4 v27, s[92:95], s52 offen offset:1024 lds\n"
  "s_waitcnt vmcnt(15)\n"
  "s_sleep 1\n"
  "s_bfm_b64 exec, 16, 48\n"
  "buffer_load_dword v22, v25, s[40:43], s83 offen\n"
  "s_mov_b64 exec, -1\n"
  "s_add_u32 s64, s64, 229376\n"
  "s_add_u32 m0, s67, 34816\n"
  "s_add_u32 s52, s21, 64512\n"
  "buffer_load_dwordx4 v26, s[92:95], s52 offen lds\n"
  "buffer_load_dwordx4 v27, s[92:95], s52 offen offset:1024 lds\n"
  "s_waitcnt vmcnt(15)\n"
  "s_sleep 1\n"
  "s_add_u32 m0, s68, 34816\n"
  "s_add_u32 s52, s22, 64512\n"
  "buffer_load_dwordx4 v26, s[92:95], s52 offen lds\n"
  "buffer_load_dwordx4 v27, s[92:95], s52 offen offset:1024 lds\n"
  "s_add_u32 m0, s69, 34816\n"
  "s_add_u32 s52, s23, 64512\n"
  "buffer_load_dwordx4 v26, s[92:95], s52 offen lds\n"
  "buffer_load_dwordx4 v27, s[92:95], s52 offen offset:1024 lds\n"
  "s_waitcnt vmcnt(15)\n"
  "s_sleep 1\n"
  "s_and_b32 s83, s49, 1\n"
  "s_mul_i32 s83, s83, 229376\n"
  "s_add_u32 s83, s64, s83\n"
  "s_bfm_b64 exec, 16, 0\n"
  "buffer_load_dword v22, v25, s[40:43], s83 offen\n"
  "s_mov_b64 exec, -1\n"
  "s_add_u32 m0, s66, 36864\n"
  "s_add_u32 s52, s20, 75264\n"
  "buffer_load_dwordx4 v26, s[92:95], s52 offen lds\n"
  "buffer_load_dwordx4 v27, s[92:95], s52 offen offset:1024 lds\n"
  "s_waitcnt vmcnt(15)\n"
  "s_sleep 1\n"
  "s_bfm_b64 exec, 16, 16\n"
  "buffer_load_dword v22, v25, s[40:43], s83 offen\n"
  "s_mov_b64 exec, -1\n"
  "s_add_u32 m0, s67, 36864\n"
  "s_add_u32 s52, s21, 75264\n"
  "buffer_load_dwordx4 v26, s[92:95], s52 offen lds\n"
  "buffer_load_dwordx4 v27, s[92:95], s52 offen offset:1024 lds\n"
  "s_waitcnt vmcnt(15)\n"
  "s_sleep 1\n"
  "s_bfm_b64 exec, 16, 32\n"
  "buffer_load_dword v22, v25, s[40:43], s83 offen\n"
  "s_mov_b64 exec, -1\n"
  "s_add_u32 m0, s68, 36864\n"
  "s_add_u32 s52, s22, 75264\n"
  "buffer_load_dwordx4 v26, s[92:95], s52 offen lds\n"
  "buffer_load_dwordx4 v27, s[92:95], s52 offen offset:1024 lds\n"
  "s_add_u32 m0, s69, 36864\n"
  "s_add_u32 s52, s23, 75264\n"
  "buffer_load_dwordx4 v26, s[92:95], s52 offen lds\n"
  "buffer_load_dwordx4 v27, s[92:95], s52 offen offset:1024 lds\n"
  "s_waitcnt vmcnt(15)\n"
  "s_sleep 1\n"
  "s_add_u32 m0, s66, 38912\n"
  "s_add_u32 s52, s20, 89600\n"
  "buffer_load_dwordx4 v26, s[92:95], s52 offen lds\n"
  "buffer_load_dwordx4 v27, s[92:95], s52 offen offset:1024 lds\n"
  "s_waitcnt vmcnt(15)\n"
  "s_sleep 1\n"
  "s_bfm_b64 exec, 16, 48\n"
  "buffer_load_dword v22, v25, s[40:43], s83 offen\n"
  "s_mov_b64 exec, -1\n"
  "s_add_u32 s64, s64, 229376\n"
  "s_add_u32 m0, s67, 38912\n"
  "s_add_u32 s52, s21, 89600\n"
  "buffer_load_dwordx4 v26, s[92:95], s52 offen lds\n"
  "buffer_load_dwordx4 v27, s[92:95], s52 offen offset:1024 lds\n"
  "s_waitcnt vmcnt(15)\n"
  "s_sleep 1\n"
  "s_and_b32 s83, s49, 1\n"
  "s_mul_i32 s83, s83, 229376\n"
  "s_sub_u32 s83, s64, s83\n"
  "s_bfm_b64 exec, 16, 0\n"
  "buffer_load_dword v22, v25, s[40:43], s83 offen\n"
  "s_mov_b64 exec, -1\n"
  "s_add_u32 m0, s68, 38912\n"
  "s_add_u32 s52, s22, 89600\n"
  "buffer_load_dwordx4 v26, s[92:95], s52 offen lds\n"
  "buffer_load_dwordx4 v27, s[92:95], s52 offen offset:1024 lds\n"
  "s_waitcnt vmcnt(15)\n"
  "s_sleep 1\n"
  "s_bfm_b64 exec, 16, 16\n"
  "buffer_load_dword v22, v25, s[40:43], s83 offen\n"
  "s_mov_b64 exec, -1\n"
  "s_add_u32 m0, s69, 38912\n"
  "s_add_u32 s52, s23, 89600\n"
  "buffer_load_dwordx4 v26, s[92:95], s52 offen lds\n"
  "buffer_load_dwordx4 v27, s[92:95], s52 offen offset:1024 lds\n"
  "s_add_u32 m0, s66, 0\n"
  "s_add_u32 s52, s70, 0\n"
  "buffer_load_dwordx4 v9, s[44:47], s52 offen lds\n"
  "buffer_load_dwordx4 v10, s[44:47], s52 offen offset:1024 lds\n"
  "s_waitcnt vmcnt(15)\n"
  "s_sleep 1\n"
  "s_bfm_b64 exec, 16, 32\n"
  "buffer_load_dword v22, v25, s[40:43], s83 offen\n"
  "s_mov_b64 exec, -1\n"
  "s_add_u32 m0, s67, 0\n"
  "s_add_u32 s52, s70, 1835008\n"
  "buffer_load_dwordx4 v9, s[44:47], s52 offen lds\n"
  "buffer_load_dwordx4 v10, s[44:47], s52 offen offset:1024 lds\n"
  "s_waitcnt vmcnt(15)\n"
  "s_sleep 1\n"
  "s_add_u32 m0, s68, 0\n"
  "s_add_u32 s52, s70, 3670016\n"
  "buffer_load_dwordx4 v9, s[44:47], s52 offen lds\n"
  "buffer_load_dwordx4 v10, s[44:47], s52 offen offset:1024 lds\n"
  "s_waitcnt vmcnt(15)\n"
  "s_sleep 1\n"
  "s_bfm_b64 exec, 16, 48\n"
  "buffer_load_dword v22, v25, s[40:43], s83 offen\n"
  "s_mov_b64 exec, -1\n"
  "s_add_u32 s64, s64, 229376\n"
  "s_add_u32 m0, s69, 0\n"
  "s_add_u32 s52, s70, 5505024\n"
  "buffer_load_dwordx4 v9, s[44:47], s52 offen lds\n"
  "buffer_load_dwordx4 v10, s[44:47], s52 offen offset:1024 lds\n"
  "s_waitcnt vmcnt(15)\n"
  "s_sleep 1\n"
  "s_and_b32 s83, s49, 1\n"
  "s_mul_i32 s83, s83, 229376\n"
  "s_add_u32 s83, s64, s83\n"
  "s_bfm_b64 exec, 16, 0\n"
  "buffer_load_dword v22, v25, s[40:43], s83 offen\n"
  "s_mov_b64 exec, -1\n"
  "s_add_u32 m0, s66, 2048\n"
  "s_add_u32 s52, s70, 57344\n"
  "buffer_load_dwordx4 v9, s[44:47], s52 offen lds\n"
  "buffer_load_dwordx4 v10, s[44:47], s52 offen offset:1024 lds\n"
  "s_add_u32 m0, s67, 2048\n"
  "s_add_u32 s52, s70, 1892352\n"
  "buffer_load_dwordx4 v9, s[44:47], s52 offen lds\n"
  "buffer_load_dwordx4 v10, s[44:47], s52 offen offset:1024 lds\n"
  "s_waitcnt vmcnt(15)\n"
  "s_sleep 1\n"
  "s_bfm_b64 exec, 16, 16\n"
  "buffer_load_dword v22, v25, s[40:43], s83 offen\n"
  "s_mov_b64 exec, -1\n"
  "s_add_u32 m0, s68, 2048\n"
  "s_add_u32 s52, s70, 3727360\n"
  "buffer_load_dwordx4 v9, s[44:47], s52 offen lds\n"
  "buffer_load_dwordx4 v10, s[44:47], s52 offen offset:1024 lds\n"
  "s_waitcnt vmcnt(15)\n"
  "s_sleep 1\n"
  "s_add_u32 m0, s69, 2048\n"
  "s_add_u32 s52, s70, 5562368\n"
  "buffer_load_dwordx4 v9, s[44:47], s52 offen lds\n"
  "buffer_load_dwordx4 v10, s[44:47], s52 offen offset:1024 lds\n"
  "s_waitcnt vmcnt(15)\n"
  "s_sleep 1\n"
  "s_bfm_b64 exec, 16, 32\n"
  "buffer_load_dword v22, v25, s[40:43], s83 offen\n"
  "s_mov_b64 exec, -1\n"
  "s_add_u32 m0, s66, 4096\n"
  "s_add_u32 s52, s70, 114688\n"
  "buffer_load_dwordx4 v9, s[44:47], s52 offen lds\n"
  "buffer_load_dwordx4 v10, s[44:47], s52 offen offset:1024 lds\n"
  "s_add_u32 m0, s67, 4096\n"
  "s_add_u32 s52, s70, 1949696\n"
  "buffer_load_dwordx4 v9, s[44:47], s52 offen lds\n"
  "buffer_load_dwordx4 v10, s[44:47], s52 offen offset:1024 lds\n"
  "s_waitcnt vmcnt(15)\n"
  "s_sleep 1\n"
  "s_bfm_b64 exec, 16, 48\n"
  "buffer_load_dword v22, v25, s[40:43], s83 offen\n"
  "s_mov_b64 exec, -1\n"
  "s_add_u32 s64, s64, 229376\n"
  "s_add_u32 m0, s68, 4096\n"
  "s_add_u32 s52, s70, 3784704\n"
  "buffer_load_dwordx4 v9, s[44:47], s52 offen lds\n"
  "buffer_load_dwordx4 v10, s[44:47], s52 offen offset:1024 lds\n"
  "s_waitcnt vmcnt(15)\n"
  "s_sleep 1\n"
  "s_and_b32 s83, s49, 1\n"
  "s_mul_i32 s83, s83, 229376\n"
  "s_sub_u32 s83, s64, s83\n"
  "s_bfm_b64 exec, 16, 0\n"
  "buffer_load_dword v22, v25, s[40:43], s83 offen\n"
  "s_mov_b64 exec, -1\n"
  "s_add_u32 m0, s69, 4096\n"
  "s_add_u32 s52, s70, 5619712\n"
  "buffer_load_dwordx4 v9, s[44:47], s52 offen lds\n"
  "buffer_load_dwordx4 v10, s[44:47], s52 offen offset:1024 lds\n"
  "s_waitcnt vmcnt(15)\n"
  "s_sleep 1\n"
  "s_add_u32 m0, s66, 6144\n"
  "s_add_u32 s52, s70, 172032\n"
  "buffer_load_dwordx4 v9, s[44:47], s52 offen lds\n"
  "buffer_load_dwordx4 v10, s[44:47], s52 offen offset:1024 lds\n"
  "s_waitcnt vmcnt(15)\n"
  "s_sleep 1\n"
  "s_bfm_b64 exec, 16, 16\n"
  "buffer_load_dword v22, v25, s[40:43], s83 offen\n"
  "s_mov_b64 exec, -1\n"
  "s_add_u32 m0, s67, 6144\n"
  "s_add_u32 s52, s70, 2007040\n"
  "buffer_load_dwordx4 v9, s[44:47], s52 offen lds\n"
  "buffer_load_dwordx4 v10, s[44:47], s52 offen offset:1024 lds\n"
  "s_add_u32 m0, s68, 6144\n"
  "s_add_u32 s52, s70, 3842048\n"
  "buffer_load_dwordx4 v9, s[44:47], s52 offen lds\n"
  "buffer_load_dwordx4 v10, s[44:47], s52 offen offset:1024 lds\n"
  "s_waitcnt vmcnt(15)\n"
  "s_sleep 1\n"
  "s_bfm_b64 exec, 16, 32\n"
  "buffer_load_dword v22, v25, s[40:43], s83 offen\n"
  "s_mov_b64 exec, -1\n"
  "s_add_u32 m0, s69, 6144\n"
  "s_add_u32 s52, s70, 5677056\n"
  "buffer_load_dwordx4 v9, s[44:47], s52 offen lds\n"
  "buffer_load_dwordx4 v10, s[44:47], s52 offen offset:1024 lds\n"
  "s_waitcnt vmcnt(15)\n"
  "s_sleep 1\n"
  "s_bfm_b64 exec, 16, 48\n"
  "buffer_load_dword v22, v25, s[40:43], s83 offen\n"
  "s_mov_b64 exec, -1\n"
  "s_add_u32 s64, s64, 229376\n"
  "s_add_u32 m0, s66, 8192\n"
  "s_add_u32 s52, s71, 0\n"
  "buffer_load_dwordx4 v9, s[44:47], s52 offen lds\n"
  "buffer_load_dwordx4 v10, s[44:47], s52 offen offset:1024 lds\n"
  "s_waitcnt vmcnt(15)\n"
  "s_sleep 1\n"
  "s_add_u32 m0, s67, 8192\n"
  "s_add_u32 s52, s71, 1835008\n"
  "buffer_load_dwordx4 v9, s[44:47], s52 offen lds\n"
  "buffer_load_dwordx4 v10, s[44:47], s52 offen offset:1024 lds\n"
  "s_waitcnt vmcnt(15)\n"
  "s_sleep 1\n"
  "s_add_u32 m0, s68, 8192\n"
  "s_add_u32 s52, s71, 3670016\n"
  "buffer_load_dwordx4 v9, s[44:47], s52 offen lds\n"
  "buffer_load_dwordx4 v10, s[44:47], s52 offen offset:1024 lds\n"
  "s_add_u32 m0, s69, 8192\n"
  "s_add_u32 s52, s71, 5505024\n"
  "buffer_load_dwordx4 v9, s[44:47], s52 offen lds\n"
  "buffer_load_dwordx4 v10, s[44:47], s52 offen offset:1024 lds\n"
  "s_waitcnt vmcnt(15)\n"
  "s_sleep 1\n"
  "s_add_u32 m0, s66, 10240\n"
  "s_add_u32 s52, s71, 57344\n"
  "buffer_load_dwordx4 v9, s[44:47], s52 offen lds\n"
  "buffer_load_dwordx4 v10, s[44:47], s52 offen offset:1024 lds\n"
  "s_waitcnt vmcnt(15)\n"
  "s_sleep 1\n"
  "s_add_u32 m0, s67, 10240\n"
  "s_add_u32 s52, s71, 1892352\n"
  "buffer_load_dwordx4 v9, s[44:47], s52 offen lds\n"
  "buffer_load_dwordx4 v10, s[44:47], s52 offen offset:1024 lds\n"
  "s_waitcnt vmcnt(15)\n"
  "s_sleep 1\n"
  "s_add_u32 m0, s68, 10240\n"
  "s_add_u32 s52, s71, 3727360\n"
  "buffer_load_dwordx4 v9, s[44:47], s52 offen lds\n"
  "buffer_load_dwordx4 v10, s[44:47], s52 offen offset:1024 lds\n"
  "s_add_u32 m0, s69, 10240\n"
  "s_add_u32 s52, s71, 5562368\n"
  "buffer_load_dwordx4 v9, s[44:47], s52 offen lds\n"
  "buffer_load_dwordx4 v10, s[44:47], s52 offen offset:1024 lds\n"
  "s_waitcnt vmcnt(15)\n"
  "s_sleep 1\n"
  "s_waitcnt vmcnt(0)\n"
  ".Lb_h7:\n"
  "s_barrier\n"
  "s_cmp_eq_u32 s89, 0\n"
  "s_cbranch_scc1 .Lb_hspare\n"
  "s_barrier\n"
  "s_add_u32 m0, s66, 12288\n"
  "s_add_u32 s52, s71, 114688\n"
  "buffer_load_dwordx4 v9, s[44:47], s52 offen lds\n"
  "buffer_load_dwordx4 v10, s[44:47], s52 offen offset:1024 lds\n"
  "s_add_u32 m0, s67, 12288\n"
  "s_add_u32 s52, s71, 1949696\n"
  "buffer_load_dwordx4 v9, s[44:47], s52 offen lds\n"
  "buffer_load_dwordx4 v10, s[44:47], s52 offen offset:1024 lds\n"
  "s_add_u32 m0, s68, 12288\n"
  "s_add_u32 s52, s71, 3784704\n"
  "buffer_load_dwordx4 v9, s[44:47], s52 offen lds\n"
  "buffer_load_dwordx4 v10, s[44:47], s52 offen offset:1024 lds\n"
  "s_add_u32 m0, s69, 12288\n"
  "s_add_u32 s52, s71, 5619712\n"
  "buffer_load_dwordx4 v9, s[44:47], s52 offen lds\n"
  "buffer_load_dwordx4 v10, s[44:47], s52 offen offset:1024 lds\n"
  "s_add_u32 m0, s66, 14336\n"
  "s_add_u32 s52, s71, 172032\n"
  "buffer_load_dwordx4 v9, s[44:47], s52 offen lds\n"
  "buffer_load_dwordx4 v10, s[44:47], s52 offen offset:1024 lds\n"
  "s_add_u32 m0, s67, 14336\n"
  "s_add_u32 s52, s71, 2007040\n"
  "buffer_load_dwordx4 v9, s[44:47], s52 offen lds\n"
  "buffer_load_dwordx4 v10, s[44:47], s52 offen offset:1024 lds\n"
  "s_add_u32 m0, s68, 14336\n"
  "s_add_u32 s52, s71, 3842048\n"
  "buffer_load_dwordx4 v9, s[44:47], s52 offen lds\n"
  "buffer_load_dwordx4 v10, s[44:47], s52 offen offset:1024 lds\n"
  "s_add_u32 m0, s69, 14336\n"
  "s_add_u32 s52, s71, 5677056\n"
  "buffer_load_dwordx4 v9, s[44:47], s52 offen lds\n"
  "buffer_load_dwordx4 v10, s[44:47], s52 offen offset:1024 lds\n"
  "s_add_u32 m0, s66, 16384\n"
  "s_add_u32 s52, s72, 0\n"
  "buffer_load_dwordx4 v9, s[44:47], s52 offen lds\n"
  "buffer_load_dwordx4 v10, s[44:47], s52 offen offset:1024 lds\n"
  "s_add_u32 m0, s67, 16384\n"
  "s_add_u32 s52, s72, 1835008\n"
  "buffer_load_dwordx4 v9, s[44:47], s52 offen lds\n"
  "buffer_load_dwordx4 v10, s[44:47], s52 offen offset:1024 lds\n"
  "s_add_u32 m0, s68, 16384\n"
  "s_add_u32 s52, s72, 3670016\n"
  "buffer_load_dwordx4 v9, s[44:47], s52 offen lds\n"
  "buffer_load_dwordx4 v10, s[44:47], s52 offen offset:1024 lds\n"
  "s_add_u32 m0, s69, 16384\n"
  "s_add_u32 s52, s72, 5505024\n"
  "buffer_load_dwordx4 v9, s[44:47], s52 offen lds\n"
  "buffer_load_dwordx4 v10, s[44:47], s52 offen offset:1024 lds\n"
  "s_add_u32 m0, s66, 18432\n"
  "s_add_u32 s52, s72, 57344\n"
  "buffer_load_dwordx4 v9, s[44:47], s52 offen lds\n"
  "buffer_load_dwordx4 v10, s[44:47], s52 offen offset:1024 lds\n"
  "s_add_u32 m0, s67, 18432\n"
  "s_add_u32 s52, s72, 1892352\n"
  "buffer_load_dwordx4 v9, s[44:47], s52 offen lds\n"
  "buffer_load_dwordx4 v10, s[44:47], s52 offen offset:1024 lds\n"
  "s_add_u32 m0, s68, 18432\n"
  "s_add_u32 s52, s72, 3727360\n"
  "buffer_load_dwordx4 v9, s[44:47], s52 offen lds\n"
  "buffer_load_dwordx4 v10, s[44:47], s52 offen offset:1024 lds\n"
  "s_add_u32 m0, s69, 18432\n"
  "s_add_u32 s52, s72, 5562368\n"
  "buffer_load_dwordx4 v9, s[44:47], s52 offen lds\n"
  "buffer_load_dwordx4 v10, s[44:47], s52 offen offset:1024 lds\n"
  "s_add_u32 m0, s66, 20480\n"
  "s_add_u32 s52, s72, 114688\n"
  "buffer_load_dwordx4 v9, s[44:47], s52 offen lds\n"
  "buffer_load_dwordx4 v10, s[44:47], s52 offen offset:1024 lds\n"
  "s_add_u32 m0, s67, 20480\n"
  "s_add_u32 s52, s72, 1949696\n"
  "buffer_load_dwordx4 v9, s[44:47], s52 offen lds\n"
  "buffer_load_dwordx4 v10, s[44:47], s52 offen offset:1024 lds\n"
  "s_add_u32 m0, s68, 20480\n"
  "s_add_u32 s52, s72, 3784704\n"
  "buffer_load_dwordx4 v9, s[44:47], s52 offen lds\n"
  "buffer_load_dwordx4 v10, s[44:47], s52 offen offset:1024 lds\n"
  "s_add_u32 m0, s69, 20480\n"
  "s_add_u32 s52, s72, 5619712\n"
  "buffer_load_dwordx4 v9, s[44:47], s52 offen lds\n"
  "buffer_load_dwordx4 v10, s[44:47], s52 offen offset:1024 lds\n"
  "s_add_u32 m0, s66, 22528\n"
  "s_add_u32 s52, s72, 172032\n"
  "buffer_load_dwordx4 v9, s[44:47], s52 offen lds\n"
  "buffer_load_dwordx4 v10, s[44:47], s52 offen offset:1024 lds\n"
  "s_add_u32 m0, s67, 22528\n"
  "s_add_u32 s52, s72, 2007040\n"
  "buffer_load_dwordx4 v9, s[44:47], s52 offen lds\n"
  "buffer_load_dwordx4 v10, s[44:47], s52 offen offset:1024 lds\n"
  "s_add_u32 m0, s68, 22528\n"
  "s_add_u32 s52, s72, 3842048\n"
  "buffer_load_dwordx4 v9, s[44:47], s52 offen lds\n"
  "buffer_load_dwordx4 v10, s[44:47], s52 offen offset:1024 lds\n"
  "s_add_u32 m0, s69, 22528\n"
  "s_add_u32 s52, s72, 5677056\n"
  "buffer_load_dwordx4 v9, s[44:47], s52 offen lds\n"
  "buffer_load_dwordx4 v10, s[44:47], s52 offen offset:1024 lds\n"
  "s_waitcnt vmcnt(0)\n"
  "s_barrier\n"
  "s_add_u32 m0, s66, 36864\n"
  "s_add_u32 s52, s78, 114688\n"
  "buffer_load_dwordx4 v9, s[44:47], s52 offen lds\n"
  "buffer_load_dwordx4 v10, s[44:47], s52 offen offset:1024 lds\n"
  "s_add_u32 m0, s67, 36864\n"
  "s_add_u32 s52, s78, 1949696\n"
  "buffer_load_dwordx4 v9, s[44:47], s52 offen lds\n"
  "buffer_load_dwordx4 v10, s[44:47], s52 offen offset:1024 lds\n"
  "s_add_u32 m0, s68, 36864\n"
  "s_add_u32 s52, s78, 3784704\n"
  "buffer_load_dwordx4 v9, s[44:47], s52 offen lds\n"
  "buffer_load_dwordx4 v10, s[44:47], s52 offen offset:1024 lds\n"
  "s_add_u32 m0, s69, 36864\n"
  "s_add_u32 s52, s78, 5619712\n"
  "buffer_load_dwordx4 v9, s[44:47], s52 offen lds\n"
  "buffer_load_dwordx4 v10, s[44:47], s52 offen offset:1024 lds\n"
  "s_barrier\n"
  "s_sleep 2\n"
  "s_add_u32 m0, s66, 24576\n"
  "s_add_u32 s52, s73, 0\n"
  "buffer_load_dwordx4 v9, s[44:47], s52 offen lds\n"
  "buffer_load_dwordx4 v10, s[44:47], s52 offen offset:1024 lds\n"
  "s_add_u32 m0, s67, 24576\n"
  "s_add_u32 s52, s73, 1835008\n"
  "buffer_load_dwordx4 v9, s[44:47], s52 offen lds\n"
  "buffer_load_dwordx4 v10, s[44:47], s52 offen offset:1024 lds\n"
  "s_add_u32 m0, s68, 24576\n"
  "s_add_u32 s52, s73, 3670016\n"
  "buffer_load_dwordx4 v9, s[44:47], s52 offen lds\n"
  "buffer_load_dwordx4 v10, s[44:47], s52 offen offset:1024 lds\n"
  "s_add_u32 m0, s69, 24576\n"
  "s_add_u32 s52, s73, 5505024\n"
  "buffer_load_dwordx4 v9, s[44:47], s52 offen lds\n"
  "buffer_load_dwordx4 v10, s[44:47], s52 offen offset:1024 lds\n"
  "s_add_u32 m0, s66, 26624\n"
  "s_add_u32 s52, s73, 57344\n"
  "buffer_load_dwordx4 v9, s[44:47], s52 offen lds\n"
  "buffer_load_dwordx4 v10, s[44:47], s52 offen offset:1024 lds\n"
  "s_add_u32 m0, s67, 26624\n"
  "s_add_u32 s52, s73, 1892352\n"
  "buffer_load_dwordx4 v9, s[44:47], s52 offen lds\n"
  "buffer_load_dwordx4 v10, s[44:47], s52 offen offset:1024 lds\n"
  "s_add_u32 m0, s68, 26624\n"
  "s_add_u32 s52, s73, 3727360\n"
  "buffer_load_dwordx4 v9, s[44:47], s52 offen lds\n"
  "buffer_load_dwordx4 v10, s[44:47], s52 offen offset:1024 lds\n"
  "s_add_u32 m0, s69, 26624\n"
  "s_add_u32 s52, s73, 5562368\n"
  "buffer_load_dwordx4 v9, s[44:47], s52 offen lds\n"
  "buffer_load_dwordx4 v10, s[44:47], s52 offen offset:1024 lds\n"
  "s_add_u32 m0, s66, 28672\n"
  "s_add_u32 s52, s73, 114688\n"
  "buffer_load_dwordx4 v9, s[44:47], s52 offen lds\n"
  "buffer_load_dwordx4 v10, s[44:47], s52 offen offset:1024 lds\n"
  "s_add_u32 m0, s67, 28672\n"
  "s_add_u32 s52, s73, 1949696\n"
  "buffer_load_dwordx4 v9, s[44:47], s52 offen lds\n"
  "buffer_load_dwordx4 v10, s[44:47], s52 offen offset:1024 lds\n"
  "s_add_u32 m0, s68, 28672\n"
  "s_add_u32 s52, s73, 3784704\n"
  "buffer_load_dwordx4 v9, s[44:47], s52 offen lds\n"
  "buffer_load_dwordx4 v10, s[44:47], s52 offen offset:1024 lds\n"
  "s_add_u32 m0, s69, 28672\n"
  "s_add_u32 s52, s73, 5619712\n"
  "buffer_load_dwordx4 v9, s[44:47], s52 offen lds\n"
  "buffer_load_dwordx4 v10, s[44:47], s52 offen offset:1024 lds\n"
  "s_add_u32 m0, s66, 30720\n"
  "s_add_u32 s52, s73, 172032\n"
  "buffer_load_dwordx4 v9, s[44:47], s52 offen lds\n"
  "buffer_load_dwordx4 v10, s[44:47], s52 offen offset:1024 lds\n"
  "s_add_u32 m0, s67, 30720\n"
  "s_add_u32 s52, s73, 2007040\n"
  "buffer_load_dwordx4 v9, s[44:47], s52 offen lds\n"
  "buffer_load_dwordx4 v10, s[44:47], s52 offen offset:1024 lds\n"
  "s_add_u32 m0, s68, 30720\n"
  "s_add_u32 s52, s73, 3842048\n"
  "buffer_load_dwordx4 v9, s[44:47], s52 offen lds\n"
  "buffer_load_dwordx4 v10, s[44:47], s52 offen offset:1024 lds\n"
  "s_add_u32 m0, s69, 30720\n"
  "s_add_u32 s52, s73, 5677056\n"
  "buffer_load_dwordx4 v9, s[44:47], s52 offen lds\n"
  "buffer_load_dwordx4 v10, s[44:47], s52 offen offset:1024 lds\n"
  "s_add_u32 m0, s66, 32768\n"
  "s_add_u32 s52, s78, 0\n"
  "buffer_load_dwordx4 v9, s[44:47], s52 offen lds\n"
  "buffer_load_dwordx4 v10, s[44:47], s52 offen offset:1024 lds\n"
  "s_add_u32 m0, s67, 32768\n"
  "s_add_u32 s52, s78, 1835008\n"
  "buffer_load_dwordx4 v9, s[44:47], s52 offen lds\n"
  "buffer_load_dwordx4 v10, s[44:47], s52 offen offset:1024 lds\n"
  "s_add_u32 m0, s68, 32768\n"
  "s_add_u32 s52, s78, 3670016\n"
  "buffer_load_dwordx4 v9, s[44:47], s52 offen lds\n"
  "buffer_load_dwordx4 v10, s[44:47], s52 offen offset:1024 lds\n"
  "s_add_u32 m0, s69, 32768\n"
  "s_add_u32 s52, s78, 5505024\n"
  "buffer_load_dwordx4 v9, s[44:47], s52 offen lds\n"
  "buffer_load_dwordx4 v10, s[44:47], s52 offen offset:1024 lds\n"
  "s_add_u32 m0, s66, 34816\n"
  "s_add_u32 s52, s78, 57344\n"
  "buffer_load_dwordx4 v9, s[44:47], s52 offen lds\n"
  "buffer_load_dwordx4 v10, s[44:47], s52 offen offset:1024 lds\n"
  "s_add_u32 m0, s67, 34816\n"
  "s_add_u32 s52, s78, 1892352\n"
  "buffer_load_dwordx4 v9, s[44:47], s52 offen lds\n"
  "buffer_load_dwordx4 v10, s[44:47], s52 offen offset:1024 lds\n"
  "s_add_u32 m0, s68, 34816\n"
  "s_add_u32 s52, s78, 3727360\n"
  "buffer_load_dwordx4 v9, s[44:47], s52 offen lds\n"
  "buffer_load_dwordx4 v10, s[44:47], s52 offen offset:1024 lds\n"
  "s_add_u32 m0, s69, 34816\n"
  "s_add_u32 s52, s78, 5562368\n"
  "buffer_load_dwordx4 v9, s[44:47], s52 offen lds\n"
  "buffer_load_dwordx4 v10, s[44:47], s52 offen offset:1024 lds\n"
  "s_waitcnt vmcnt(0)\n"
  "s_barrier\n"
  "s_barrier\n"
  "s_add_u32 m0, s66, 24576\n"
  "s_add_u32 s52, s74, 0\n"
  "buffer_load_dwordx4 v28, s[96:99], s52 offen lds\n"
  "buffer_load_dwordx4 v29, s[96:99], s52 offen offset:1024 lds\n"
  "s_add_u32 m0, s67, 24576\n"
  "s_add_u32 s52, s74, 1835008\n"
  "buffer_load_dwordx4 v28, s[96:99], s52 offen lds\n"
  "buffer_load_dwordx4 v29, s[96:99], s52 offen offset:1024 lds\n"
  "s_add_u32 m0, s68, 24576\n"
  "s_add_u32 s52, s74, 3670016\n"
  "buffer_load_dwordx4 v28, s[96:99], s52 offen lds\n"
  "buffer_load_dwordx4 v29, s[96:99], s52 offen offset:1024 lds\n"
  "s_add_u32 m0, s69, 24576\n"
  "s_add_u32 s52, s74, 5505024\n"
  "buffer_load_dwordx4 v28, s[96:99], s52 offen lds\n"
  "buffer_load_dwordx4 v29, s[96:99], s52 offen offset:1024 lds\n"
  "s_add_u32 m0, s66, 26624\n"
  "s_add_u32 s52, s74, 65536\n"
  "buffer_load_dwordx4 v28, s[96:99], s52 offen lds\n"
  "buffer_load_dwordx4 v29, s[96:99], s52 offen offset:1024 lds\n"
  "s_add_u32 m0, s67, 26624\n"
  "s_add_u32 s52, s74, 1900544\n"
  "buffer_load_dwordx4 v28, s[96:99], s52 offen lds\n"
  "buffer_load_dwordx4 v29, s[96:99], s52 offen offset:1024 lds\n"
  "s_add_u32 m0, s68, 26624\n"
  "s_add_u32 s52, s74, 3735552\n"
  "buffer_load_dwordx4 v28, s[96:99], s52 offen lds\n"
  "buffer_load_dwordx4 v29, s[96:99], s52 offen offset:1024 lds\n"
  "s_add_u32 m0, s69, 26624\n"
  "s_add_u32 s52, s74, 5570560\n"
  "buffer_load_dwordx4 v28, s[96:99], s52 offen lds\n"
  "buffer_load_dwordx4 v29, s[96:99], s52 offen offset:1024 lds\n"
  "s_add_u32 m0, s66, 28672\n"
  "s_add_u32 s52, s74, 131072\n"
  "buffer_load_dwordx4 v28, s[96:99], s52 offen lds\n"
  "buffer_load_dwordx4 v29, s[96:99], s52 offen offset:1024 lds\n"
  "s_add_u32 m0, s67, 28672\n"
  "s_add_u32 s52, s74, 1966080\n"
  "buffer_load_dwordx4 v28, s[96:99], s52 offen lds\n"
  "buffer_load_dwordx4 v29, s[96:99], s52 offen offset:1024 lds\n"
  "s_add_u32 m0, s68, 28672\n"
  "s_add_u32 s52, s74, 3801088\n"
  "buffer_load_dwordx4 v28, s[96:99], s52 offen lds\n"
  "buffer_load_dwordx4 v29, s[96:99], s52 offen offset:1024 lds\n"
  "s_add_u32 m0, s69, 28672\n"
  "s_add_u32 s52, s74, 5636096\n"
  "buffer_load_dwordx4 v28, s[96:99], s52 offen lds\n"
  "buffer_load_dwordx4 v29, s[96:99], s52 offen offset:1024 lds\n"
  "s_add_u32 m0, s66, 30720\n"
  "s_add_u32 s52, s74, 196608\n"
  "buffer_load_dwordx4 v28, s[96:99], s52 offen lds\n"
  "buffer_load_dwordx4 v29, s[96:99], s52 offen offset:1024 lds\n"
  "s_add_u32 m0, s67, 30720\n"
  "s_add_u32 s52, s74, 2031616\n"
  "buffer_load_dwordx4 v28, s[96:99], s52 offen lds\n"
  "buffer_load_dwordx4 v29, s[96:99], s52 offen offset:1024 lds\n"
  "s_add_u32 m0, s68, 30720\n"
  "s_add_u32 s52, s74, 3866624\n"
  "buffer_load_dwordx4 v28, s[96:99], s52 offen lds\n"
  "buffer_load_dwordx4 v29, s[96:99], s52 offen offset:1024 lds\n"
  "s_add_u32 m0, s69, 30720\n"
  "s_add_u32 s52, s74, 5701632\n"
  "buffer_load_dwordx4 v28, s[96:99], s52 offen lds\n"
  "buffer_load_dwordx4 v29, s[96:99], s52 offen offset:1024 lds\n"
  "s_add_u32 m0, s66, 32768\n"
  "s_add_u32 s52, s75, 0\n"
  "buffer_load_dwordx4 v28, s[96:99], s52 offen lds\n"
  "buffer_load_dwordx4 v29, s[96:99], s52 offen offset:1024 lds\n"
  "s_add_u32 m0, s67, 32768\n"
  "s_add_u32 s52, s75, 1835008\n"
  "buffer_load_dwordx4 v28, s[96:99], s52 offen lds\n"
  "buffer_load_dwordx4 v29, s[96:99], s52 offen offset:1024 lds\n"
  "s_add_u32 m0, s68, 32768\n"
  "s_add_u32 s52, s75, 3670016\n"
  "buffer_load_dwordx4 v28, s[96:99], s52 offen lds\n"
  "buffer_load_dwordx4 v29, s[96:99], s52 offen offset:1024 lds\n"
  "s_add_u32 m0, s69, 32768\n"
  "s_add_u32 s52, s75, 5505024\n"
  "buffer_load_dwordx4 v28, s[96:99], s52 offen lds\n"
  "buffer_load_dwordx4 v29, s[96:99], s52 offen offset:1024 lds\n"
  "s_add_u32 m0, s66, 34816\n"
  "s_add_u32 s52, s75, 65536\n"
  "buffer_load_dwordx4 v28, s[96:99], s52 offen lds\n"
  "buffer_load_dwordx4 v29, s[96:99], s52 offen offset:1024 lds\n"
  "s_add_u32 m0, s67, 34816\n"
  "s_add_u32 s52, s75, 1900544\n"
  "buffer_load_dwordx4 v28, s[96:99], s52 offen lds\n"
  "buffer_load_dwordx4 v29, s[96:99], s52 offen offset:1024 lds\n"
  "s_add_u32 m0, s68, 34816\n"
  "s_add_u32 s52, s75, 3735552\n"
  "buffer_load_dwordx4 v28, s[96:99], s52 offen lds\n"
  "buffer_load_dwordx4 v29, s[96:99], s52 offen offset:1024 lds\n"
  "s_add_u32 m0, s69, 34816\n"
  "s_add_u32 s52, s75, 5570560\n"
  "buffer_load_dwordx4 v28, s[96:99], s52 offen lds\n"
  "buffer_load_dwordx4 v29, s[96:99], s52 offen offset:1024 lds\n"
  "s_add_u32 m0, s66, 36864\n"
  "s_add_u32 s52, s75, 131072\n"
  "buffer_load_dwordx4 v28, s[96:99], s52 offen lds\n"
  "buffer_load_dwordx4 v29, s[96:99], s52 offen offset:1024 lds\n"
  "s_add_u32 m0, s67, 36864\n"
  "s_add_u32 s52, s75, 1966080\n"
  "buffer_load_dwordx4 v28, s[96:99], s52 offen lds\n"
  "buffer_load_dwordx4 v29, s[96:99], s52 offen offset:1024 lds\n"
  "s_add_u32 m0, s68, 36864\n"
  "s_add_u32 s52, s75, 3801088\n"
  "buffer_load_dwordx4 v28, s[96:99], s52 offen lds\n"
  "buffer_load_dwordx4 v29, s[96:99], s52 offen offset:1024 lds\n"
  "s_add_u32 m0, s69, 36864\n"
  "s_add_u32 s52, s75, 5636096\n"
  "buffer_load_dwordx4 v28, s[96:99], s52 offen lds\n"
  "buffer_load_dwordx4 v29, s[96:99], s52 offen offset:1024 lds\n"
  "s_add_u32 m0, s66, 38912\n"
  "s_add_u32 s52, s75, 196608\n"
  "buffer_load_dwordx4 v28, s[96:99], s52 offen lds\n"
  "buffer_load_dwordx4 v29, s[96:99], s52 offen offset:1024 lds\n"
  "s_add_u32 m0, s67, 38912\n"
  "s_add_u32 s52, s75, 2031616\n"
  "buffer_load_dwordx4 v28, s[96:99], s52 offen lds\n"
  "buffer_load_dwordx4 v29, s[96:99], s52 offen offset:1024 lds\n"
  "s_add_u32 m0, s68, 38912\n"
  "s_add_u32 s52, s75, 3866624\n"
  "buffer_load_dwordx4 v28, s[96:99], s52 offen lds\n"
  "buffer_load_dwordx4 v29, s[96:99], s52 offen offset:1024 lds\n"
  "s_add_u32 m0, s69, 38912\n"
  "s_add_u32 s52, s75, 5701632\n"
  "buffer_load_dwordx4 v28, s[96:99], s52 offen lds\n"
  "buffer_load_dwordx4 v29, s[96:99], s52 offen offset:1024 lds\n"
  "s_add_u32 m0, s66, 0\n"
  "s_add_u32 s52, s76, 0\n"
  "buffer_load_dwordx4 v28, s[96:99], s52 offen lds\n"
  "buffer_load_dwordx4 v29, s[96:99], s52 offen offset:1024 lds\n"
  "s_add_u32 m0, s67, 0\n"
  "s_add_u32 s52, s76, 1835008\n"
  "buffer_load_dwordx4 v28, s[96:99], s52 offen lds\n"
  "buffer_load_dwordx4 v29, s[96:99], s52 offen offset:1024 lds\n"
  "s_add_u32 m0, s68, 0\n"
  "s_add_u32 s52, s76, 3670016\n"
  "buffer_load_dwordx4 v28, s[96:99], s52 offen lds\n"
  "buffer_load_dwordx4 v29, s[96:99], s52 offen offset:1024 lds\n"
  "s_add_u32 m0, s69, 0\n"
  "s_add_u32 s52, s76, 5505024\n"
  "buffer_load_dwordx4 v28, s[96:99], s52 offen lds\n"
  "buffer_load_dwordx4 v29, s[96:99], s52 offen offset:1024 lds\n"
  "s_add_u32 m0, s66, 2048\n"
  "s_add_u32 s52, s76, 65536\n"
  "buffer_load_dwordx4 v28, s[96:99], s52 offen lds\n"
  "buffer_load_dwordx4 v29, s[96:99], s52 offen offset:1024 lds\n"
  "s_add_u32 m0, s67, 2048\n"
  "s_add_u32 s52, s76, 1900544\n"
  "buffer_load_dwordx4 v28, s[96:99], s52 offen lds\n"
  "buffer_load_dwordx4 v29, s[96:99], s52 offen offset:1024 lds\n"
  "s_add_u32 m0, s68, 2048\n"
  "s_add_u32 s52, s76, 3735552\n"
  "buffer_load_dwordx4 v28, s[96:99], s52 offen lds\n"
  "buffer_load_dwordx4 v29, s[96:99], s52 offen offset:1024 lds\n"
  "s_add_u32 m0, s69, 2048\n"
  "s_add_u32 s52, s76, 5570560\n"
  "buffer_load_dwordx4 v28, s[96:99], s52 offen lds\n"
  "buffer_load_dwordx4 v29, s[96:99], s52 offen offset:1024 lds\n"
  "s_add_u32 m0, s66, 4096\n"
  "s_add_u32 s52, s76, 131072\n"
  "buffer_load_dwordx4 v28, s[96:99], s52 offen lds\n"
  "buffer_load_dwordx4 v29, s[96:99], s52 offen offset:1024 lds\n"
  "s_add_u32 m0, s67, 4096\n"
  "s_add_u32 s52, s76, 1966080\n"
  "buffer_load_dwordx4 v28, s[96:99], s52 offen lds\n"
  "buffer_load_dwordx4 v29, s[96:99], s52 offen offset:1024 lds\n"
  "s_add_u32 m0, s68, 4096\n"
  "s_add_u32 s52, s76, 3801088\n"
  "buffer_load_dwordx4 v28, s[96:99], s52 offen lds\n"
  "buffer_load_dwordx4 v29, s[96:99], s52 offen offset:1024 lds\n"
  "s_add_u32 m0, s69, 4096\n"
  "s_add_u32 s52, s76, 5636096\n"
  "buffer_load_dwordx4 v28, s[96:99], s52 offen lds\n"
  "buffer_load_dwordx4 v29, s[96:99], s52 offen offset:1024 lds\n"
  "s_barrier\n"
  "s_add_u32 m0, s66, 6144\n"
  "s_add_u32 s52, s76, 196608\n"
  "buffer_load_dwordx4 v28, s[96:99], s52 offen lds\n"
  "buffer_load_dwordx4 v29, s[96:99], s52 offen offset:1024 lds\n"
  "s_add_u32 m0, s67, 6144\n"
  "s_add_u32 s52, s76, 2031616\n"
  "buffer_load_dwordx4 v28, s[96:99], s52 offen lds\n"
  "buffer_load_dwordx4 v29, s[96:99], s52 offen offset:1024 lds\n"
  "s_add_u32 m0, s68, 6144\n"
  "s_add_u32 s52, s76, 3866624\n"
  "buffer_load_dwordx4 v28, s[96:99], s52 offen lds\n"
  "buffer_load_dwordx4 v29, s[96:99], s52 offen offset:1024 lds\n"
  "s_add_u32 m0, s69, 6144\n"
  "s_add_u32 s52, s76, 5701632\n"
  "buffer_load_dwordx4 v28, s[96:99], s52 offen lds\n"
  "buffer_load_dwordx4 v29, s[96:99], s52 offen offset:1024 lds\n"
  "s_add_u32 m0, s66, 8192\n"
  "s_add_u32 s52, s77, 0\n"
  "buffer_load_dwordx4 v28, s[96:99], s52 offen lds\n"
  "buffer_load_dwordx4 v29, s[96:99], s52 offen offset:1024 lds\n"
  "s_add_u32 m0, s67, 8192\n"
  "s_add_u32 s52, s77, 1835008\n"
  "buffer_load_dwordx4 v28, s[96:99], s52 offen lds\n"
  "buffer_load_dwordx4 v29, s[96:99], s52 offen offset:1024 lds\n"
  "s_add_u32 m0, s68, 8192\n"
  "s_add_u32 s52, s77, 3670016\n"
  "buffer_load_dwordx4 v28, s[96:99], s52 offen lds\n"
  "buffer_load_dwordx4 v29, s[96:99], s52 offen offset:1024 lds\n"
  "s_add_u32 m0, s69, 8192\n"
  "s_add_u32 s52, s77, 5505024\n"
  "buffer_load_dwordx4 v28, s[96:99], s52 offen lds\n"
  "buffer_load_dwordx4 v29, s[96:99], s52 offen offset:1024 lds\n"
  "s_waitcnt vmcnt(40)\n"
  "s_barrier\n"
  "s_add_u32 m0, s66, 10240\n"
  "s_add_u32 s52, s77, 65536\n"
  "buffer_load_dwordx4 v28, s[96:99], s52 offen lds\n"
  "buffer_load_dwordx4 v29, s[96:99], s52 offen offset:1024 lds\n"
  "s_add_u32 m0, s67, 10240\n"
  "s_add_u32 s52, s77, 1900544\n"
  "buffer_load_dwordx4 v28, s[96:99], s52 offen lds\n"
  "buffer_load_dwordx4 v29, s[96:99], s52 offen offset:1024 lds\n"
  "s_add_u32 m0, s68, 10240\n"
  "s_add_u32 s52, s77, 3735552\n"
  "buffer_load_dwordx4 v28, s[96:99], s52 offen lds\n"
  "buffer_load_dwordx4 v29, s[96:99], s52 offen offset:1024 lds\n"
  "s_add_u32 m0, s69, 10240\n"
  "s_add_u32 s52, s77, 5570560\n"
  "buffer_load_dwordx4 v28, s[96:99], s52 offen lds\n"
  "buffer_load_dwordx4 v29, s[96:99], s52 offen offset:1024 lds\n"
  "s_add_u32 m0, s66, 12288\n"
  "s_add_u32 s52, s77, 131072\n"
  "buffer_load_dwordx4 v28, s[96:99], s52 offen lds\n"
  "buffer_load_dwordx4 v29, s[96:99], s52 offen offset:1024 lds\n"
  "s_add_u32 m0, s67, 12288\n"
  "s_add_u32 s52, s77, 1966080\n"
  "buffer_load_dwordx4 v28, s[96:99], s52 offen lds\n"
  "buffer_load_dwordx4 v29, s[96:99], s52 offen offset:1024 lds\n"
  "s_add_u32 m0, s68, 12288\n"
  "s_add_u32 s52, s77, 3801088\n"
  "buffer_load_dwordx4 v28, s[96:99], s52 offen lds\n"
  "buffer_load_dwordx4 v29, s[96:99], s52 offen offset:1024 lds\n"
  "s_add_u32 m0, s69, 12288\n"
  "s_add_u32 s52, s77, 5636096\n"
  "buffer_load_dwordx4 v28, s[96:99], s52 offen lds\n"
  "buffer_load_dwordx4 v29, s[96:99], s52 offen offset:1024 lds\n"
  "s_add_u32 m0, s66, 14336\n"
  "s_add_u32 s52, s77, 196608\n"
  "buffer_load_dwordx4 v28, s[96:99], s52 offen lds\n"
  "buffer_load_dwordx4 v29, s[96:99], s52 offen offset:1024 lds\n"
  "s_add_u32 m0, s67, 14336\n"
  "s_add_u32 s52, s77, 2031616\n"
  "buffer_load_dwordx4 v28, s[96:99], s52 offen lds\n"
  "buffer_load_dwordx4 v29, s[96:99], s52 offen offset:1024 lds\n"
  "s_add_u32 m0, s68, 14336\n"
  "s_add_u32 s52, s77, 3866624\n"
  "buffer_load_dwordx4 v28, s[96:99], s52 offen lds\n"
  "buffer_load_dwordx4 v29, s[96:99], s52 offen offset:1024 lds\n"
  "s_add_u32 m0, s69, 14336\n"
  "s_add_u32 s52, s77, 5701632\n"
  "buffer_load_dwordx4 v28, s[96:99], s52 offen lds\n"
  "buffer_load_dwordx4 v29, s[96:99], s52 offen offset:1024 lds\n"
  "s_add_u32 m0, s66, 16384\n"
  "s_add_u32 s52, s79, 0\n"
  "buffer_load_dwordx4 v28, s[96:99], s52 offen lds\n"
  "buffer_load_dwordx4 v29, s[96:99], s52 offen offset:1024 lds\n"
  "s_add_u32 m0, s67, 16384\n"
  "s_add_u32 s52, s79, 1835008\n"
  "buffer_load_dwordx4 v28, s[96:99], s52 offen lds\n"
  "buffer_load_dwordx4 v29, s[96:99], s52 offen offset:1024 lds\n"
  "s_add_u32 m0, s68, 16384\n"
  "s_add_u32 s52, s79, 3670016\n"
  "buffer_load_dwordx4 v28, s[96:99], s52 offen lds\n"
  "buffer_load_dwordx4 v29, s[96:99], s52 offen offset:1024 lds\n"
  "s_add_u32 m0, s69, 16384\n"
  "s_add_u32 s52, s79, 5505024\n"
  "buffer_load_dwordx4 v28, s[96:99], s52 offen lds\n"
  "buffer_load_dwordx4 v29, s[96:99], s52 offen offset:1024 lds\n"
  "s_add_u32 m0, s66, 18432\n"
  "s_add_u32 s52, s79, 65536\n"
  "buffer_load_dwordx4 v28, s[96:99], s52 offen lds\n"
  "buffer_load_dwordx4 v29, s[96:99], s52 offen offset:1024 lds\n"
  "s_add_u32 m0, s67, 18432\n"
  "s_add_u32 s52, s79, 1900544\n"
  "buffer_load_dwordx4 v28, s[96:99], s52 offen lds\n"
  "buffer_load_dwordx4 v29, s[96:99], s52 offen offset:1024 lds\n"
  "s_add_u32 m0, s68, 18432\n"
  "s_add_u32 s52, s79, 3735552\n"
  "buffer_load_dwordx4 v28, s[96:99], s52 offen lds\n"
  "buffer_load_dwordx4 v29, s[96:99], s52 offen offset:1024 lds\n"
  "s_add_u32 m0, s69, 18432\n"
  "s_add_u32 s52, s79, 5570560\n"
  "buffer_load_dwordx4 v28, s[96:99], s52 offen lds\n"
  "buffer_load_dwordx4 v29, s[96:99], s52 offen offset:1024 lds\n"
  "s_add_u32 m0, s66, 20480\n"
  "s_add_u32 s52, s79, 131072\n"
  "buffer_load_dwordx4 v28, s[96:99], s52 offen lds\n"
  "buffer_load_dwordx4 v29, s[96:99], s52 offen offset:1024 lds\n"
  "s_add_u32 m0, s67, 20480\n"
  "s_add_u32 s52, s79, 1966080\n"
  "buffer_load_dwordx4 v28, s[96:99], s52 offen lds\n"
  "buffer_load_dwordx4 v29, s[96:99], s52 offen offset:1024 lds\n"
  "s_add_u32 m0, s68, 20480\n"
  "s_add_u32 s52, s79, 3801088\n"
  "buffer_load_dwordx4 v28, s[96:99], s52 offen lds\n"
  "buffer_load_dwordx4 v29, s[96:99], s52 offen offset:1024 lds\n"
  "s_add_u32 m0, s69, 20480\n"
  "s_add_u32 s52, s79, 5636096\n"
  "buffer_load_dwordx4 v28, s[96:99], s52 offen lds\n"
  "buffer_load_dwordx4 v29, s[96:99], s52 offen offset:1024 lds\n"
  "s_add_u32 m0, s66, 22528\n"
  "s_add_u32 s52, s79, 196608\n"
  "buffer_load_dwordx4 v28, s[96:99], s52 offen lds\n"
  "buffer_load_dwordx4 v29, s[96:99], s52 offen offset:1024 lds\n"
  "s_add_u32 m0, s67, 22528\n"
  "s_add_u32 s52, s79, 2031616\n"
  "buffer_load_dwordx4 v28, s[96:99], s52 offen lds\n"
  "buffer_load_dwordx4 v29, s[96:99], s52 offen offset:1024 lds\n"
  "s_add_u32 m0, s68, 22528\n"
  "s_add_u32 s52, s79, 3866624\n"
  "buffer_load_dwordx4 v28, s[96:99], s52 offen lds\n"
  "buffer_load_dwordx4 v29, s[96:99], s52 offen offset:1024 lds\n"
  "s_add_u32 m0, s69, 22528\n"
  "s_add_u32 s52, s79, 5701632\n"
  "buffer_load_dwordx4 v28, s[96:99], s52 offen lds\n"
  "buffer_load_dwordx4 v29, s[96:99], s52 offen offset:1024 lds\n"
  "s_barrier\n"
  "s_add_u32 m0, s66, 24576\n"
  "s_add_u32 s52, s80, 0\n"
  "buffer_load_dwordx4 v28, s[96:99], s52 offen lds\n"
  "buffer_load_dwordx4 v29, s[96:99], s52 offen offset:1024 lds\n"
  "s_add_u32 m0, s67, 24576\n"
  "s_add_u32 s52, s80, 1835008\n"
  "buffer_load_dwordx4 v28, s[96:99], s52 offen lds\n"
  "buffer_load_dwordx4 v29, s[96:99], s52 offen offset:1024 lds\n"
  "s_add_u32 m0, s68, 24576\n"
  "s_add_u32 s52, s80, 3670016\n"
  "buffer_load_dwordx4 v28, s[96:99], s52 offen lds\n"
  "buffer_load_dwordx4 v29, s[96:99], s52 offen offset:1024 lds\n"
  "s_add_u32 m0, s69, 24576\n"
  "s_add_u32 s52, s80, 5505024\n"
  "buffer_load_dwordx4 v28, s[96:99], s52 offen lds\n"
  "buffer_load_dwordx4 v29, s[96:99], s52 offen offset:1024 lds\n"
  "s_add_u32 m0, s66, 26624\n"
  "s_add_u32 s52, s80, 65536\n"
  "buffer_load_dwordx4 v28, s[96:99], s52 offen lds\n"
  "buffer_load_dwordx4 v29, s[96:99], s52 offen offset:1024 lds\n"
  "s_add_u32 m0, s67, 26624\n"
  "s_add_u32 s52, s80, 1900544\n"
  "buffer_load_dwordx4 v28, s[96:99], s52 offen lds\n"
  "buffer_load_dwordx4 v29, s[96:99], s52 offen offset:1024 lds\n"
  "s_add_u32 m0, s68, 26624\n"
  "s_add_u32 s52, s80, 3735552\n"
  "buffer_load_dwordx4 v28, s[96:99], s52 offen lds\n"
  "buffer_load_dwordx4 v29, s[96:99], s52 offen offset:1024 lds\n"
  "s_add_u32 m0, s69, 26624\n"
  "s_add_u32 s52, s80, 5570560\n"
  "buffer_load_dwordx4 v28, s[96:99], s52 offen lds\n"
  "buffer_load_dwordx4 v29, s[96:99], s52 offen offset:1024 lds\n"
  "s_add_u32 m0, s66, 28672\n"
  "s_add_u32 s52, s80, 131072\n"
  "buffer_load_dwordx4 v28, s[96:99], s52 offen lds\n"
  "buffer_load_dwordx4 v29, s[96:99], s52 offen offset:1024 lds\n"
  "s_add_u32 m0, s67, 28672\n"
  "s_add_u32 s52, s80, 1966080\n"
  "buffer_load_dwordx4 v28, s[96:99], s52 offen lds\n"
  "buffer_load_dwordx4 v29, s[96:99], s52 offen offset:1024 lds\n"
  "s_add_u32 m0, s68, 28672\n"
  "s_add_u32 s52, s80, 3801088\n"
  "buffer_load_dwordx4 v28, s[96:99], s52 offen lds\n"
  "buffer_load_dwordx4 v29, s[96:99], s52 offen offset:1024 lds\n"
  "s_add_u32 m0, s69, 28672\n"
  "s_add_u32 s52, s80, 5636096\n"
  "buffer_load_dwordx4 v28, s[96:99], s52 offen lds\n"
  "buffer_load_dwordx4 v29, s[96:99], s52 offen offset:1024 lds\n"
  "s_add_u32 m0, s66, 30720\n"
  "s_add_u32 s52, s80, 196608\n"
  "buffer_load_dwordx4 v28, s[96:99], s52 offen lds\n"
  "buffer_load_dwordx4 v29, s[96:99], s52 offen offset:1024 lds\n"
  "s_add_u32 m0, s67, 30720\n"
  "s_add_u32 s52, s80, 2031616\n"
  "buffer_load_dwordx4 v28, s[96:99], s52 offen lds\n"
  "buffer_load_dwordx4 v29, s[96:99], s52 offen offset:1024 lds\n"
  "s_add_u32 m0, s68, 30720\n"
  "s_add_u32 s52, s80, 3866624\n"
  "buffer_load_dwordx4 v28, s[96:99], s52 offen lds\n"
  "buffer_load_dwordx4 v29, s[96:99], s52 offen offset:1024 lds\n"
  "s_add_u32 m0, s69, 30720\n"
  "s_add_u32 s52, s80, 5701632\n"
  "buffer_load_dwordx4 v28, s[96:99], s52 offen lds\n"
  "buffer_load_dwordx4 v29, s[96:99], s52 offen offset:1024 lds\n"
  "s_add_u32 m0, s66, 32768\n"
  "s_add_u32 s52, s81, 0\n"
  "buffer_load_dwordx4 v28, s[96:99], s52 offen lds\n"
  "buffer_load_dwordx4 v29, s[96:99], s52 offen offset:1024 lds\n"
  "s_add_u32 m0, s67, 32768\n"
  "s_add_u32 s52, s81, 1835008\n"
  "buffer_load_dwordx4 v28, s[96:99], s52 offen lds\n"
  "buffer_load_dwordx4 v29, s[96:99], s52 offen offset:1024 lds\n"
  "s_add_u32 m0, s68, 32768\n"
  "s_add_u32 s52, s81, 3670016\n"
  "buffer_load_dwordx4 v28, s[96:99], s52 offen lds\n"
  "buffer_load_dwordx4 v29, s[96:99], s52 offen offset:1024 lds\n"
  "s_add_u32 m0, s69, 32768\n"
  "s_add_u32 s52, s81, 5505024\n"
  "buffer_load_dwordx4 v28, s[96:99], s52 offen lds\n"
  "buffer_load_dwordx4 v29, s[96:99], s52 offen offset:1024 lds\n"
  "s_add_u32 m0, s66, 34816\n"
  "s_add_u32 s52, s81, 65536\n"
  "buffer_load_dwordx4 v28, s[96:99], s52 offen lds\n"
  "buffer_load_dwordx4 v29, s[96:99], s52 offen offset:1024 lds\n"
  "s_add_u32 m0, s67, 34816\n"
  "s_add_u32 s52, s81, 1900544\n"
  "buffer_load_dwordx4 v28, s[96:99], s52 offen lds\n"
  "buffer_load_dwordx4 v29, s[96:99], s52 offen offset:1024 lds\n"
  "s_add_u32 m0, s68, 34816\n"
  "s_add_u32 s52, s81, 3735552\n"
  "buffer_load_dwordx4 v28, s[96:99], s52 offen lds\n"
  "buffer_load_dwordx4 v29, s[96:99], s52 offen offset:1024 lds\n"
  "s_add_u32 m0, s69, 34816\n"
  "s_add_u32 s52, s81, 5570560\n"
  "buffer_load_dwordx4 v28, s[96:99], s52 offen lds\n"
  "buffer_load_dwordx4 v29, s[96:99], s52 offen offset:1024 lds\n"
  "s_add_u32 m0, s66, 36864\n"
  "s_add_u32 s52, s81, 131072\n"
  "buffer_load_dwordx4 v28, s[96:99], s52 offen lds\n"
  "buffer_load_dwordx4 v29, s[96:99], s52 offen offset:1024 lds\n"
  "s_add_u32 m0, s67, 36864\n"
  "s_add_u32 s52, s81, 1966080\n"
  "buffer_load_dwordx4 v28, s[96:99], s52 offen lds\n"
  "buffer_load_dwordx4 v29, s[96:99], s52 offen offset:1024 lds\n"
  "s_add_u32 m0, s68, 36864\n"
  "s_add_u32 s52, s81, 3801088\n"
  "buffer_load_dwordx4 v28, s[96:99], s52 offen lds\n"
  "buffer_load_dwordx4 v29, s[96:99], s52 offen offset:1024 lds\n"
  "s_add_u32 m0, s69, 36864\n"
  "s_add_u32 s52, s81, 5636096\n"
  "buffer_load_dwordx4 v28, s[96:99], s52 offen lds\n"
  "buffer_load_dwordx4 v29, s[96:99], s52 offen offset:1024 lds\n"
  "s_add_u32 m0, s66, 38912\n"
  "s_add_u32 s52, s81, 196608\n"
  "buffer_load_dwordx4 v28, s[96:99], s52 offen lds\n"
  "buffer_load_dwordx4 v29, s[96:99], s52 offen offset:1024 lds\n"
  "s_add_u32 m0, s67, 38912\n"
  "s_add_u32 s52, s81, 2031616\n"
  "buffer_load_dwordx4 v28, s[96:99], s52 offen lds\n"
  "buffer_load_dwordx4 v29, s[96:99], s52 offen offset:1024 lds\n"
  "s_add_u32 m0, s68, 38912\n"
  "s_add_u32 s52, s81, 3866624\n"
  "buffer_load_dwordx4 v28, s[96:99], s52 offen lds\n"
  "buffer_load_dwordx4 v29, s[96:99], s52 offen offset:1024 lds\n"
  "s_add_u32 m0, s69, 38912\n"
  "s_add_u32 s52, s81, 5701632\n"
  "buffer_load_dwordx4 v28, s[96:99], s52 offen lds\n"
  "buffer_load_dwordx4 v29, s[96:99], s52 offen offset:1024 lds\n"
  "s_waitcnt vmcnt(63)\n"
  "s_barrier\n"
  "s_waitcnt vmcnt(0) lgkmcnt(0)\n"
  "s_barrier\n"
  "s_endpgm\n"
  ".Lb_hspare:\n"
  "s_cmp_lt_u32 s50, 28\n"
  "s_cbranch_scc1 .Lb_hend\n"
  "s_barrier\n"
  "s_sub_u32 s53, s50, 28\n"
  "s_mul_i32 s53, s53, 1835008\n"
  "s_lshr_b32 s54, s49, 1\n"
  "s_lshl_b32 s54, s54, 10\n"
  "s_add_u32 s53, s53, s54\n"
  "s_mov_b32 s55, 8\n"
  "s_mov_b32 s54, 32768\n"
  "s_bitcmp1_b32 s49, 0\n"
  "s_cbranch_scc1 .Lb_h8\n"
  "v_lshrrev_b32 v13, 2, v1\n"
  "v_and_b32 v12, 3, v1\n"
  "v_lshlrev_b32 v13, 12, v13\n"
  "v_lshl_add_u32 v21, v12, 7, v13\n"
  "s_mov_b32 s55, 4\n"
  "s_mov_b32 s54, 65536\n"
  ".Lb_h8:\n"
  "s_and_b32 s52, s49, 1\n"
  "s_xor_b32 s52, s52, 0\n"
  "v_lshl_add_u32 v8, s52, 18, v21\n"
  "s_mov_b32 s57, s55\n"
  "s_mov_b32 s52, s53\n"
  ".Lb_h8_0:\n"
  "buffer_load_dword v22, v8, s[96:99], s52 offen\n"
  "s_add_u32 s52, s52, s54\n"
  "s_waitcnt vmcnt(30)\n"
  "s_sub_u32 s57, s57, 1\n"
  "s_cmp_lg_u32 s57, 0\n"
  "s_cbranch_scc1 .Lb_h8_0\n"
  "s_and_b32 s52, s49, 1\n"
  "s_xor_b32 s52, s52, 1\n"
  "v_lshl_add_u32 v8, s52, 18, v21\n"
  "s_mov_b32 s57, s55\n"
  "s_mov_b32 s52, s53\n"
  ".Lb_h8_1:\n"
  "buffer_load_dword v22, v8, s[96:99], s52 offen\n"
  "s_add_u32 s52, s52, s54\n"
  "s_waitcnt vmcnt(30)\n"
  "s_sub_u32 s57, s57, 1\n"
  "s_cmp_lg_u32 s57, 0\n"
  "s_cbranch_scc1 .Lb_h8_1\n"
  "s_and_b32 s52, s49, 1\n"
  "s_xor_b32 s52, s52, 2\n"
  "v_lshl_add_u32 v8, s52, 18, v21\n"
  "s_mov_b32 s57, s55\n"
  "s_mov_b32 s52, s53\n"
  ".Lb_h8_2:\n"
  "buffer_load_dword v22, v8, s[96:99], s52 offen\n"
  "s_add_u32 s52, s52, s54\n"
  "s_waitcnt vmcnt(30)\n"
  "s_sub_u32 s57, s57, 1\n"
  "s_cmp_lg_u32 s57, 0\n"
  "s_cbranch_scc1 .Lb_h8_2\n"
  "s_and_b32 s52, s49, 1\n"
  "s_xor_b32 s52, s52, 3\n"
  "v_lshl_add_u32 v8, s52, 18, v21\n"
  "s_mov_b32 s57, s55\n"
  "s_mov_b32 s52, s53\n"
  ".Lb_h8_3:\n"
  "buffer_load_dword v22, v8, s[96:99], s52 offen\n"
  "s_add_u32 s52, s52, s54\n"
  "s_waitcnt vmcnt(30)\n"
  "s_sub_u32 s57, s57, 1\n"
  "s_cmp_lg_u32 s57, 0\n"
  "s_cbranch_scc1 .Lb_h8_3\n"
  "s_and_b32 s52, s49, 1\n"
  "s_xor_b32 s52, s52, 4\n"
  "v_lshl_add_u32 v8, s52, 18, v21\n"
  "s_mov_b32 s57, s55\n"
  "s_mov_b32 s52, s53\n"
  ".Lb_h8_4:\n"
  "buffer_load_dword v22, v8, s[96:99], s52 offen\n"
  "s_add_u32 s52, s52, s54\n"
  "s_waitcnt vmcnt(30)\n"
  "s_sub_u32 s57, s57, 1\n"
  "s_cmp_lg_u32 s57, 0\n"
  "s_cbranch_scc1 .Lb_h8_4\n"
  "s_and_b32 s52, s49, 1\n"
  "s_xor_b32 s52, s52, 5\n"
  "v_lshl_add_u32 v8, s52, 18, v21\n"
  "s_mov_b32 s57, s55\n"
  "s_mov_b32 s52, s53\n"
  ".Lb_h8_5:\n"
  "buffer_load_dword v22, v8, s[96:99], s52 offen\n"
  "s_add_u32 s52, s52, s54\n"
  "s_waitcnt vmcnt(30)\n"
  "s_sub_u32 s57, s57, 1\n"
  "s_cmp_lg_u32 s57, 0\n"
  "s_cbranch_scc1 .Lb_h8_5\n"
  "v_add_u32 v8, 1572864, v21\n"
  "s_mov_b32 s57, s55\n"
  "s_mov_b32 s52, s53\n"
  ".Lb_h8_6:\n"
  "buffer_load_dword v22, v8, s[96:99], s52 offen\n"
  "s_add_u32 s52, s52, s54\n"
  "s_waitcnt vmcnt(30)\n"
  "s_sub_u32 s57, s57, 1\n"
  "s_cmp_lg_u32 s57, 0\n"
  "s_cbranch_scc1 .Lb_h8_6\n"
  ".Lb_hend:\n"
  "s_endpgm\n"
  ".Lh_ring:\n"
  "s_waitcnt vmcnt(0)\n"
  ".Lr_ring:\n"
  "s_mov_b32 s55, 0\n"
  "v_max3_u32 v13, v128, v130, v132\n"
  "v_max_u32 v13, v13, v134\n"
  "v_cmp_eq_u32 vcc, -1, v13\n"
  "s_cbranch_vccz .Ln_ring_0\n"
  "buffer_load_dwordx4 v[128:131], v11, s[36:39], s64 offen sc1\n"
  "buffer_load_dwordx4 v[132:135], v12, s[36:39], s64 offen sc1\n"
  "s_mov_b32 s55, 1\n"
  ".Ln_ring_0:\n"
  "v_max3_u32 v13, v136, v138, v140\n"
  "v_max_u32 v13, v13, v142\n"
  "v_cmp_eq_u32 vcc, -1, v13\n"
  "s_cbranch_vccz .Ln_ring_1\n"
  "buffer_load_dwordx4 v[136:139], v11, s[36:39], s65 offen sc1\n"
  "buffer_load_dwordx4 v[140:143], v12, s[36:39], s65 offen sc1\n"
  "s_mov_b32 s55, 1\n"
  ".Ln_ring_1:\n"
  "v_max3_u32 v13, v144, v146, v148\n"
  "v_max_u32 v13, v13, v150\n"
  "v_cmp_eq_u32 vcc, -1, v13\n"
  "s_cbranch_vccz .Ln_ring_2\n"
  "buffer_load_dwordx4 v[144:147], v11, s[36:39], s66 offen sc1\n"
  "buffer_load_dwordx4 v[148:151], v12, s[36:39], s66 offen sc1\n"
  "s_mov_b32 s55, 1\n"
  ".Ln_ring_2:\n"
  "v_max3_u32 v13, v152, v154, v156\n"
  "v_max_u32 v13, v13, v158\n"
  "v_cmp_eq_u32 vcc, -1, v13\n"
  "s_cbranch_vccz .Ln_ring_3\n"
  "buffer_load_dwordx4 v[152:155], v11, s[36:39], s67 offen sc1\n"
  "buffer_load_dwordx4 v[156:159], v12, s[36:39], s67 offen sc1\n"
  "s_mov_b32 s55, 1\n"
  ".Ln_ring_3:\n"
  "v_max3_u32 v13, v160, v162, v164\n"
  "v_max_u32 v13, v13, v166\n"
  "v_cmp_eq_u32 vcc, -1, v13\n"
  "s_cbranch_vccz .Ln_ring_4\n"
  "buffer_load_dwordx4 v[160:163], v11, s[36:39], s68 offen sc1\n"
  "buffer_load_dwordx4 v[164:167], v12, s[36:39], s68 offen sc1\n"
  "s_mov_b32 s55, 1\n"
  ".Ln_ring_4:\n"
  "v_max3_u32 v13, v168, v170, v172\n"
  "v_max_u32 v13, v13, v174\n"
  "v_cmp_eq_u32 vcc, -1, v13\n"
  "s_cbranch_vccz .Ln_ring_5\n"
  "buffer_load_dwordx4 v[168:171], v11, s[36:39], s69 offen sc1\n"
  "buffer_load_dwordx4 v[172:175], v12, s[36:39], s69 offen sc1\n"
  "s_mov_b32 s55, 1\n"
  ".Ln_ring_5:\n"
  "v_max3_u32 v13, v176, v178, v180\n"
  "v_max_u32 v13, v13, v182\n"
  "v_cmp_eq_u32 vcc, -1, v13\n"
  "s_cbranch_vccz .Ln_ring_6\n"
  "buffer_load_dwordx4 v[176:179], v11, s[36:39], s70 offen sc1\n"
  "buffer_load_dwordx4 v[180:183], v12, s[36:39], s70 offen sc1\n"
  "s_mov_b32 s55, 1\n"
  ".Ln_ring_6:\n"
  "v_max3_u32 v13, v184, v186, v188\n"
  "v_max_u32 v13, v13, v190\n"
  "v_cmp_eq_u32 vcc, -1, v13\n"
  "s_cbranch_vccz .Ln_ring_7\n"
  "buffer_load_dwordx4 v[184:187], v11, s[36:39], s71 offen sc1\n"
  "buffer_load_dwordx4 v[188:191], v12, s[36:39], s71 offen sc1\n"
  "s_mov_b32 s55, 1\n"
  ".Ln_ring_7:\n"
  "s_waitcnt vmcnt(0)\n"
  "s_add_u32 s63, s63, s55\n"
  "s_cmp_lt_u32 s63, 65536\n"
  "s_cselect_b32 s55, s55, 0\n"
  "s_cmp_lg_u32 s55, 0\n"
  "s_cbranch_scc1 .Lr_ring\n"
  "s_setpc_b64 s[82:83]\n"
  ".Ls_p3_0:\n"
  "s_swappc_b64 s[82:83], s[80:81]\n"
  "s_branch .Lo_p3_0\n"
  ".Ls_p3_1:\n"
  "s_swappc_b64 s[82:83], s[80:81]\n"
  "s_branch .Lo_p3_1\n"
  ".Ls_p3_2:\n"
  "s_swappc_b64 s[82:83], s[80:81]\n"
  "s_branch .Lo_p3_2\n"
  ".Ls_p3_3:\n"
  "s_swappc_b64 s[82:83], s[80:81]\n"
  "s_branch .Lo_p3_3\n"
  ".Ls_p3_4:\n"
  "s_swappc_b64 s[82:83], s[80:81]\n"
  "s_branch .Lo_p3_4\n"
  ".Ls_p3_5:\n"
  "s_swappc_b64 s[82:83], s[80:81]\n"
  "s_branch .Lo_p3_5\n"
  ".Ls_p3_6:\n"
  "s_swappc_b64 s[82:83], s[80:81]\n"
  "s_branch .Lo_p3_6\n"
  ".Ls_p3_7:\n"
  "s_swappc_b64 s[82:83], s[80:81]\n"
  "s_branch .Lo_p3_7\n"
  ".Lb_stat_retry:\n"
  "buffer_load_dwordx2 v[136:137], v35, s[36:39], s22 offen offset:0 sc1\n"
  "buffer_load_dwordx2 v[138:139], v35, s[36:39], s22 offen offset:256 sc1\n"
  "buffer_load_dwordx2 v[140:141], v35, s[36:39], s22 offen offset:512 sc1\n"
  "buffer_load_dwordx2 v[142:143], v35, s[36:39], s22 offen offset:768 sc1\n"
  "buffer_load_dwordx2 v[144:145], v35, s[36:39], s22 offen offset:1024 sc1\n"
  "buffer_load_dwordx2 v[146:147], v35, s[36:39], s22 offen offset:1280 sc1\n"
  "buffer_load_dwordx2 v[148:149], v35, s[36:39], s22 offen offset:1536 sc1\n"
  "s_waitcnt vmcnt(0)\n"
  "v_max3_u32 v13, v136, v137, v138\n"
  "v_max3_u32 v14, v139, v140, v141\n"
  "v_max3_u32 v13, v13, v142, v143\n"
  "v_max3_u32 v14, v14, v144, v145\n"
  "v_max3_u32 v13, v13, v146, v147\n"
  "v_max3_u32 v14, v14, v148, v149\n"
  "v_max_u32 v13, v13, v14\n"
  "v_cmp_eq_u32 vcc, -1, v13\n"
  "s_add_u32 s63, s63, 1\n"
  "s_cbranch_vccz .Lb_stat_ok\n"
  "s_cmp_lt_u32 s63, 65536\n"
  "s_cbranch_scc1 .Lb_stat_retry\n"
  "s_branch .Lb_stat_ok\n"
  ".Ls_p4_0:\n"
  "s_swappc_b64 s[82:83], s[80:81]\n"
  "s_branch .Lo_p4_0\n"
  ".Ls_p4_1:\n"
  "s_swappc_b64 s[82:83], s[80:81]\n"
  "s_branch .Lo_p4_1\n"
  ".Ls_p4_2:\n"
  "s_swappc_b64 s[82:83], s[80:81]\n"
  "s_branch .Lo_p4_2\n"
  ".Ls_p4_3:\n"
  "s_swappc_b64 s[82:83], s[80:81]\n"
  "s_branch .Lo_p4_3\n"
  ".Ls_p4_4:\n"
  "s_swappc_b64 s[82:83], s[80:81]\n"
  "s_branch .Lo_p4_4\n"
  ".Ls_p4_5:\n"
  "s_swappc_b64 s[82:83], s[80:81]\n"
  "s_branch .Lo_p4_5\n"
  ".Ls_p4_6:\n"
  "s_swappc_b64 s[82:83], s[80:81]\n"
  "s_branch .Lo_p4_6\n"
  ".Ls_p4_7:\n"
  "s_swappc_b64 s[82:83], s[80:81]\n"
  "s_branch .Lo_p4_7\n"
  ".Ls_p4_8:\n"
  "s_swappc_b64 s[82:83], s[80:81]\n"
  "s_branch .Lo_p4_8\n"
  ".Ls_p4_9:\n"
  "s_swappc_b64 s[82:83], s[80:81]\n"
  "s_branch .Lo_p4_9\n"
  ".Ls_p4_10:\n"
  "s_swappc_b64 s[82:83], s[80:81]\n"
  "s_branch .Lo_p4_10\n"
  ".Ls_p4_11:\n"
  "s_swappc_b64 s[82:83], s[80:81]\n"
  "s_branch .Lo_p4_11\n"
  ".Ls_p4_12:\n"
  "s_swappc_b64 s[82:83], s[80:81]\n"
  "s_branch .Lo_p4_12\n"
  ".Ls_p4_13:\n"
  "s_swappc_b64 s[82:83], s[80:81]\n"
  "s_branch .Lo_p4_13\n"
  ".Ls_p4_14:\n"
  "s_swappc_b64 s[82:83], s[80:81]\n"
  "s_branch .Lo_p4_14\n"
  ".Ls_p4_15:\n"
  "s_swappc_b64 s[82:83], s[80:81]\n"
  "s_branch .Lo_p4_15\n"
  ".Ls_p4_16:\n"
  "s_swappc_b64 s[82:83], s[80:81]\n"
  "s_branch .Lo_p4_16\n"
  ".Ls_p4_17:\n"
  "s_swappc_b64 s[82:83], s[80:81]\n"
  "s_branch .Lo_p4_17\n"
  ".Ls_p4_18:\n"
  "s_swappc_b64 s[82:83], s[80:81]\n"
  "s_branch .Lo_p4_18\n"
  ".Ls_p4_19:\n"
  "s_swappc_b64 s[82:83], s[80:81]\n"
  "s_branch .Lo_p4_19\n"
  ".Ls_p4_20:\n"
  "s_swappc_b64 s[82:83], s[80:81]\n"
  "s_branch .Lo_p4_20\n"
  ".Ls_p4_21:\n"
  "s_swappc_b64 s[82:83], s[80:81]\n"
  "s_branch .Lo_p4_21\n"
  ".Ls_p4_22:\n"
  "s_swappc_b64 s[82:83], s[80:81]\n"
  "s_branch .Lo_p4_22\n"
  ".Ls_p4_23:\n"
  "s_swappc_b64 s[82:83], s[80:81]\n"
  "s_branch .Lo_p4_23\n"
  ".Ls_p4_24:\n"
  "s_swappc_b64 s[82:83], s[80:81]\n"
  "s_branch .Lo_p4_24\n"
  ".Ls_p4_25:\n"
  "s_swappc_b64 s[82:83], s[80:81]\n"
  "s_branch .Lo_p4_25\n"
  ".Ls_p4_26:\n"
  "s_swappc_b64 s[82:83], s[80:81]\n"
  "s_branch .Lo_p4_26\n"
  ".Ls_p4_27:\n"
  "s_swappc_b64 s[82:83], s[80:81]\n"
  "s_branch .Lo_p4_27\n"
  ".Ls_p4_28:\n"
  "s_swappc_b64 s[82:83], s[80:81]\n"
  "s_branch .Lo_p4_28\n"
  ".Ls_p4_29:\n"
  "s_swappc_b64 s[82:83], s[80:81]\n"
  "s_branch .Lo_p4_29\n"
  ".Ls_p4_30:\n"
  "s_swappc_b64 s[82:83], s[80:81]\n"
  "s_branch .Lo_p4_30\n"
  ".Ls_p4_31:\n"
  "s_swappc_b64 s[82:83], s[80:81]\n"
  "s_branch .Lo_p4_31\n"
  ".Ls_p5_0:\n"
  "s_swappc_b64 s[82:83], s[80:81]\n"
  "s_branch .Lo_p5_0\n"
  ".Ls_p5_1:\n"
  "s_swappc_b64 s[82:83], s[80:81]\n"
  "s_branch .Lo_p5_1\n"
  ".Ls_p5_2:\n"
  "s_swappc_b64 s[82:83], s[80:81]\n"
  "s_branch .Lo_p5_2\n"
  ".Ls_p5_3:\n"
  "s_swappc_b64 s[82:83], s[80:81]\n"
  "s_branch .Lo_p5_3\n"
  ".Ls_p5_4:\n"
  "s_swappc_b64 s[82:83], s[80:81]\n"
  "s_branch .Lo_p5_4\n"
  ".Ls_p5_5:\n"
  "s_swappc_b64 s[82:83], s[80:81]\n"
  "s_branch .Lo_p5_5\n"
  ".Ls_p5_6:\n"
  "s_swappc_b64 s[82:83], s[80:81]\n"
  "s_branch .Lo_p5_6\n"
  ".Ls_p5_7:\n"
  "s_swappc_b64 s[82:83], s[80:81]\n"
  "s_branch .Lo_p5_7\n"
  ".Ls_p5_8:\n"
  "s_swappc_b64 s[82:83], s[80:81]\n"
  "s_branch .Lo_p5_8\n"
  ".Ls_p5_9:\n"
  "s_swappc_b64 s[82:83], s[80:81]\n"
  "s_branch .Lo_p5_9\n"
  ".Ls_p5_10:\n"
  "s_swappc_b64 s[82:83], s[80:81]\n"
  "s_branch .Lo_p5_10\n"
  ".Ls_p5_11:\n"
  "s_swappc_b64 s[82:83], s[80:81]\n"
  "s_branch .Lo_p5_11\n"
  ".Ls_p5_12:\n"
  "s_swappc_b64 s[82:83], s[80:81]\n"
  "s_branch .Lo_p5_12\n"
  ".Ls_p5_13:\n"
  "s_swappc_b64 s[82:83], s[80:81]\n"
  "s_branch .Lo_p5_13\n"
  ".Ls_p5_14:\n"
  "s_swappc_b64 s[82:83], s[80:81]\n"
  "s_branch .Lo_p5_14\n"
  ".Ls_p5_15:\n"
  "s_swappc_b64 s[82:83], s[80:81]\n"
  "s_branch .Lo_p5_15\n"
  ".Ls_p5_16:\n"
  "s_swappc_b64 s[82:83], s[80:81]\n"
  "s_branch .Lo_p5_16\n"
  ".Ls_p5_17:\n"
  "s_swappc_b64 s[82:83], s[80:81]\n"
  "s_branch .Lo_p5_17\n"
  ".Ls_p5_18:\n"
  "s_swappc_b64 s[82:83], s[80:81]\n"
  "s_branch .Lo_p5_18\n"
  ".Ls_p5_19:\n"
  "s_swappc_b64 s[82:83], s[80:81]\n"
  "s_branch .Lo_p5_19\n"
  ".Ls_p5_20:\n"
  "s_swappc_b64 s[82:83], s[80:81]\n"
  "s_branch .Lo_p5_20\n"
  ".Ls_p5_21:\n"
  "s_swappc_b64 s[82:83], s[80:81]\n"
  "s_branch .Lo_p5_21\n"
  ".Ls_p5_22:\n"
  "s_swappc_b64 s[82:83], s[80:81]\n"
  "s_branch .Lo_p5_22\n"
  ".Ls_p5_23:\n"
  "s_swappc_b64 s[82:83], s[80:81]\n"
  "s_branch .Lo_p5_23\n"
  ".Ls_p5_24:\n"
  "s_swappc_b64 s[82:83], s[80:81]\n"
  "s_branch .Lo_p5_24\n"
  ".Ls_p5_25:\n"
  "s_swappc_b64 s[82:83], s[80:81]\n"
  "s_branch .Lo_p5_25\n"
  ".Ls_p5_26:\n"
  "s_swappc_b64 s[82:83], s[80:81]\n"
  "s_branch .Lo_p5_26\n"
  ".Ls_p5_27:\n"
  "s_swappc_b64 s[82:83], s[80:81]\n"
  "s_branch .Lo_p5_27\n"
  ".Lb_codeend:\n"
    :: [ka] "s"(__builtin_amdgcn_kernarg_segment_ptr()), [bid] "s"(blockIdx.x), [tid] "v"(threadIdx.x)
    : "v1","v2","v3","v4","v5","v6","v7","v8","v9","v10","v11","v12","v13","v14","v15","v16","v17","v18","v19","v20","v21","v22","v23","v24","v25","v26","v27","v28","v29","v30","v31","v32","v33","v34","v35","v36","v37","v38","v39","v40","v41","v42","v43","v44","v45","v46","v47","v48","v49","v50","v51","v52","v53","v54","v55","v56","v57","v58","v59","v60","v61","v62","v63","v64","v65","v66","v67","v68","v69","v70","v71","v72","v73","v74","v75","v76","v77","v78","v79","v80","v81","v82","v83","v84","v85","v86","v87","v88","v89","v90","v91","v92","v93","v94","v95","v96","v97","v98","v99","v100","v101","v102","v103","v104","v105","v106","v107","v108","v109","v110","v111","v112","v113","v114","v115","v116","v117","v118","v119","v120","v121","v122","v123","v124","v125","v126","v127","v128","v129","v130","v131","v132","v133","v134","v135","v136","v137","v138","v139","v140","v141","v142","v143","v144","v145","v146","v147","v148","v149","v150","v151","v152","v153","v154","v155","v156","v157","v158","v159","v160","v161","v162","v163","v164","v165","v166","v167","v168","v169","v170","v171","v172","v173","v174","v175","v176","v177","v178","v179","v180","v181","v182","v183","v184","v185","v186","v187","v188","v189","v190","v191","v192","v193","v194","v195","v196","v197","v198","v199","v200","v201","v202","v203","v204","v205","v206","v207","v208","v209","v210","v211","v212","v213","v214","v215","v216","v217","v218","v219","v220","v221","v222","v223","v224","v225","v226","v227","v228","v229","v230","v231","v232","v233","v234","v235","v236","v237","v238","v239","v240","v241","v242","v243","v244","v245","v246","v247","v248","v249","v250","v251","v252","v253","v254","v255", "s4","s5","s6","s7","s8","s9","s10","s11","s12","s13","s14","s15","s16","s17","s18","s19","s20","s21","s22","s23","s24","s25","s26","s27","s28","s29","s30","s31","s34","s35","s36","s37","s38","s39","s40","s41","s42","s43","s44","s45","s46","s47","s48","s49","s50","s51","s52","s53","s54","s55","s56","s57","s58","s59","s60","s61","s62","s63","s64","s65","s66","s67","s68","s69","s70","s71","s72","s73","s74","s75","s76","s77","s78","s79","s80","s81","s82","s83","s84","s85","s86","s87","s88","s89","s90","s91","s92","s93","s94","s95","s96","s97","s98","s99", "vcc", "scc", "memory");
}

extern "C" void kernel_launch(void* const* d_in, const int* in_sizes, int n_in, void* d_out, int out_size, void* d_ws, size_t ws_size, hipStream_t stream) {
  (void)hipFuncSetAttribute((const void*)k_a, hipFuncAttributeMaxDynamicSharedMemorySize, LDS_A);
  (void)hipFuncSetAttribute((const void*)k_b, hipFuncAttributeMaxDynamicSharedMemorySize, LDS_B);
  k_a<<<256, 320, LDS_A, stream>>>((const char*)d_in[0], (const char*)d_in[1], (const char*)d_in[2], (const char*)d_in[4], (const char*)d_in[6], (char*)d_ws);
  k_b<<<256, 320, LDS_B, stream>>>((const char*)d_in[0], (const char*)d_in[2], (const char*)d_in[4], (const char*)d_in[5], (const char*)d_in[6], (const char*)d_in[7], (char*)d_ws, (char*)d_out);
}
```
